```python
import torch

def _softmax(x, axis=-1):
    return torch.softmax(x, dim=axis)
def _gelu(x):
    return torch.nn.functional.gelu(x, approximate="tanh")
def _silu(x):
    return torch.nn.functional.silu(x)
def _sigmoid(x):
    return torch.sigmoid(x)
def _layernorm(x, axis=-1, eps=1e-5):
    m = x.mean(dim=axis, keepdim=True)
    v = x.var(dim=axis, keepdim=True, unbiased=False)
    return (x - m) / torch.sqrt(v + eps)
def _scatter_add(tgt, idx, src, axis=0):
    return tgt.clone().index_add_(axis, idx, src)
def _segment_sum(src, seg, n):
    out = torch.zeros((n,) + src.shape[1:], dtype=src.dtype)
    return out.index_add_(0, seg, src)
def _pad(x, pw):
    flat = tuple(v for pair in reversed(pw) for v in pair)
    return torch.nn.functional.pad(x, flat)

kernel_name = 'parallel_sum__ssm_conv_s5__glu_gated_glu__prompt'

def setup_inputs():

    _w = (lambda a: (lambda t: t.to(torch.bfloat16) if t.is_floating_point() else t)(torch.as_tensor(a)))
    def _A_setup():
        B, S, D = 1, 2048, 1024
        P = D
        Lambda_re = -0.5 * (lambda _x: torch.exp(torch.as_tensor(_x)))((lambda *s: torch.randn(*s, dtype=torch.bfloat16) * 0.8)(P))
        Lambda_im = (lambda *s: torch.randn(*s, dtype=torch.bfloat16) * 0.8)(P) * 1.0
        B_re = (lambda *s: torch.randn(*s, dtype=torch.bfloat16) * 0.8)(P, D) * (D ** -0.5)
        B_im = (lambda *s: torch.randn(*s, dtype=torch.bfloat16) * 0.8)(P, D) * (D ** -0.5)
        C_re = (lambda *s: torch.randn(*s, dtype=torch.bfloat16) * 0.8)(D, P) * (P ** -0.5)
        C_im = (lambda *s: torch.randn(*s, dtype=torch.bfloat16) * 0.8)(D, P) * (P ** -0.5)
        log_dt = (lambda *s: torch.randn(*s, dtype=torch.bfloat16) * 0.8)(P) * 0.3 - 2.0
        Dskip = (lambda *s: torch.randn(*s, dtype=torch.bfloat16) * 0.8)(D)
        return {
            "x": _w((lambda *s: torch.randn(*s, dtype=torch.bfloat16) * 0.05)(B, S, D)),
            "Lambda_re": _w(Lambda_re),
            "Lambda_im": _w(Lambda_im),
            "B_re": _w(B_re),
            "B_im": _w(B_im),
            "C_re": _w(C_re),
            "C_im": _w(C_im),
            "log_dt": _w(log_dt),
            "Dskip": _w(Dskip),
        }
    _A_in = _A_setup()

    def _B_setup():
        B, S, D, F = 1, 2048, 1024, 3584
        return {
            "x": _w((lambda *s: torch.randn(*s, dtype=torch.bfloat16) * 0.05)(B, S, D)),
            "w_gate": _w((lambda *s: torch.randn(*s, dtype=torch.bfloat16) * 0.05)(D, F)),
            "w_up": _w((lambda *s: torch.randn(*s, dtype=torch.bfloat16) * 0.05)(D, F)),
            "w_down": _w((lambda *s: torch.randn(*s, dtype=torch.bfloat16) * 0.05)(F, D)),
        }
    _B_in = _B_setup()

    return {
        "A_x": _A_in["x"],
        "A_Lambda_re": _A_in["Lambda_re"],
        "A_Lambda_im": _A_in["Lambda_im"],
        "A_B_re": _A_in["B_re"],
        "A_B_im": _A_in["B_im"],
        "A_C_re": _A_in["C_re"],
        "A_C_im": _A_in["C_im"],
        "A_log_dt": _A_in["log_dt"],
        "A_Dskip": _A_in["Dskip"],
        "B_x": _B_in["x"],
        "B_w_gate": _B_in["w_gate"],
        "B_w_up": _B_in["w_up"],
        "B_w_down": _B_in["w_down"],
    }

def reference(A_x, A_Lambda_re, A_Lambda_im, A_B_re, A_B_im, A_C_re, A_C_im, A_log_dt, A_Dskip, B_x, B_w_gate, B_w_up, B_w_down):
    def _A_ref(x, Lambda_re, Lambda_im, B_re, B_im, C_re, C_im, log_dt, Dskip):
        Bsz, L, H = x.shape
        Lam = Lambda_re + 1j * Lambda_im
        Bmat = B_re + 1j * B_im
        Cmat = C_re + 1j * C_im
        dt = torch.exp(log_dt)
        Lam_bar = torch.exp(dt * Lam)
        B_bar = ((Lam_bar - 1.0) / Lam)[:, None] * Bmat
        Bu = torch.einsum("bld,pd->blp", x + 0j, B_bar)
        h = torch.zeros_like(Bu[:, 0, :])
        hs = []
        for t in range(L):
            h = Lam_bar[None, :] * h + Bu[:, t, :]
            hs.append(h)
        hseq = torch.stack(hs, 1)
        y = 2.0 * torch.einsum("blp,dp->bld", hseq, Cmat).real + x * Dskip[None, None, :]
        return y

    def _B_ref(x, w_gate, w_up, w_down):
        g = torch.einsum("bsd,df->bsf", x, w_gate)
        u = torch.einsum("bsd,df->bsf", x, w_up)
        h = _sigmoid(g) * u
        return torch.einsum("bsf,fd->bsd", h, w_down)

    x = A_x
    xn = _layernorm(x, axis=-1)
    return x + _A_ref(xn, A_Lambda_re, A_Lambda_im, A_B_re, A_B_im, A_C_re, A_C_im, A_log_dt, A_Dskip) + _B_ref(xn, B_w_gate, B_w_up, B_w_down)
```

```cpp
#include <hip/hip_runtime.h>
#include <hip/hip_bf16.h>
#include <stdint.h>

typedef unsigned short u16;
typedef short bf16x8 __attribute__((ext_vector_type(8)));
typedef float f32x4 __attribute__((ext_vector_type(4)));

constexpr int L = 2048, D = 1024, P = 1024, F = 3584;

__device__ __forceinline__ float bf2f(u16 v) { return __uint_as_float(((unsigned)v) << 16); }
__device__ __forceinline__ u16 f2bf(float f) {
  unsigned u = __float_as_uint(f);
  u += 0x7FFF + ((u >> 16) & 1);
  return (u16)(u >> 16);
}
__device__ __forceinline__ float rbf(float f) { return bf2f(f2bf(f)); }

__global__ __launch_bounds__(256) void ln_kernel(const u16* __restrict__ x, u16* __restrict__ xn) {
  int row = blockIdx.x * 4 + (threadIdx.x >> 6);
  int lane = threadIdx.x & 63;
  const u16* xr = x + (size_t)row * D;
  float v[16];
  float s = 0.f;
  for (int i = 0; i < 16; ++i) { v[i] = bf2f(xr[lane * 16 + i]); s += v[i]; }
  for (int o = 32; o > 0; o >>= 1) s += __shfl_xor(s, o);
  float mean = s * (1.0f / D);
  float q = 0.f;
  for (int i = 0; i < 16; ++i) { float d = v[i] - mean; q += d * d; }
  for (int o = 32; o > 0; o >>= 1) q += __shfl_xor(q, o);
  float var = rbf(q * (1.0f / D));
  float mb = rbf(mean);
  float sd = rbf(sqrtf(rbf(var + 1e-5f)));
  for (int i = 0; i < 16; ++i) xn[(size_t)row * D + lane * 16 + i] = f2bf(rbf(v[i] - mb) / sd);
}

template <bool BT>
__global__ __launch_bounds__(256) void gemm_simple(const u16* __restrict__ A, int lda, const u16* __restrict__ B, int ldb,
                                                   float* __restrict__ C, int ldc, int K, int accumulate) {
  __shared__ u16 sA[64][40];
  __shared__ u16 sB[64][40];
  int bm = blockIdx.y * 64, bn = blockIdx.x * 64;
  int tid = threadIdx.x, wid = tid >> 6, lane = tid & 63;
  int wr = wid >> 1, wc = wid & 1;
  f32x4 acc[2][2] = {};
  for (int k0 = 0; k0 < K; k0 += 32) {
    {
      int r = tid >> 2, c = (tid & 3) * 8;
      *(bf16x8*)&sA[r][c] = *(const bf16x8*)&A[(size_t)(bm + r) * lda + k0 + c];
      if (BT) {
        *(bf16x8*)&sB[r][c] = *(const bf16x8*)&B[(size_t)(bn + r) * ldb + k0 + c];
      } else {
        int kk = tid >> 3, n = (tid & 7) * 8;
        bf16x8 t = *(const bf16x8*)&B[(size_t)(k0 + kk) * ldb + bn + n];
        for (int i = 0; i < 8; ++i) sB[n + i][kk] = (u16)t[i];
      }
    }
    __syncthreads();
    bf16x8 a[2], b[2];
    for (int i = 0; i < 2; ++i) {
      a[i] = *(bf16x8*)&sA[wr * 32 + i * 16 + (lane & 15)][(lane >> 4) * 8];
      b[i] = *(bf16x8*)&sB[wc * 32 + i * 16 + (lane & 15)][(lane >> 4) * 8];
    }
    for (int i = 0; i < 2; ++i)
      for (int j = 0; j < 2; ++j)
        acc[i][j] = __builtin_amdgcn_mfma_f32_16x16x32_bf16(a[i], b[j], acc[i][j], 0, 0, 0);
    __syncthreads();
  }
  for (int i = 0; i < 2; ++i)
    for (int j = 0; j < 2; ++j)
      for (int r = 0; r < 4; ++r) {
        size_t idx = (size_t)(bm + wr * 32 + i * 16 + (lane >> 4) * 4 + r) * ldc + bn + wc * 32 + j * 16 + (lane & 15);
        C[idx] = accumulate ? C[idx] + acc[i][j][r] : acc[i][j][r];
      }
}

__global__ void scan_kernel(const float* __restrict__ bur, const float* __restrict__ bui, const u16* lre, const u16* lim,
                            const u16* logdt, u16* __restrict__ hre, u16* __restrict__ him) {
  int p = blockIdx.x * blockDim.x + threadIdx.x;
  float lr = bf2f(lre[p]), li = bf2f(lim[p]);
  float dt = rbf(expf(bf2f(logdt[p])));
  float er = expf(dt * lr), ar = er * cosf(dt * li), ai = er * sinf(dt * li);
  float nr = ar - 1.f, ni = ai, den = lr * lr + li * li;
  float cr = (nr * lr + ni * li) / den, ci = (ni * lr - nr * li) / den;
  float hr = 0.f, hi = 0.f;
  for (int t = 0; t < L; ++t) {
    float xr = bur[(size_t)t * P + p], xi = bui[(size_t)t * P + p];
    float br = cr * xr - ci * xi, bi = cr * xi + ci * xr;
    float nhr = ar * hr - ai * hi + br, nhi = ar * hi + ai * hr + bi;
    hr = nhr; hi = nhi;
    hre[(size_t)t * P + p] = f2bf(2.f * hr);
    him[(size_t)t * P + p] = f2bf(-2.f * hi);
  }
}

__global__ void glu_kernel(const float* g, const float* u, u16* h, int n) {
  int i = blockIdx.x * blockDim.x + threadIdx.x;
  if (i < n) h[i] = f2bf(u[i] / (1.f + __expf(-g[i])));
}

__global__ void final_kernel(const u16* x, const u16* xn, const u16* dsk, float* out) {
  int i = blockIdx.x * blockDim.x + threadIdx.x;
  out[i] += bf2f(x[i]) + rbf(bf2f(xn[i]) * bf2f(dsk[i & (D - 1)]));
}

extern "C" void kernel_launch(void* const* d_in, const int* in_sizes, int n_in, void* d_out, int out_size, void* d_ws,
                              size_t ws_size, hipStream_t stream) {
  const u16* x = (const u16*)d_in[0];
  const u16* lre = (const u16*)d_in[1];
  const u16* lim = (const u16*)d_in[2];
  const u16* Bre = (const u16*)d_in[3];
  const u16* Bim = (const u16*)d_in[4];
  const u16* Cre = (const u16*)d_in[5];
  const u16* Cim = (const u16*)d_in[6];
  const u16* logdt = (const u16*)d_in[7];
  const u16* dsk = (const u16*)d_in[8];
  const u16* wg = (const u16*)d_in[10];
  const u16* wu = (const u16*)d_in[11];
  const u16* wd = (const u16*)d_in[12];
  float* out = (float*)d_out;
  char* ws = (char*)d_ws;
  u16* xn = (u16*)ws; ws += (size_t)L * D * 2;
  float* bur = (float*)ws; ws += (size_t)L * P * 4;
  float* bui = (float*)ws; ws += (size_t)L * P * 4;
  float* g = (float*)ws; ws += (size_t)L * F * 4;
  float* u = (float*)ws; ws += (size_t)L * F * 4;
  u16* hre = (u16*)ws; ws += (size_t)L * P * 2;
  u16* him = (u16*)ws; ws += (size_t)L * P * 2;
  u16* h = (u16*)ws; ws += (size_t)L * F * 2;
  ln_kernel<<<L / 4, 256, 0, stream>>>(x, xn);
  gemm_simple<true><<<dim3(P / 64, L / 64), 256, 0, stream>>>(xn, D, Bre, D, bur, P, D, 0);
  gemm_simple<true><<<dim3(P / 64, L / 64), 256, 0, stream>>>(xn, D, Bim, D, bui, P, D, 0);
  gemm_simple<false><<<dim3(F / 64, L / 64), 256, 0, stream>>>(xn, D, wg, F, g, F, D, 0);
  gemm_simple<false><<<dim3(F / 64, L / 64), 256, 0, stream>>>(xn, D, wu, F, u, F, D, 0);
  scan_kernel<<<P / 64, 64, 0, stream>>>(bur, bui, lre, lim, logdt, hre, him);
  glu_kernel<<<L * F / 256, 256, 0, stream>>>(g, u, h, L * F);
  gemm_simple<true><<<dim3(D / 64, L / 64), 256, 0, stream>>>(hre, P, Cre, P, out, D, P, 0);
  gemm_simple<true><<<dim3(D / 64, L / 64), 256, 0, stream>>>(him, P, Cim, P, out, D, P, 1);
  gemm_simple<false><<<dim3(D / 64, L / 64), 256, 0, stream>>>(h, F, wd, D, out, D, F, 1);
  final_kernel<<<L * D / 256, 256, 0, stream>>>(x, xn, dsk, out);
}
```

```cpp
#include <hip/hip_runtime.h>
#include <hip/hip_bf16.h>
#define ASM_BLOCK0 \
  "s_mov_b32 s98, 0\n" \
  "s_add_u32 m0, s89, s98\n" \
  "s_nop 0\n" \
  "buffer_load_dwordx4 v231, s[12:15], 0 offen lds\n" \
  "s_add_u32 m0, m0, 1024\n" \
  "s_nop 0\n" \
  "buffer_load_dwordx4 v232, s[12:15], 0 offen lds\n" \
  "s_add_u32 m0, m0, 1024\n" \
  "s_nop 0\n" \
  "buffer_load_dwordx4 v233, s[12:15], 0 offen lds\n" \
  "s_add_u32 m0, m0, 1024\n" \
  "s_nop 0\n" \
  "buffer_load_dwordx4 v234, s[12:15], 0 offen lds\n" \
  "s_add_u32 m0, m0, 1024\n" \
  "s_nop 0\n" \
  "buffer_load_dwordx4 v235, s[12:15], 0 offen lds\n" \
  "s_add_u32 m0, m0, 1024\n" \
  "s_nop 0\n" \
  "buffer_load_dwordx4 v236, s[12:15], 0 offen lds\n" \
  "s_add_u32 m0, m0, 1024\n" \
  "s_nop 0\n" \
  "buffer_load_dwordx4 v237, s[12:15], 0 offen lds\n" \
  "s_add_u32 s12, s12, 458752\n" \
  "s_addc_u32 s13, s13, 0\n" \
  "s_sub_u32 s14, s14, 458752\n" \
  "s_add_u32 s98, s98, 28928\n" \
  "s_cmp_eq_u32 s98, 86784\n" \
  "s_cselect_b32 s98, 0, s98\n" \
  "s_mov_b32 m0, 162560\n" \
  "s_nop 0\n" \
  "buffer_load_dword v229, s[12:15], 0 offen lds\n"

#define ASM_BLOCKX \
  "s_mov_b32 s72, 32768\n" \
  "s_mov_b32 s73, 65536\n" \
  "s_mov_b32 s74, 98304\n" \
  "s_mov_b32 s75, 131072\n" \
  "s_mov_b32 s76, 163840\n" \
  "s_mov_b32 s77, 196608\n" \
  "s_mov_b32 s78, 229376\n" \
  "s_mov_b32 s87, 0x20000\n" \
  "Lxw_%=:\n" \
  "buffer_load_dwordx2 v[6:7], v2, s[52:55], s4 offen sc1\n" \
  "s_waitcnt vmcnt(0)\n" \
  "v_xor_b32 v6, v6, v4\n" \
  "v_xor_b32 v7, v7, v5\n" \
  "v_or_b32 v6, v6, v7\n" \
  "v_cmp_ne_u32 vcc, 0, v6\n" \
  "s_sub_u32 s87, s87, 1\n" \
  "s_cbranch_scc1 Lxd_%=\n" \
  "s_nop 2\n" \
  "s_cbranch_vccnz Lxw_%=\n" \
  "Lxd_%=:\n" \
  "s_add_u32 m0, s88, 0\n" \
  "s_nop 0\n" \
  "buffer_load_dwordx4 v230, s[8:11], 0 offen sc1 lds\n" \
  "s_add_u32 m0, m0, 2048\n" \
  "s_nop 0\n" \
  "buffer_load_dwordx4 v230, s[8:11], s72 offen sc1 lds\n" \
  "s_add_u32 m0, m0, 2048\n" \
  "s_nop 0\n" \
  "buffer_load_dwordx4 v230, s[8:11], s73 offen sc1 lds\n" \
  "s_add_u32 m0, m0, 2048\n" \
  "s_nop 0\n" \
  "buffer_load_dwordx4 v230, s[8:11], s74 offen sc1 lds\n" \
  "s_add_u32 m0, m0, 2048\n" \
  "s_nop 0\n" \
  "buffer_load_dwordx4 v230, s[8:11], s75 offen sc1 lds\n" \
  "s_add_u32 m0, m0, 2048\n" \
  "s_nop 0\n" \
  "buffer_load_dwordx4 v230, s[8:11], s76 offen sc1 lds\n" \
  "s_add_u32 m0, m0, 2048\n" \
  "s_nop 0\n" \
  "buffer_load_dwordx4 v230, s[8:11], s77 offen sc1 lds\n" \
  "s_add_u32 m0, m0, 2048\n" \
  "s_nop 0\n" \
  "buffer_load_dwordx4 v230, s[8:11], s78 offen sc1 lds\n" \
  "s_add_u32 s8, s8, 128\n" \
  "s_addc_u32 s9, s9, 0\n" \
  "s_mov_b32 s98, 28928\n" \
  "s_add_u32 m0, s89, s98\n" \
  "s_nop 0\n" \
  "buffer_load_dwordx4 v231, s[12:15], 0 offen lds\n" \
  "s_add_u32 m0, m0, 1024\n" \
  "s_nop 0\n" \
  "buffer_load_dwordx4 v232, s[12:15], 0 offen lds\n" \
  "s_add_u32 m0, m0, 1024\n" \
  "s_nop 0\n" \
  "buffer_load_dwordx4 v233, s[12:15], 0 offen lds\n" \
  "s_add_u32 m0, m0, 1024\n" \
  "s_nop 0\n" \
  "buffer_load_dwordx4 v234, s[12:15], 0 offen lds\n" \
  "s_add_u32 m0, m0, 1024\n" \
  "s_nop 0\n" \
  "buffer_load_dwordx4 v235, s[12:15], 0 offen lds\n" \
  "s_add_u32 m0, m0, 1024\n" \
  "s_nop 0\n" \
  "buffer_load_dwordx4 v236, s[12:15], 0 offen lds\n" \
  "s_add_u32 m0, m0, 1024\n" \
  "s_nop 0\n" \
  "buffer_load_dwordx4 v237, s[12:15], 0 offen lds\n" \
  "s_add_u32 s12, s12, 458752\n" \
  "s_addc_u32 s13, s13, 0\n" \
  "s_sub_u32 s14, s14, 458752\n" \
  "s_add_u32 s98, s98, 28928\n" \
  "s_cmp_eq_u32 s98, 86784\n" \
  "s_cselect_b32 s98, 0, s98\n"

#define ASM_BLOCK1 \
  "s_load_dwordx2 s[56:57], s[62:63], 0\n" \
  "s_load_dwordx2 s[20:21], s[62:63], 64\n" \
  "s_load_dwordx2 s[16:17], s[62:63], 72\n" \
  "s_load_dwordx2 s[44:45], s[62:63], 80\n" \
  "s_load_dwordx2 s[48:49], s[62:63], 88\n" \
  "s_load_dwordx2 s[32:33], s[62:63], 96\n" \
  "s_load_dwordx2 s[64:65], s[62:63], 104\n" \
  "s_mov_b32 s72, 32768\n" \
  "s_mov_b32 s73, 65536\n" \
  "s_mov_b32 s74, 98304\n" \
  "s_mov_b32 s75, 131072\n" \
  "s_mov_b32 s76, 163840\n" \
  "s_mov_b32 s77, 196608\n" \
  "s_mov_b32 s78, 229376\n" \
  "s_mov_b32 s85, 1376256\n" \
  "s_mov_b32 s98, 57856\n" \
  "s_mov_b32 s97, 0\n" \
  "s_mov_b32 s68, 4294909440\n" \
  "s_mov_b32 s69, 28928\n" \
  "s_add_u32 m0, s88, 32768\n" \
  "s_nop 0\n" \
  "buffer_load_dwordx4 v230, s[8:11], 0 offen sc1 lds\n" \
  "s_add_u32 m0, m0, 2048\n" \
  "s_nop 0\n" \
  "buffer_load_dwordx4 v230, s[8:11], s72 offen sc1 lds\n" \
  "s_add_u32 m0, m0, 2048\n" \
  "s_nop 0\n" \
  "buffer_load_dwordx4 v230, s[8:11], s73 offen sc1 lds\n" \
  "s_add_u32 m0, m0, 2048\n" \
  "s_nop 0\n" \
  "buffer_load_dwordx4 v230, s[8:11], s74 offen sc1 lds\n" \
  "s_add_u32 m0, m0, 2048\n" \
  "s_nop 0\n" \
  "buffer_load_dwordx4 v230, s[8:11], s75 offen sc1 lds\n" \
  "s_add_u32 m0, m0, 2048\n" \
  "s_nop 0\n" \
  "buffer_load_dwordx4 v230, s[8:11], s76 offen sc1 lds\n" \
  "s_add_u32 m0, m0, 2048\n" \
  "s_nop 0\n" \
  "buffer_load_dwordx4 v230, s[8:11], s77 offen sc1 lds\n" \
  "s_add_u32 m0, m0, 2048\n" \
  "s_nop 0\n" \
  "buffer_load_dwordx4 v230, s[8:11], s78 offen sc1 lds\n" \
  "s_add_u32 s8, s8, 128\n" \
  "s_addc_u32 s9, s9, 0\n" \
  "s_waitcnt lgkmcnt(0)\n" \
  "s_getpc_b64 s[62:63]\n" \
  "s_waitcnt vmcnt(15)\n" \
  "s_barrier\n" \
  "ds_read_b64_tr_b16 a[128:129], v226 offset:0\n" \
  "ds_read_b64_tr_b16 a[130:131], v226 offset:1792\n" \
  "ds_read_b64_tr_b16 a[132:133], v226 offset:7168\n" \
  "ds_read_b64_tr_b16 a[134:135], v226 offset:8960\n" \
  "ds_read_b128 a[0:3], v224 offset:0\n" \
  "ds_read_b128 a[4:7], v225 offset:0\n" \
  "ds_read_b128 a[8:11], v224 offset:2048\n" \
  "ds_read_b128 a[12:15], v225 offset:2048\n" \
  "ds_read_b128 a[16:19], v224 offset:4096\n" \
  "ds_read_b128 a[20:23], v225 offset:4096\n" \
  "ds_read_b128 a[24:27], v224 offset:6144\n" \
  "ds_read_b128 a[28:31], v225 offset:6144\n" \
  "ds_read_b128 a[32:35], v224 offset:8192\n" \
  "ds_read_b128 a[36:39], v225 offset:8192\n" \
  "ds_read_b128 a[40:43], v224 offset:10240\n" \
  "ds_read_b128 a[44:47], v225 offset:10240\n" \
  "ds_read_b128 a[48:51], v224 offset:12288\n" \
  "ds_read_b128 a[52:55], v225 offset:12288\n" \
  "ds_read_b128 a[56:59], v224 offset:14336\n" \
  "ds_read_b128 a[60:63], v225 offset:14336\n" \
  "ds_read_b64_tr_b16 a[136:137], v226 offset:14352\n" \
  "ds_read_b64_tr_b16 a[138:139], v226 offset:16144\n" \
  "ds_read_b64_tr_b16 a[140:141], v226 offset:21520\n" \
  "ds_read_b64_tr_b16 a[142:143], v226 offset:23312\n" \
  "ds_read_b64_tr_b16 a[144:145], v226 offset:32\n" \
  "ds_read_b64_tr_b16 a[146:147], v226 offset:1824\n" \
  "ds_read_b64_tr_b16 a[148:149], v226 offset:7200\n" \
  "ds_read_b64_tr_b16 a[150:151], v226 offset:8992\n" \
  "ds_read_b64_tr_b16 a[152:153], v226 offset:14384\n" \
  "ds_read_b64_tr_b16 a[154:155], v226 offset:16176\n" \
  "ds_read_b64_tr_b16 a[156:157], v226 offset:21552\n" \
  "ds_read_b64_tr_b16 a[158:159], v226 offset:23344\n" \
  "ds_read_b64_tr_b16 a[160:161], v226 offset:64\n" \
  "ds_read_b64_tr_b16 a[162:163], v226 offset:1856\n" \
  "ds_read_b64_tr_b16 a[164:165], v226 offset:7232\n" \
  "ds_read_b64_tr_b16 a[166:167], v226 offset:9024\n" \
  "ds_read_b64_tr_b16 a[168:169], v226 offset:14416\n" \
  "ds_read_b64_tr_b16 a[170:171], v226 offset:16208\n" \
  "ds_read_b64_tr_b16 a[172:173], v226 offset:21584\n" \
  "ds_read_b64_tr_b16 a[174:175], v226 offset:23376\n" \
  "ds_read_b64_tr_b16 a[176:177], v227 offset:0\n" \
  "ds_read_b64_tr_b16 a[178:179], v227 offset:1792\n" \
  "ds_read_b64_tr_b16 a[180:181], v227 offset:7168\n" \
  "ds_read_b64_tr_b16 a[182:183], v227 offset:8960\n" \
  "s_waitcnt lgkmcnt(15)\n" \
  "v_mfma_f32_16x16x32_bf16 v[0:3], a[128:131], a[0:3], 0\n" \
  "v_mfma_f32_16x16x32_bf16 v[0:3], a[132:135], a[4:7], v[0:3]\n" \
  "s_and_b32 s99, s90, 1024\n" \
  "s_cselect_b64 s[20:21], s[16:17], s[20:21]\n" \
  "v_mfma_f32_16x16x32_bf16 v[4:7], a[132:135], a[12:15], 0\n" \
  "s_add_u32 m0, s89, s98\n" \
  "v_mfma_f32_16x16x32_bf16 v[4:7], a[128:131], a[8:11], v[4:7]\n" \
  "buffer_load_dwordx4 v231, s[12:15], 0 offen lds\n" \
  "v_mfma_f32_16x16x32_bf16 v[8:11], a[128:131], a[16:19], 0\n" \
  "s_add_u32 s20, s20, s22\n" \
  "s_addc_u32 s21, s21, 0\n" \
  "v_mfma_f32_16x16x32_bf16 v[8:11], a[132:135], a[20:23], v[8:11]\n" \
  "s_and_b32 s21, s21, 0xffff\n" \
  "s_sub_u32 s22, 2097152, s22\n" \
  "v_mfma_f32_16x16x32_bf16 v[12:15], a[132:135], a[28:31], 0\n" \
  "s_add_u32 m0, m0, 1024\n" \
  "v_mfma_f32_16x16x32_bf16 v[12:15], a[128:131], a[24:27], v[12:15]\n" \
  "buffer_load_dwordx4 v232, s[12:15], 0 offen lds\n" \
  "v_mfma_f32_16x16x32_bf16 v[16:19], a[128:131], a[32:35], 0\n" \
  "s_mov_b32 s23, 0x20000\n" \
  "v_mfma_f32_16x16x32_bf16 v[16:19], a[132:135], a[36:39], v[16:19]\n" \
  "s_add_u32 s32, s32, s34\n" \
  "s_addc_u32 s33, s33, 0\n" \
  "v_mfma_f32_16x16x32_bf16 v[20:23], a[132:135], a[44:47], 0\n" \
  "s_add_u32 m0, m0, 1024\n" \
  "v_mfma_f32_16x16x32_bf16 v[20:23], a[128:131], a[40:43], v[20:23]\n" \
  "buffer_load_dwordx4 v233, s[12:15], 0 offen lds\n" \
  "v_mfma_f32_16x16x32_bf16 v[24:27], a[128:131], a[48:51], 0\n" \
  "s_and_b32 s33, s33, 0xffff\n" \
  "s_sub_u32 s34, 7340032, s34\n" \
  "v_mfma_f32_16x16x32_bf16 v[24:27], a[132:135], a[52:55], v[24:27]\n" \
  "s_mov_b32 s35, 0x20000\n" \
  "v_mfma_f32_16x16x32_bf16 v[28:31], a[132:135], a[60:63], 0\n" \
  "s_add_u32 m0, m0, 1024\n" \
  "v_mfma_f32_16x16x32_bf16 v[28:31], a[128:131], a[56:59], v[28:31]\n" \
  "buffer_load_dwordx4 v234, s[12:15], 0 offen lds\n" \
  "v_mfma_f32_16x16x32_bf16 v[32:35], a[136:139], a[0:3], 0\n" \
  "s_add_u32 s44, s44, s46\n" \
  "s_addc_u32 s45, s45, 0\n" \
  "v_mfma_f32_16x16x32_bf16 v[32:35], a[140:143], a[4:7], v[32:35]\n" \
  "s_and_b32 s45, s45, 0xffff\n" \
  "s_sub_u32 s46, 2097152, s46\n" \
  "v_mfma_f32_16x16x32_bf16 v[36:39], a[140:143], a[12:15], 0\n" \
  "s_mov_b32 s47, 0x20000\n" \
  "v_mfma_f32_16x16x32_bf16 v[36:39], a[136:139], a[8:11], v[36:39]\n" \
  "s_add_u32 m0, m0, 1024\n" \
  "v_mfma_f32_16x16x32_bf16 v[40:43], a[136:139], a[16:19], 0\n" \
  "buffer_load_dwordx4 v235, s[12:15], 0 offen lds\n" \
  "v_mfma_f32_16x16x32_bf16 v[40:43], a[140:143], a[20:23], v[40:43]\n" \
  "s_add_u32 s48, s48, s50\n" \
  "s_addc_u32 s49, s49, 0\n" \
  "v_mfma_f32_16x16x32_bf16 v[44:47], a[140:143], a[28:31], 0\n" \
  "s_and_b32 s49, s49, 0xffff\n" \
  "s_sub_u32 s50, 2097152, s50\n" \
  "v_mfma_f32_16x16x32_bf16 v[44:47], a[136:139], a[24:27], v[44:47]\n" \
  "s_add_u32 m0, m0, 1024\n" \
  "v_mfma_f32_16x16x32_bf16 v[48:51], a[136:139], a[32:35], 0\n" \
  "buffer_load_dwordx4 v236, s[12:15], 0 offen lds\n" \
  "v_mfma_f32_16x16x32_bf16 v[48:51], a[140:143], a[36:39], v[48:51]\n" \
  "s_mov_b32 s51, 0x20000\n" \
  "v_mfma_f32_16x16x32_bf16 v[52:55], a[140:143], a[44:47], 0\n" \
  "s_add_u32 s56, s56, s58\n" \
  "s_addc_u32 s57, s57, 0\n" \
  "v_mfma_f32_16x16x32_bf16 v[52:55], a[136:139], a[40:43], v[52:55]\n" \
  "s_add_u32 m0, m0, 1024\n" \
  "v_mfma_f32_16x16x32_bf16 v[56:59], a[136:139], a[48:51], 0\n" \
  "buffer_load_dwordx4 v237, s[12:15], 0 offen lds\n" \
  "v_mfma_f32_16x16x32_bf16 v[56:59], a[140:143], a[52:55], v[56:59]\n" \
  "s_and_b32 s57, s57, 0xffff\n" \
  "s_sub_u32 s58, 4194304, s58\n" \
  "v_mfma_f32_16x16x32_bf16 v[60:63], a[140:143], a[60:63], 0\n" \
  "s_mov_b32 s59, 0x20000\n" \
  "v_mfma_f32_16x16x32_bf16 v[60:63], a[136:139], a[56:59], v[60:63]\n" \
  "s_add_u32 s64, s64, s66\n" \
  "s_addc_u32 s65, s65, 0\n" \
  "v_mfma_f32_16x16x32_bf16 v[64:67], a[144:147], a[0:3], 0\n" \
  "s_and_b32 s65, s65, 0xffff\n" \
  "s_sub_u32 s66, 8388608, s66\n" \
  "v_mfma_f32_16x16x32_bf16 v[64:67], a[148:151], a[4:7], v[64:67]\n" \
  "s_mov_b32 s67, 0x20000\n" \
  "v_mfma_f32_16x16x32_bf16 v[68:71], a[148:151], a[12:15], 0\n" \
  "s_add_u32 s16, s52, s18\n" \
  "s_addc_u32 s17, s53, 0\n" \
  "v_mfma_f32_16x16x32_bf16 v[68:71], a[144:147], a[8:11], v[68:71]\n" \
  "s_add_u32 s12, s12, 458752\n" \
  "s_addc_u32 s13, s13, 0\n" \
  "s_sub_u32 s14, s14, 458752\n" \
  "v_mfma_f32_16x16x32_bf16 v[72:75], a[144:147], a[16:19], 0\n" \
  "s_add_u32 s98, s98, 28928\n" \
  "s_cmp_eq_u32 s98, 86784\n" \
  "s_cselect_b32 s98, 0, s98\n" \
  "v_mfma_f32_16x16x32_bf16 v[72:75], a[148:151], a[20:23], v[72:75]\n" \
  "s_add_u32 s97, s97, 28928\n" \
  "s_cmp_eq_u32 s97, 86784\n" \
  "s_cselect_b32 s99, s68, s69\n" \
  "s_cselect_b32 s97, 0, s97\n" \
  "v_mfma_f32_16x16x32_bf16 v[76:79], a[148:151], a[28:31], 0\n" \
  "v_mfma_f32_16x16x32_bf16 v[76:79], a[144:147], a[24:27], v[76:79]\n" \
  "v_add_u32 v226, s99, v226\n" \
  "v_add_u32 v227, s99, v227\n" \
  "v_mfma_f32_16x16x32_bf16 v[80:83], a[144:147], a[32:35], 0\n" \
  "s_waitcnt lgkmcnt(0)\n" \
  "s_waitcnt vmcnt(7)\n" \
  "s_barrier\n" \
  "v_mfma_f32_16x16x32_bf16 v[80:83], a[148:151], a[36:39], v[80:83]\n" \
  "ds_read_b128 a[64:67], v224 offset:32768\n" \
  "ds_read_b64_tr_b16 a[128:129], v226 offset:0\n" \
  "v_mfma_f32_16x16x32_bf16 v[84:87], a[148:151], a[44:47], 0\n" \
  "s_add_u32 m0, s88, 0\n" \
  "v_mfma_f32_16x16x32_bf16 v[84:87], a[144:147], a[40:43], v[84:87]\n" \
  "buffer_load_dwordx4 v230, s[8:11], 0 offen sc1 lds\n" \
  "ds_read_b64_tr_b16 a[130:131], v226 offset:1792\n" \
  "s_add_u32 m0, m0, 2048\n" \
  "v_mfma_f32_16x16x32_bf16 v[88:91], a[144:147], a[48:51], 0\n" \
  "buffer_load_dwordx4 v230, s[8:11], s72 offen sc1 lds\n" \
  "ds_read_b128 a[68:71], v225 offset:32768\n" \
  "v_mfma_f32_16x16x32_bf16 v[88:91], a[148:151], a[52:55], v[88:91]\n" \
  "ds_read_b64_tr_b16 a[132:133], v226 offset:7168\n" \
  "s_add_u32 m0, m0, 2048\n" \
  "v_mfma_f32_16x16x32_bf16 v[92:95], a[148:151], a[60:63], 0\n" \
  "buffer_load_dwordx4 v230, s[8:11], s73 offen sc1 lds\n" \
  "s_add_u32 m0, m0, 2048\n" \
  "v_mfma_f32_16x16x32_bf16 v[92:95], a[144:147], a[56:59], v[92:95]\n" \
  "buffer_load_dwordx4 v230, s[8:11], s74 offen sc1 lds\n" \
  "ds_read_b64_tr_b16 a[134:135], v226 offset:8960\n" \
  "v_mfma_f32_16x16x32_bf16 v[96:99], a[152:155], a[0:3], 0\n" \
  "ds_read_b128 a[72:75], v224 offset:34816\n" \
  "s_add_u32 m0, m0, 2048\n" \
  "v_mfma_f32_16x16x32_bf16 v[96:99], a[156:159], a[4:7], v[96:99]\n" \
  "buffer_load_dwordx4 v230, s[8:11], s75 offen sc1 lds\n" \
  "ds_read_b64_tr_b16 a[136:137], v226 offset:14352\n" \
  "s_add_u32 m0, m0, 2048\n" \
  "v_mfma_f32_16x16x32_bf16 v[100:103], a[156:159], a[12:15], 0\n" \
  "buffer_load_dwordx4 v230, s[8:11], s76 offen sc1 lds\n" \
  "v_mfma_f32_16x16x32_bf16 v[100:103], a[152:155], a[8:11], v[100:103]\n" \
  "ds_read_b64_tr_b16 a[138:139], v226 offset:16144\n" \
  "s_add_u32 m0, m0, 2048\n" \
  "v_mfma_f32_16x16x32_bf16 v[104:107], a[152:155], a[16:19], 0\n" \
  "buffer_load_dwordx4 v230, s[8:11], s77 offen sc1 lds\n" \
  "ds_read_b128 a[76:79], v225 offset:34816\n" \
  "s_add_u32 m0, m0, 2048\n" \
  "v_mfma_f32_16x16x32_bf16 v[104:107], a[156:159], a[20:23], v[104:107]\n" \
  "buffer_load_dwordx4 v230, s[8:11], s78 offen sc1 lds\n" \
  "ds_read_b64_tr_b16 a[140:141], v226 offset:21520\n" \
  "v_mfma_f32_16x16x32_bf16 v[108:111], a[156:159], a[28:31], 0\n" \
  "s_add_u32 s8, s8, 128\n" \
  "s_addc_u32 s9, s9, 0\n" \
  "v_mfma_f32_16x16x32_bf16 v[108:111], a[152:155], a[24:27], v[108:111]\n" \
  "ds_read_b128 a[80:83], v224 offset:36864\n" \
  "ds_read_b64_tr_b16 a[142:143], v226 offset:23312\n" \
  "s_mov_b32 m0, 162560\n" \
  "v_mfma_f32_16x16x32_bf16 v[112:115], a[152:155], a[32:35], 0\n" \
  "buffer_load_dword v229, s[12:15], s85 offen lds\n" \
  "v_mfma_f32_16x16x32_bf16 v[112:115], a[156:159], a[36:39], v[112:115]\n" \
  "ds_read_b64_tr_b16 a[144:145], v226 offset:32\n" \
  "v_mfma_f32_16x16x32_bf16 v[116:119], a[156:159], a[44:47], 0\n" \
  "v_accvgpr_read_b32 v252, a213\n" \
  "s_mov_b32 m0, 162560\n" \
  "s_nop 0\n" \
  "global_load_lds_dword v252, s[62:63]\n" \
  "v_mfma_f32_16x16x32_bf16 v[116:119], a[152:155], a[40:43], v[116:119]\n" \
  "ds_read_b128 a[84:87], v225 offset:36864\n" \
  "ds_read_b64_tr_b16 a[146:147], v226 offset:1824\n" \
  "v_mfma_f32_16x16x32_bf16 v[120:123], a[152:155], a[48:51], 0\n" \
  "s_add_u32 s16, s16, 9439104\n" \
  "s_addc_u32 s17, s17, 0\n" \
  "v_mfma_f32_16x16x32_bf16 v[120:123], a[156:159], a[52:55], v[120:123]\n" \
  "ds_read_b64_tr_b16 a[148:149], v226 offset:7200\n" \
  "v_mfma_f32_16x16x32_bf16 v[124:127], a[156:159], a[60:63], 0\n" \
  "s_add_u32 m0, s89, s98\n" \
  "v_mfma_f32_16x16x32_bf16 v[124:127], a[152:155], a[56:59], v[124:127]\n" \
  "buffer_load_dwordx4 v231, s[12:15], 0 offen lds\n" \
  "ds_read_b128 a[88:91], v224 offset:38912\n" \
  "ds_read_b64_tr_b16 a[150:151], v226 offset:8992\n" \
  "v_mfma_f32_16x16x32_bf16 v[128:131], a[160:163], a[0:3], 0\n" \
  "s_sub_u32 s18, 4194304, s18\n" \
  "s_mov_b32 s19, 0x20000\n" \
  "v_mfma_f32_16x16x32_bf16 v[128:131], a[164:167], a[4:7], v[128:131]\n" \
  "ds_read_b64_tr_b16 a[152:153], v226 offset:14384\n" \
  "v_mfma_f32_16x16x32_bf16 v[132:135], a[164:167], a[12:15], 0\n" \
  "ds_read_b128 a[92:95], v225 offset:38912\n" \
  "v_mfma_f32_16x16x32_bf16 v[132:135], a[160:163], a[8:11], v[132:135]\n" \
  "ds_read_b64_tr_b16 a[154:155], v226 offset:16176\n" \
  "s_add_u32 m0, m0, 1024\n" \
  "v_mfma_f32_16x16x32_bf16 v[136:139], a[160:163], a[16:19], 0\n" \
  "buffer_load_dwordx4 v232, s[12:15], 0 offen lds\n" \
  "v_mfma_f32_16x16x32_bf16 v[136:139], a[164:167], a[20:23], v[136:139]\n" \
  "ds_read_b64_tr_b16 a[156:157], v226 offset:21552\n" \
  "v_mfma_f32_16x16x32_bf16 v[140:143], a[164:167], a[28:31], 0\n" \
  "ds_read_b128 a[96:99], v224 offset:40960\n" \
  "v_mfma_f32_16x16x32_bf16 v[140:143], a[160:163], a[24:27], v[140:143]\n" \
  "ds_read_b64_tr_b16 a[158:159], v226 offset:23344\n" \
  "v_mfma_f32_16x16x32_bf16 v[144:147], a[160:163], a[32:35], 0\n" \
  "s_add_u32 s24, s52, s26\n" \
  "s_addc_u32 s25, s53, 0\n" \
  "v_mfma_f32_16x16x32_bf16 v[144:147], a[164:167], a[36:39], v[144:147]\n" \
  "s_add_u32 m0, m0, 1024\n" \
  "v_mfma_f32_16x16x32_bf16 v[148:151], a[164:167], a[44:47], 0\n" \
  "buffer_load_dwordx4 v233, s[12:15], 0 offen lds\n" \
  "ds_read_b128 a[100:103], v225 offset:40960\n" \
  "v_mfma_f32_16x16x32_bf16 v[148:151], a[160:163], a[40:43], v[148:151]\n" \
  "s_add_u32 s24, s24, 13631488\n" \
  "s_addc_u32 s25, s25, 0\n" \
  "v_mfma_f32_16x16x32_bf16 v[152:155], a[160:163], a[48:51], 0\n" \
  "s_sub_u32 s26, 14680064, s26\n" \
  "s_mov_b32 s27, 0x20000\n" \
  "v_mfma_f32_16x16x32_bf16 v[152:155], a[164:167], a[52:55], v[152:155]\n" \
  "ds_read_b128 a[104:107], v224 offset:43008\n" \
  "v_mfma_f32_16x16x32_bf16 v[156:159], a[164:167], a[60:63], 0\n" \
  "s_add_u32 s28, s52, s30\n" \
  "s_addc_u32 s29, s53, 0\n" \
  "v_mfma_f32_16x16x32_bf16 v[156:159], a[160:163], a[56:59], v[156:159]\n" \
  "s_add_u32 m0, m0, 1024\n" \
  "v_mfma_f32_16x16x32_bf16 v[160:163], a[168:171], a[0:3], 0\n" \
  "buffer_load_dwordx4 v234, s[12:15], 0 offen lds\n" \
  "ds_read_b64_tr_b16 a[160:161], v226 offset:64\n" \
  "v_mfma_f32_16x16x32_bf16 v[160:163], a[172:175], a[4:7], v[160:163]\n" \
  "ds_read_b128 a[108:111], v225 offset:43008\n" \
  "v_mfma_f32_16x16x32_bf16 v[164:167], a[172:175], a[12:15], 0\n" \
  "ds_read_b64_tr_b16 a[162:163], v226 offset:1856\n" \
  "v_mfma_f32_16x16x32_bf16 v[164:167], a[168:171], a[8:11], v[164:167]\n" \
  "s_add_u32 s28, s28, 13631488\n" \
  "s_addc_u32 s29, s29, 0\n" \
  "v_mfma_f32_16x16x32_bf16 v[168:171], a[168:171], a[16:19], 0\n" \
  "ds_read_b64_tr_b16 a[164:165], v226 offset:7232\n" \
  "s_add_u32 m0, m0, 1024\n" \
  "v_mfma_f32_16x16x32_bf16 v[168:171], a[172:175], a[20:23], v[168:171]\n" \
  "buffer_load_dwordx4 v235, s[12:15], 0 offen lds\n" \
  "ds_read_b128 a[112:115], v224 offset:45056\n" \
  "v_mfma_f32_16x16x32_bf16 v[172:175], a[172:175], a[28:31], 0\n" \
  "ds_read_b64_tr_b16 a[166:167], v226 offset:9024\n" \
  "v_mfma_f32_16x16x32_bf16 v[172:175], a[168:171], a[24:27], v[172:175]\n" \
  "s_sub_u32 s30, 14680064, s30\n" \
  "s_mov_b32 s31, 0x20000\n" \
  "v_mfma_f32_16x16x32_bf16 v[176:179], a[168:171], a[32:35], 0\n" \
  "ds_read_b128 a[116:119], v225 offset:45056\n" \
  "v_mfma_f32_16x16x32_bf16 v[176:179], a[172:175], a[36:39], v[176:179]\n" \
  "s_add_u32 s36, s52, s38\n" \
  "s_addc_u32 s37, s53, 0\n" \
  "v_mfma_f32_16x16x32_bf16 v[180:183], a[172:175], a[44:47], 0\n" \
  "s_add_u32 m0, m0, 1024\n" \
  "v_mfma_f32_16x16x32_bf16 v[180:183], a[168:171], a[40:43], v[180:183]\n" \
  "buffer_load_dwordx4 v236, s[12:15], 0 offen lds\n" \
  "v_mfma_f32_16x16x32_bf16 v[184:187], a[168:171], a[48:51], 0\n" \
  "ds_read_b128 a[120:123], v224 offset:47104\n" \
  "v_mfma_f32_16x16x32_bf16 v[184:187], a[172:175], a[52:55], v[184:187]\n" \
  "s_add_u32 s36, s36, 28311552\n" \
  "s_addc_u32 s37, s37, 0\n" \
  "v_mfma_f32_16x16x32_bf16 v[188:191], a[172:175], a[60:63], 0\n" \
  "s_sub_u32 s38, 8388608, s38\n" \
  "s_mov_b32 s39, 0x20000\n" \
  "v_mfma_f32_16x16x32_bf16 v[188:191], a[168:171], a[56:59], v[188:191]\n" \
  "s_add_u32 s40, s52, s42\n" \
  "s_addc_u32 s41, s53, 0\n" \
  "v_mfma_f32_16x16x32_bf16 v[192:195], a[176:179], a[0:3], 0\n" \
  "ds_read_b128 a[124:127], v225 offset:47104\n" \
  "ds_read_b64_tr_b16 a[168:169], v226 offset:14416\n" \
  "s_add_u32 m0, m0, 1024\n" \
  "v_mfma_f32_16x16x32_bf16 v[192:195], a[180:183], a[4:7], v[192:195]\n" \
  "buffer_load_dwordx4 v237, s[12:15], 0 offen lds\n" \
  "v_mfma_f32_16x16x32_bf16 v[196:199], a[180:183], a[12:15], 0\n" \
  "ds_read_b64_tr_b16 a[170:171], v226 offset:16208\n" \
  "v_mfma_f32_16x16x32_bf16 v[196:199], a[176:179], a[8:11], v[196:199]\n" \
  "s_add_u32 s40, s40, 28311552\n" \
  "s_addc_u32 s41, s41, 0\n" \
  "v_mfma_f32_16x16x32_bf16 v[200:203], a[176:179], a[16:19], 0\n" \
  "ds_read_b64_tr_b16 a[172:173], v226 offset:21584\n" \
  "v_mfma_f32_16x16x32_bf16 v[200:203], a[180:183], a[20:23], v[200:203]\n" \
  "s_sub_u32 s42, 8388608, s42\n" \
  "s_mov_b32 s43, 0x20000\n" \
  "v_mfma_f32_16x16x32_bf16 v[204:207], a[180:183], a[28:31], 0\n" \
  "ds_read_b64_tr_b16 a[174:175], v226 offset:23376\n" \
  "v_mfma_f32_16x16x32_bf16 v[204:207], a[176:179], a[24:27], v[204:207]\n" \
  "s_mov_b32 s79, 8192\n" \
  "s_mov_b32 s82, 4096\n" \
  "v_mfma_f32_16x16x32_bf16 v[208:211], a[176:179], a[32:35], 0\n" \
  "s_add_u32 s12, s12, 458752\n" \
  "s_addc_u32 s13, s13, 0\n" \
  "s_sub_u32 s14, s14, 458752\n" \
  "v_mfma_f32_16x16x32_bf16 v[208:211], a[180:183], a[36:39], v[208:211]\n" \
  "s_add_u32 s98, s98, 28928\n" \
  "s_cmp_eq_u32 s98, 86784\n" \
  "s_cselect_b32 s98, 0, s98\n" \
  "v_mfma_f32_16x16x32_bf16 v[212:215], a[180:183], a[44:47], 0\n" \
  "s_mov_b32 s80, 16384\n" \
  "s_mov_b32 s83, 8192\n" \
  "v_mfma_f32_16x16x32_bf16 v[212:215], a[176:179], a[40:43], v[212:215]\n" \
  "s_mov_b32 s81, 24576\n" \
  "s_mov_b32 s84, 12288\n" \
  "v_mfma_f32_16x16x32_bf16 v[216:219], a[176:179], a[48:51], 0\n" \
  "s_mov_b32 s86, 0xbfb8aa3b\n" \
  "s_mov_b32 s96, 6\n" \
  "v_mfma_f32_16x16x32_bf16 v[216:219], a[180:183], a[52:55], v[216:219]\n" \
  "v_mfma_f32_16x16x32_bf16 v[220:223], a[180:183], a[60:63], 0\n" \
  "v_mfma_f32_16x16x32_bf16 v[220:223], a[176:179], a[56:59], v[220:223]\n" \
  ".p2align 4\n" \
  "LloopA_%=:\n" \
  "s_waitcnt lgkmcnt(0)\n" \
  "v_mfma_f32_16x16x32_bf16 v[0:3], a[128:131], a[64:67], v[0:3]\n" \
  "ds_read_b64_tr_b16 a[176:177], v227 offset:0\n" \
  "v_mfma_f32_16x16x32_bf16 v[0:3], a[132:135], a[68:71], v[0:3]\n" \
  "ds_read_b64_tr_b16 a[178:179], v227 offset:1792\n" \
  "v_mfma_f32_16x16x32_bf16 v[4:7], a[132:135], a[76:79], v[4:7]\n" \
  "ds_read_b64_tr_b16 a[180:181], v227 offset:7168\n" \
  "v_mfma_f32_16x16x32_bf16 v[4:7], a[128:131], a[72:75], v[4:7]\n" \
  "ds_read_b64_tr_b16 a[182:183], v227 offset:8960\n" \
  "v_mfma_f32_16x16x32_bf16 v[8:11], a[128:131], a[80:83], v[8:11]\n" \
  "v_mfma_f32_16x16x32_bf16 v[8:11], a[132:135], a[84:87], v[8:11]\n" \
  "v_mfma_f32_16x16x32_bf16 v[12:15], a[132:135], a[92:95], v[12:15]\n" \
  "s_add_u32 s97, s97, 28928\n" \
  "s_cmp_eq_u32 s97, 86784\n" \
  "s_cselect_b32 s99, s68, s69\n" \
  "s_cselect_b32 s97, 0, s97\n" \
  "v_mfma_f32_16x16x32_bf16 v[12:15], a[128:131], a[88:91], v[12:15]\n" \
  "v_mfma_f32_16x16x32_bf16 v[16:19], a[128:131], a[96:99], v[16:19]\n" \
  "v_add_u32 v226, s99, v226\n" \
  "v_add_u32 v227, s99, v227\n" \
  "v_mfma_f32_16x16x32_bf16 v[16:19], a[132:135], a[100:103], v[16:19]\n" \
  "s_waitcnt lgkmcnt(0)\n" \
  "s_waitcnt vmcnt(9)\n" \
  "s_barrier\n" \
  "v_mfma_f32_16x16x32_bf16 v[20:23], a[132:135], a[108:111], v[20:23]\n" \
  "ds_read_b128 a[0:3], v224 offset:0\n" \
  "v_mfma_f32_16x16x32_bf16 v[20:23], a[128:131], a[104:107], v[20:23]\n" \
  "s_add_u32 m0, s88, 32768\n" \
  "v_mfma_f32_16x16x32_bf16 v[24:27], a[128:131], a[112:115], v[24:27]\n" \
  "buffer_load_dwordx4 v230, s[8:11], 0 offen sc1 lds\n" \
  "v_mfma_f32_16x16x32_bf16 v[24:27], a[132:135], a[116:119], v[24:27]\n" \
  "v_mfma_f32_16x16x32_bf16 v[28:31], a[132:135], a[124:127], v[28:31]\n" \
  "ds_read_b128 a[4:7], v225 offset:0\n" \
  "v_mfma_f32_16x16x32_bf16 v[28:31], a[128:131], a[120:123], v[28:31]\n" \
  "v_mfma_f32_16x16x32_bf16 v[32:35], a[136:139], a[64:67], v[32:35]\n" \
  "ds_read_b64_tr_b16 a[128:129], v226 offset:0\n" \
  "s_add_u32 m0, m0, 2048\n" \
  "v_mfma_f32_16x16x32_bf16 v[32:35], a[140:143], a[68:71], v[32:35]\n" \
  "buffer_load_dwordx4 v230, s[8:11], s72 offen sc1 lds\n" \
  "v_mfma_f32_16x16x32_bf16 v[36:39], a[140:143], a[76:79], v[36:39]\n" \
  "ds_read_b128 a[8:11], v224 offset:2048\n" \
  "ds_read_b64_tr_b16 a[130:131], v226 offset:1792\n" \
  "v_mfma_f32_16x16x32_bf16 v[36:39], a[136:139], a[72:75], v[36:39]\n" \
  "v_mfma_f32_16x16x32_bf16 v[40:43], a[136:139], a[80:83], v[40:43]\n" \
  "ds_read_b64_tr_b16 a[132:133], v226 offset:7168\n" \
  "v_mfma_f32_16x16x32_bf16 v[40:43], a[140:143], a[84:87], v[40:43]\n" \
  "s_add_u32 m0, m0, 2048\n" \
  "v_mfma_f32_16x16x32_bf16 v[44:47], a[140:143], a[92:95], v[44:47]\n" \
  "buffer_load_dwordx4 v230, s[8:11], s73 offen sc1 lds\n" \
  "ds_read_b64_tr_b16 a[134:135], v226 offset:8960\n" \
  "v_mfma_f32_16x16x32_bf16 v[44:47], a[136:139], a[88:91], v[44:47]\n" \
  "ds_read_b128 a[12:15], v225 offset:2048\n" \
  "v_mfma_f32_16x16x32_bf16 v[48:51], a[136:139], a[96:99], v[48:51]\n" \
  "v_mfma_f32_16x16x32_bf16 v[48:51], a[140:143], a[100:103], v[48:51]\n" \
  "v_mfma_f32_16x16x32_bf16 v[52:55], a[140:143], a[108:111], v[52:55]\n" \
  "s_add_u32 m0, m0, 2048\n" \
  "v_mfma_f32_16x16x32_bf16 v[52:55], a[136:139], a[104:107], v[52:55]\n" \
  "buffer_load_dwordx4 v230, s[8:11], s74 offen sc1 lds\n" \
  "ds_read_b128 a[16:19], v224 offset:4096\n" \
  "v_mfma_f32_16x16x32_bf16 v[56:59], a[136:139], a[112:115], v[56:59]\n" \
  "v_mfma_f32_16x16x32_bf16 v[56:59], a[140:143], a[116:119], v[56:59]\n" \
  "v_mfma_f32_16x16x32_bf16 v[60:63], a[140:143], a[124:127], v[60:63]\n" \
  "v_mfma_f32_16x16x32_bf16 v[60:63], a[136:139], a[120:123], v[60:63]\n" \
  "ds_read_b128 a[20:23], v225 offset:4096\n" \
  "s_add_u32 m0, m0, 2048\n" \
  "v_mfma_f32_16x16x32_bf16 v[64:67], a[144:147], a[64:67], v[64:67]\n" \
  "buffer_load_dwordx4 v230, s[8:11], s75 offen sc1 lds\n" \
  "ds_read_b64_tr_b16 a[136:137], v226 offset:14352\n" \
  "v_mfma_f32_16x16x32_bf16 v[64:67], a[148:151], a[68:71], v[64:67]\n" \
  "v_mfma_f32_16x16x32_bf16 v[68:71], a[148:151], a[76:79], v[68:71]\n" \
  "ds_read_b64_tr_b16 a[138:139], v226 offset:16144\n" \
  "v_mfma_f32_16x16x32_bf16 v[68:71], a[144:147], a[72:75], v[68:71]\n" \
  "v_mfma_f32_16x16x32_bf16 v[72:75], a[144:147], a[80:83], v[72:75]\n" \
  "ds_read_b128 a[24:27], v224 offset:6144\n" \
  "ds_read_b64_tr_b16 a[140:141], v226 offset:21520\n" \
  "s_add_u32 m0, m0, 2048\n" \
  "v_mfma_f32_16x16x32_bf16 v[72:75], a[148:151], a[84:87], v[72:75]\n" \
  "buffer_load_dwordx4 v230, s[8:11], s76 offen sc1 lds\n" \
  "v_mfma_f32_16x16x32_bf16 v[76:79], a[148:151], a[92:95], v[76:79]\n" \
  "ds_read_b64_tr_b16 a[142:143], v226 offset:23312\n" \
  "v_mfma_f32_16x16x32_bf16 v[76:79], a[144:147], a[88:91], v[76:79]\n" \
  "v_mfma_f32_16x16x32_bf16 v[80:83], a[144:147], a[96:99], v[80:83]\n" \
  "ds_read_b128 a[28:31], v225 offset:6144\n" \
  "v_mfma_f32_16x16x32_bf16 v[80:83], a[148:151], a[100:103], v[80:83]\n" \
  "s_add_u32 m0, m0, 2048\n" \
  "v_mfma_f32_16x16x32_bf16 v[84:87], a[148:151], a[108:111], v[84:87]\n" \
  "buffer_load_dwordx4 v230, s[8:11], s77 offen sc1 lds\n" \
  "v_mfma_f32_16x16x32_bf16 v[84:87], a[144:147], a[104:107], v[84:87]\n" \
  "v_mfma_f32_16x16x32_bf16 v[88:91], a[144:147], a[112:115], v[88:91]\n" \
  "v_mfma_f32_16x16x32_bf16 v[88:91], a[148:151], a[116:119], v[88:91]\n" \
  "ds_read_b128 a[32:35], v224 offset:8192\n" \
  "v_mfma_f32_16x16x32_bf16 v[92:95], a[148:151], a[124:127], v[92:95]\n" \
  "s_add_u32 m0, m0, 2048\n" \
  "v_mfma_f32_16x16x32_bf16 v[92:95], a[144:147], a[120:123], v[92:95]\n" \
  "buffer_load_dwordx4 v230, s[8:11], s78 offen sc1 lds\n" \
  "v_mfma_f32_16x16x32_bf16 v[96:99], a[152:155], a[64:67], v[96:99]\n" \
  "ds_read_b64_tr_b16 a[144:145], v226 offset:32\n" \
  "v_mfma_f32_16x16x32_bf16 v[96:99], a[156:159], a[68:71], v[96:99]\n" \
  "ds_read_b128 a[36:39], v225 offset:8192\n" \
  "v_mfma_f32_16x16x32_bf16 v[100:103], a[156:159], a[76:79], v[100:103]\n" \
  "ds_read_b64_tr_b16 a[146:147], v226 offset:1824\n" \
  "v_mfma_f32_16x16x32_bf16 v[100:103], a[152:155], a[72:75], v[100:103]\n" \
  "v_mfma_f32_16x16x32_bf16 v[104:107], a[152:155], a[80:83], v[104:107]\n" \
  "ds_read_b64_tr_b16 a[148:149], v226 offset:7200\n" \
  "v_mfma_f32_16x16x32_bf16 v[104:107], a[156:159], a[84:87], v[104:107]\n" \
  "ds_read_b128 a[40:43], v224 offset:10240\n" \
  "v_mfma_f32_16x16x32_bf16 v[108:111], a[156:159], a[92:95], v[108:111]\n" \
  "ds_read_b64_tr_b16 a[150:151], v226 offset:8992\n" \
  "s_add_u32 s8, s8, 128\n" \
  "s_addc_u32 s9, s9, 0\n" \
  "v_mfma_f32_16x16x32_bf16 v[108:111], a[152:155], a[88:91], v[108:111]\n" \
  "s_mov_b32 m0, 162560\n" \
  "v_mfma_f32_16x16x32_bf16 v[112:115], a[152:155], a[96:99], v[112:115]\n" \
  "buffer_load_dword v229, s[12:15], s85 offen lds\n" \
  "v_mfma_f32_16x16x32_bf16 v[112:115], a[156:159], a[100:103], v[112:115]\n" \
  "v_mfma_f32_16x16x32_bf16 v[116:119], a[156:159], a[108:111], v[116:119]\n" \
  "ds_read_b128 a[44:47], v225 offset:10240\n" \
  "global_load_lds_dword v[250:251], off\n" \
  "v_mfma_f32_16x16x32_bf16 v[116:119], a[152:155], a[104:107], v[116:119]\n" \
  "v_mfma_f32_16x16x32_bf16 v[120:123], a[152:155], a[112:115], v[120:123]\n" \
  "v_add_co_u32 v250, vcc, v228, v250\n" \
  "s_nop 1\n" \
  "v_addc_co_u32 v251, vcc, 0, v251, vcc\n" \
  "v_mfma_f32_16x16x32_bf16 v[120:123], a[156:159], a[116:119], v[120:123]\n" \
  "v_mfma_f32_16x16x32_bf16 v[124:127], a[156:159], a[124:127], v[124:127]\n" \
  "ds_read_b128 a[48:51], v224 offset:12288\n" \
  "s_add_u32 m0, s89, s98\n" \
  "v_mfma_f32_16x16x32_bf16 v[124:127], a[152:155], a[120:123], v[124:127]\n" \
  "buffer_load_dwordx4 v231, s[12:15], 0 offen lds\n" \
  "v_mfma_f32_16x16x32_bf16 v[128:131], a[160:163], a[64:67], v[128:131]\n" \
  "ds_read_b64_tr_b16 a[152:153], v226 offset:14384\n" \
  "v_mfma_f32_16x16x32_bf16 v[128:131], a[164:167], a[68:71], v[128:131]\n" \
  "v_mfma_f32_16x16x32_bf16 v[132:135], a[164:167], a[76:79], v[132:135]\n" \
  "ds_read_b128 a[52:55], v225 offset:12288\n" \
  "ds_read_b64_tr_b16 a[154:155], v226 offset:16176\n" \
  "v_mfma_f32_16x16x32_bf16 v[132:135], a[160:163], a[72:75], v[132:135]\n" \
  "s_add_u32 m0, m0, 1024\n" \
  "v_mfma_f32_16x16x32_bf16 v[136:139], a[160:163], a[80:83], v[136:139]\n" \
  "buffer_load_dwordx4 v232, s[12:15], 0 offen lds\n" \
  "ds_read_b64_tr_b16 a[156:157], v226 offset:21552\n" \
  "v_mfma_f32_16x16x32_bf16 v[136:139], a[164:167], a[84:87], v[136:139]\n" \
  "v_mfma_f32_16x16x32_bf16 v[140:143], a[164:167], a[92:95], v[140:143]\n" \
  "ds_read_b64_tr_b16 a[158:159], v226 offset:23344\n" \
  "v_mfma_f32_16x16x32_bf16 v[140:143], a[160:163], a[88:91], v[140:143]\n" \
  "ds_read_b128 a[56:59], v224 offset:14336\n" \
  "v_mfma_f32_16x16x32_bf16 v[144:147], a[160:163], a[96:99], v[144:147]\n" \
  "v_mfma_f32_16x16x32_bf16 v[144:147], a[164:167], a[100:103], v[144:147]\n" \
  "s_add_u32 m0, m0, 1024\n" \
  "v_mfma_f32_16x16x32_bf16 v[148:151], a[164:167], a[108:111], v[148:151]\n" \
  "buffer_load_dwordx4 v233, s[12:15], 0 offen lds\n" \
  "v_mfma_f32_16x16x32_bf16 v[148:151], a[160:163], a[104:107], v[148:151]\n" \
  "ds_read_b128 a[60:63], v225 offset:14336\n" \
  "v_mfma_f32_16x16x32_bf16 v[152:155], a[160:163], a[112:115], v[152:155]\n" \
  "v_mfma_f32_16x16x32_bf16 v[152:155], a[164:167], a[116:119], v[152:155]\n" \
  "v_mfma_f32_16x16x32_bf16 v[156:159], a[164:167], a[124:127], v[156:159]\n" \
  "v_mfma_f32_16x16x32_bf16 v[156:159], a[160:163], a[120:123], v[156:159]\n" \
  "s_add_u32 m0, m0, 1024\n" \
  "v_mfma_f32_16x16x32_bf16 v[160:163], a[168:171], a[64:67], v[160:163]\n" \
  "buffer_load_dwordx4 v234, s[12:15], 0 offen lds\n" \
  "ds_read_b64_tr_b16 a[160:161], v226 offset:64\n" \
  "v_mfma_f32_16x16x32_bf16 v[160:163], a[172:175], a[68:71], v[160:163]\n" \
  "v_mfma_f32_16x16x32_bf16 v[164:167], a[172:175], a[76:79], v[164:167]\n" \
  "ds_read_b64_tr_b16 a[162:163], v226 offset:1856\n" \
  "v_mfma_f32_16x16x32_bf16 v[164:167], a[168:171], a[72:75], v[164:167]\n" \
  "v_mfma_f32_16x16x32_bf16 v[168:171], a[168:171], a[80:83], v[168:171]\n" \
  "ds_read_b64_tr_b16 a[164:165], v226 offset:7232\n" \
  "s_add_u32 m0, m0, 1024\n" \
  "v_mfma_f32_16x16x32_bf16 v[168:171], a[172:175], a[84:87], v[168:171]\n" \
  "buffer_load_dwordx4 v235, s[12:15], 0 offen lds\n" \
  "v_mfma_f32_16x16x32_bf16 v[172:175], a[172:175], a[92:95], v[172:175]\n" \
  "ds_read_b64_tr_b16 a[166:167], v226 offset:9024\n" \
  "v_mfma_f32_16x16x32_bf16 v[172:175], a[168:171], a[88:91], v[172:175]\n" \
  "v_mfma_f32_16x16x32_bf16 v[176:179], a[168:171], a[96:99], v[176:179]\n" \
  "v_mfma_f32_16x16x32_bf16 v[176:179], a[172:175], a[100:103], v[176:179]\n" \
  "v_mfma_f32_16x16x32_bf16 v[180:183], a[172:175], a[108:111], v[180:183]\n" \
  "s_add_u32 m0, m0, 1024\n" \
  "v_mfma_f32_16x16x32_bf16 v[180:183], a[168:171], a[104:107], v[180:183]\n" \
  "buffer_load_dwordx4 v236, s[12:15], 0 offen lds\n" \
  "v_mfma_f32_16x16x32_bf16 v[184:187], a[168:171], a[112:115], v[184:187]\n" \
  "v_mfma_f32_16x16x32_bf16 v[184:187], a[172:175], a[116:119], v[184:187]\n" \
  "v_mfma_f32_16x16x32_bf16 v[188:191], a[172:175], a[124:127], v[188:191]\n" \
  "v_mfma_f32_16x16x32_bf16 v[188:191], a[168:171], a[120:123], v[188:191]\n" \
  "v_mfma_f32_16x16x32_bf16 v[192:195], a[176:179], a[64:67], v[192:195]\n" \
  "ds_read_b64_tr_b16 a[168:169], v226 offset:14416\n" \
  "s_add_u32 m0, m0, 1024\n" \
  "v_mfma_f32_16x16x32_bf16 v[192:195], a[180:183], a[68:71], v[192:195]\n" \
  "buffer_load_dwordx4 v237, s[12:15], 0 offen lds\n" \
  "v_mfma_f32_16x16x32_bf16 v[196:199], a[180:183], a[76:79], v[196:199]\n" \
  "ds_read_b64_tr_b16 a[170:171], v226 offset:16208\n" \
  "v_mfma_f32_16x16x32_bf16 v[196:199], a[176:179], a[72:75], v[196:199]\n" \
  "v_mfma_f32_16x16x32_bf16 v[200:203], a[176:179], a[80:83], v[200:203]\n" \
  "ds_read_b64_tr_b16 a[172:173], v226 offset:21584\n" \
  "v_mfma_f32_16x16x32_bf16 v[200:203], a[180:183], a[84:87], v[200:203]\n" \
  "v_mfma_f32_16x16x32_bf16 v[204:207], a[180:183], a[92:95], v[204:207]\n" \
  "ds_read_b64_tr_b16 a[174:175], v226 offset:23376\n" \
  "v_mfma_f32_16x16x32_bf16 v[204:207], a[176:179], a[88:91], v[204:207]\n" \
  "v_mfma_f32_16x16x32_bf16 v[208:211], a[176:179], a[96:99], v[208:211]\n" \
  "s_add_u32 s12, s12, 458752\n" \
  "s_addc_u32 s13, s13, 0\n" \
  "s_sub_u32 s14, s14, 458752\n" \
  "v_mfma_f32_16x16x32_bf16 v[208:211], a[180:183], a[100:103], v[208:211]\n" \
  "s_add_u32 s98, s98, 28928\n" \
  "s_cmp_eq_u32 s98, 86784\n" \
  "s_cselect_b32 s98, 0, s98\n" \
  "v_mfma_f32_16x16x32_bf16 v[212:215], a[180:183], a[108:111], v[212:215]\n" \
  "v_mfma_f32_16x16x32_bf16 v[212:215], a[176:179], a[104:107], v[212:215]\n" \
  "v_mfma_f32_16x16x32_bf16 v[216:219], a[176:179], a[112:115], v[216:219]\n" \
  "v_mfma_f32_16x16x32_bf16 v[216:219], a[180:183], a[116:119], v[216:219]\n" \
  "v_mfma_f32_16x16x32_bf16 v[220:223], a[180:183], a[124:127], v[220:223]\n" \
  "v_mfma_f32_16x16x32_bf16 v[220:223], a[176:179], a[120:123], v[220:223]\n" \
  "s_waitcnt lgkmcnt(0)\n" \
  "v_mfma_f32_16x16x32_bf16 v[0:3], a[128:131], a[0:3], v[0:3]\n" \
  "ds_read_b64_tr_b16 a[176:177], v227 offset:0\n" \
  "v_mfma_f32_16x16x32_bf16 v[0:3], a[132:135], a[4:7], v[0:3]\n" \
  "ds_read_b64_tr_b16 a[178:179], v227 offset:1792\n" \
  "v_mfma_f32_16x16x32_bf16 v[4:7], a[132:135], a[12:15], v[4:7]\n" \
  "ds_read_b64_tr_b16 a[180:181], v227 offset:7168\n" \
  "v_mfma_f32_16x16x32_bf16 v[4:7], a[128:131], a[8:11], v[4:7]\n" \
  "ds_read_b64_tr_b16 a[182:183], v227 offset:8960\n" \
  "v_mfma_f32_16x16x32_bf16 v[8:11], a[128:131], a[16:19], v[8:11]\n" \
  "v_mfma_f32_16x16x32_bf16 v[8:11], a[132:135], a[20:23], v[8:11]\n" \
  "v_mfma_f32_16x16x32_bf16 v[12:15], a[132:135], a[28:31], v[12:15]\n" \
  "s_add_u32 s97, s97, 28928\n" \
  "s_cmp_eq_u32 s97, 86784\n" \
  "s_cselect_b32 s99, s68, s69\n" \
  "s_cselect_b32 s97, 0, s97\n" \
  "v_mfma_f32_16x16x32_bf16 v[12:15], a[128:131], a[24:27], v[12:15]\n" \
  "v_mfma_f32_16x16x32_bf16 v[16:19], a[128:131], a[32:35], v[16:19]\n" \
  "v_add_u32 v226, s99, v226\n" \
  "v_add_u32 v227, s99, v227\n" \
  "v_mfma_f32_16x16x32_bf16 v[16:19], a[132:135], a[36:39], v[16:19]\n" \
  "s_waitcnt lgkmcnt(0)\n" \
  "s_waitcnt vmcnt(9)\n" \
  "s_barrier\n" \
  "v_mfma_f32_16x16x32_bf16 v[20:23], a[132:135], a[44:47], v[20:23]\n" \
  "ds_read_b128 a[64:67], v224 offset:32768\n" \
  "v_mfma_f32_16x16x32_bf16 v[20:23], a[128:131], a[40:43], v[20:23]\n" \
  "s_add_u32 m0, s88, 0\n" \
  "v_mfma_f32_16x16x32_bf16 v[24:27], a[128:131], a[48:51], v[24:27]\n" \
  "buffer_load_dwordx4 v230, s[8:11], 0 offen sc1 lds\n" \
  "v_mfma_f32_16x16x32_bf16 v[24:27], a[132:135], a[52:55], v[24:27]\n" \
  "v_mfma_f32_16x16x32_bf16 v[28:31], a[132:135], a[60:63], v[28:31]\n" \
  "ds_read_b128 a[68:71], v225 offset:32768\n" \
  "v_mfma_f32_16x16x32_bf16 v[28:31], a[128:131], a[56:59], v[28:31]\n" \
  "v_mfma_f32_16x16x32_bf16 v[32:35], a[136:139], a[0:3], v[32:35]\n" \
  "ds_read_b64_tr_b16 a[128:129], v226 offset:0\n" \
  "s_add_u32 m0, m0, 2048\n" \
  "v_mfma_f32_16x16x32_bf16 v[32:35], a[140:143], a[4:7], v[32:35]\n" \
  "buffer_load_dwordx4 v230, s[8:11], s72 offen sc1 lds\n" \
  "v_mfma_f32_16x16x32_bf16 v[36:39], a[140:143], a[12:15], v[36:39]\n" \
  "ds_read_b128 a[72:75], v224 offset:34816\n" \
  "ds_read_b64_tr_b16 a[130:131], v226 offset:1792\n" \
  "v_mfma_f32_16x16x32_bf16 v[36:39], a[136:139], a[8:11], v[36:39]\n" \
  "v_mfma_f32_16x16x32_bf16 v[40:43], a[136:139], a[16:19], v[40:43]\n" \
  "ds_read_b64_tr_b16 a[132:133], v226 offset:7168\n" \
  "v_mfma_f32_16x16x32_bf16 v[40:43], a[140:143], a[20:23], v[40:43]\n" \
  "s_add_u32 m0, m0, 2048\n" \
  "v_mfma_f32_16x16x32_bf16 v[44:47], a[140:143], a[28:31], v[44:47]\n" \
  "buffer_load_dwordx4 v230, s[8:11], s73 offen sc1 lds\n" \
  "ds_read_b64_tr_b16 a[134:135], v226 offset:8960\n" \
  "v_mfma_f32_16x16x32_bf16 v[44:47], a[136:139], a[24:27], v[44:47]\n" \
  "ds_read_b128 a[76:79], v225 offset:34816\n" \
  "v_mfma_f32_16x16x32_bf16 v[48:51], a[136:139], a[32:35], v[48:51]\n" \
  "v_mfma_f32_16x16x32_bf16 v[48:51], a[140:143], a[36:39], v[48:51]\n" \
  "v_mfma_f32_16x16x32_bf16 v[52:55], a[140:143], a[44:47], v[52:55]\n" \
  "s_add_u32 m0, m0, 2048\n" \
  "v_mfma_f32_16x16x32_bf16 v[52:55], a[136:139], a[40:43], v[52:55]\n" \
  "buffer_load_dwordx4 v230, s[8:11], s74 offen sc1 lds\n" \
  "ds_read_b128 a[80:83], v224 offset:36864\n" \
  "v_mfma_f32_16x16x32_bf16 v[56:59], a[136:139], a[48:51], v[56:59]\n" \
  "v_mfma_f32_16x16x32_bf16 v[56:59], a[140:143], a[52:55], v[56:59]\n" \
  "v_mfma_f32_16x16x32_bf16 v[60:63], a[140:143], a[60:63], v[60:63]\n" \
  "v_mfma_f32_16x16x32_bf16 v[60:63], a[136:139], a[56:59], v[60:63]\n" \
  "ds_read_b128 a[84:87], v225 offset:36864\n" \
  "s_add_u32 m0, m0, 2048\n" \
  "v_mfma_f32_16x16x32_bf16 v[64:67], a[144:147], a[0:3], v[64:67]\n" \
  "buffer_load_dwordx4 v230, s[8:11], s75 offen sc1 lds\n" \
  "ds_read_b64_tr_b16 a[136:137], v226 offset:14352\n" \
  "v_mfma_f32_16x16x32_bf16 v[64:67], a[148:151], a[4:7], v[64:67]\n" \
  "v_mfma_f32_16x16x32_bf16 v[68:71], a[148:151], a[12:15], v[68:71]\n" \
  "ds_read_b64_tr_b16 a[138:139], v226 offset:16144\n" \
  "v_mfma_f32_16x16x32_bf16 v[68:71], a[144:147], a[8:11], v[68:71]\n" \
  "v_mfma_f32_16x16x32_bf16 v[72:75], a[144:147], a[16:19], v[72:75]\n" \
  "ds_read_b128 a[88:91], v224 offset:38912\n" \
  "ds_read_b64_tr_b16 a[140:141], v226 offset:21520\n" \
  "s_add_u32 m0, m0, 2048\n" \
  "v_mfma_f32_16x16x32_bf16 v[72:75], a[148:151], a[20:23], v[72:75]\n" \
  "buffer_load_dwordx4 v230, s[8:11], s76 offen sc1 lds\n" \
  "v_mfma_f32_16x16x32_bf16 v[76:79], a[148:151], a[28:31], v[76:79]\n" \
  "ds_read_b64_tr_b16 a[142:143], v226 offset:23312\n" \
  "v_mfma_f32_16x16x32_bf16 v[76:79], a[144:147], a[24:27], v[76:79]\n" \
  "v_mfma_f32_16x16x32_bf16 v[80:83], a[144:147], a[32:35], v[80:83]\n" \
  "ds_read_b128 a[92:95], v225 offset:38912\n" \
  "v_mfma_f32_16x16x32_bf16 v[80:83], a[148:151], a[36:39], v[80:83]\n" \
  "s_add_u32 m0, m0, 2048\n" \
  "v_mfma_f32_16x16x32_bf16 v[84:87], a[148:151], a[44:47], v[84:87]\n" \
  "buffer_load_dwordx4 v230, s[8:11], s77 offen sc1 lds\n" \
  "v_mfma_f32_16x16x32_bf16 v[84:87], a[144:147], a[40:43], v[84:87]\n" \
  "v_mfma_f32_16x16x32_bf16 v[88:91], a[144:147], a[48:51], v[88:91]\n" \
  "v_mfma_f32_16x16x32_bf16 v[88:91], a[148:151], a[52:55], v[88:91]\n" \
  "ds_read_b128 a[96:99], v224 offset:40960\n" \
  "v_mfma_f32_16x16x32_bf16 v[92:95], a[148:151], a[60:63], v[92:95]\n" \
  "s_add_u32 m0, m0, 2048\n" \
  "v_mfma_f32_16x16x32_bf16 v[92:95], a[144:147], a[56:59], v[92:95]\n" \
  "buffer_load_dwordx4 v230, s[8:11], s78 offen sc1 lds\n" \
  "v_mfma_f32_16x16x32_bf16 v[96:99], a[152:155], a[0:3], v[96:99]\n" \
  "ds_read_b64_tr_b16 a[144:145], v226 offset:32\n" \
  "v_mfma_f32_16x16x32_bf16 v[96:99], a[156:159], a[4:7], v[96:99]\n" \
  "ds_read_b128 a[100:103], v225 offset:40960\n" \
  "v_mfma_f32_16x16x32_bf16 v[100:103], a[156:159], a[12:15], v[100:103]\n" \
  "ds_read_b64_tr_b16 a[146:147], v226 offset:1824\n" \
  "v_mfma_f32_16x16x32_bf16 v[100:103], a[152:155], a[8:11], v[100:103]\n" \
  "v_mfma_f32_16x16x32_bf16 v[104:107], a[152:155], a[16:19], v[104:107]\n" \
  "ds_read_b64_tr_b16 a[148:149], v226 offset:7200\n" \
  "v_mfma_f32_16x16x32_bf16 v[104:107], a[156:159], a[20:23], v[104:107]\n" \
  "ds_read_b128 a[104:107], v224 offset:43008\n" \
  "v_mfma_f32_16x16x32_bf16 v[108:111], a[156:159], a[28:31], v[108:111]\n" \
  "ds_read_b64_tr_b16 a[150:151], v226 offset:8992\n" \
  "s_add_u32 s8, s8, 128\n" \
  "s_addc_u32 s9, s9, 0\n" \
  "v_mfma_f32_16x16x32_bf16 v[108:111], a[152:155], a[24:27], v[108:111]\n" \
  "s_mov_b32 m0, 162560\n" \
  "v_mfma_f32_16x16x32_bf16 v[112:115], a[152:155], a[32:35], v[112:115]\n" \
  "buffer_load_dword v229, s[12:15], s85 offen lds\n" \
  "v_mfma_f32_16x16x32_bf16 v[112:115], a[156:159], a[36:39], v[112:115]\n" \
  "v_mfma_f32_16x16x32_bf16 v[116:119], a[156:159], a[44:47], v[116:119]\n" \
  "ds_read_b128 a[108:111], v225 offset:43008\n" \
  "global_load_lds_dword v[250:251], off\n" \
  "v_mfma_f32_16x16x32_bf16 v[116:119], a[152:155], a[40:43], v[116:119]\n" \
  "v_mfma_f32_16x16x32_bf16 v[120:123], a[152:155], a[48:51], v[120:123]\n" \
  "v_add_co_u32 v250, vcc, v228, v250\n" \
  "s_nop 1\n" \
  "v_addc_co_u32 v251, vcc, 0, v251, vcc\n" \
  "v_mfma_f32_16x16x32_bf16 v[120:123], a[156:159], a[52:55], v[120:123]\n" \
  "v_mfma_f32_16x16x32_bf16 v[124:127], a[156:159], a[60:63], v[124:127]\n" \
  "ds_read_b128 a[112:115], v224 offset:45056\n" \
  "s_add_u32 m0, s89, s98\n" \
  "v_mfma_f32_16x16x32_bf16 v[124:127], a[152:155], a[56:59], v[124:127]\n" \
  "buffer_load_dwordx4 v231, s[12:15], 0 offen lds\n" \
  "v_mfma_f32_16x16x32_bf16 v[128:131], a[160:163], a[0:3], v[128:131]\n" \
  "ds_read_b64_tr_b16 a[152:153], v226 offset:14384\n" \
  "v_mfma_f32_16x16x32_bf16 v[128:131], a[164:167], a[4:7], v[128:131]\n" \
  "v_mfma_f32_16x16x32_bf16 v[132:135], a[164:167], a[12:15], v[132:135]\n" \
  "ds_read_b128 a[116:119], v225 offset:45056\n" \
  "ds_read_b64_tr_b16 a[154:155], v226 offset:16176\n" \
  "v_mfma_f32_16x16x32_bf16 v[132:135], a[160:163], a[8:11], v[132:135]\n" \
  "s_add_u32 m0, m0, 1024\n" \
  "v_mfma_f32_16x16x32_bf16 v[136:139], a[160:163], a[16:19], v[136:139]\n" \
  "buffer_load_dwordx4 v232, s[12:15], 0 offen lds\n" \
  "ds_read_b64_tr_b16 a[156:157], v226 offset:21552\n" \
  "v_mfma_f32_16x16x32_bf16 v[136:139], a[164:167], a[20:23], v[136:139]\n" \
  "v_mfma_f32_16x16x32_bf16 v[140:143], a[164:167], a[28:31], v[140:143]\n" \
  "ds_read_b64_tr_b16 a[158:159], v226 offset:23344\n" \
  "v_mfma_f32_16x16x32_bf16 v[140:143], a[160:163], a[24:27], v[140:143]\n" \
  "ds_read_b128 a[120:123], v224 offset:47104\n" \
  "v_mfma_f32_16x16x32_bf16 v[144:147], a[160:163], a[32:35], v[144:147]\n" \
  "v_mfma_f32_16x16x32_bf16 v[144:147], a[164:167], a[36:39], v[144:147]\n" \
  "s_add_u32 m0, m0, 1024\n" \
  "v_mfma_f32_16x16x32_bf16 v[148:151], a[164:167], a[44:47], v[148:151]\n" \
  "buffer_load_dwordx4 v233, s[12:15], 0 offen lds\n" \
  "v_mfma_f32_16x16x32_bf16 v[148:151], a[160:163], a[40:43], v[148:151]\n" \
  "ds_read_b128 a[124:127], v225 offset:47104\n" \
  "v_mfma_f32_16x16x32_bf16 v[152:155], a[160:163], a[48:51], v[152:155]\n" \
  "v_mfma_f32_16x16x32_bf16 v[152:155], a[164:167], a[52:55], v[152:155]\n" \
  "v_mfma_f32_16x16x32_bf16 v[156:159], a[164:167], a[60:63], v[156:159]\n" \
  "v_mfma_f32_16x16x32_bf16 v[156:159], a[160:163], a[56:59], v[156:159]\n" \
  "s_add_u32 m0, m0, 1024\n" \
  "v_mfma_f32_16x16x32_bf16 v[160:163], a[168:171], a[0:3], v[160:163]\n" \
  "buffer_load_dwordx4 v234, s[12:15], 0 offen lds\n" \
  "ds_read_b64_tr_b16 a[160:161], v226 offset:64\n" \
  "v_mfma_f32_16x16x32_bf16 v[160:163], a[172:175], a[4:7], v[160:163]\n" \
  "v_mfma_f32_16x16x32_bf16 v[164:167], a[172:175], a[12:15], v[164:167]\n" \
  "ds_read_b64_tr_b16 a[162:163], v226 offset:1856\n" \
  "v_mfma_f32_16x16x32_bf16 v[164:167], a[168:171], a[8:11], v[164:167]\n" \
  "v_mfma_f32_16x16x32_bf16 v[168:171], a[168:171], a[16:19], v[168:171]\n" \
  "ds_read_b64_tr_b16 a[164:165], v226 offset:7232\n" \
  "s_add_u32 m0, m0, 1024\n" \
  "v_mfma_f32_16x16x32_bf16 v[168:171], a[172:175], a[20:23], v[168:171]\n" \
  "buffer_load_dwordx4 v235, s[12:15], 0 offen lds\n" \
  "v_mfma_f32_16x16x32_bf16 v[172:175], a[172:175], a[28:31], v[172:175]\n" \
  "ds_read_b64_tr_b16 a[166:167], v226 offset:9024\n" \
  "v_mfma_f32_16x16x32_bf16 v[172:175], a[168:171], a[24:27], v[172:175]\n" \
  "v_mfma_f32_16x16x32_bf16 v[176:179], a[168:171], a[32:35], v[176:179]\n" \
  "v_mfma_f32_16x16x32_bf16 v[176:179], a[172:175], a[36:39], v[176:179]\n" \
  "v_mfma_f32_16x16x32_bf16 v[180:183], a[172:175], a[44:47], v[180:183]\n" \
  "s_add_u32 m0, m0, 1024\n" \
  "v_mfma_f32_16x16x32_bf16 v[180:183], a[168:171], a[40:43], v[180:183]\n" \
  "buffer_load_dwordx4 v236, s[12:15], 0 offen lds\n" \
  "v_mfma_f32_16x16x32_bf16 v[184:187], a[168:171], a[48:51], v[184:187]\n" \
  "v_mfma_f32_16x16x32_bf16 v[184:187], a[172:175], a[52:55], v[184:187]\n" \
  "v_mfma_f32_16x16x32_bf16 v[188:191], a[172:175], a[60:63], v[188:191]\n" \
  "v_mfma_f32_16x16x32_bf16 v[188:191], a[168:171], a[56:59], v[188:191]\n" \
  "v_mfma_f32_16x16x32_bf16 v[192:195], a[176:179], a[0:3], v[192:195]\n" \
  "ds_read_b64_tr_b16 a[168:169], v226 offset:14416\n" \
  "s_add_u32 m0, m0, 1024\n" \
  "v_mfma_f32_16x16x32_bf16 v[192:195], a[180:183], a[4:7], v[192:195]\n" \
  "buffer_load_dwordx4 v237, s[12:15], 0 offen lds\n" \
  "v_mfma_f32_16x16x32_bf16 v[196:199], a[180:183], a[12:15], v[196:199]\n" \
  "ds_read_b64_tr_b16 a[170:171], v226 offset:16208\n" \
  "v_mfma_f32_16x16x32_bf16 v[196:199], a[176:179], a[8:11], v[196:199]\n" \
  "v_mfma_f32_16x16x32_bf16 v[200:203], a[176:179], a[16:19], v[200:203]\n" \
  "ds_read_b64_tr_b16 a[172:173], v226 offset:21584\n" \
  "v_mfma_f32_16x16x32_bf16 v[200:203], a[180:183], a[20:23], v[200:203]\n" \
  "v_mfma_f32_16x16x32_bf16 v[204:207], a[180:183], a[28:31], v[204:207]\n" \
  "ds_read_b64_tr_b16 a[174:175], v226 offset:23376\n" \
  "v_mfma_f32_16x16x32_bf16 v[204:207], a[176:179], a[24:27], v[204:207]\n" \
  "v_mfma_f32_16x16x32_bf16 v[208:211], a[176:179], a[32:35], v[208:211]\n" \
  "s_add_u32 s12, s12, 458752\n" \
  "s_addc_u32 s13, s13, 0\n" \
  "s_sub_u32 s14, s14, 458752\n" \
  "v_mfma_f32_16x16x32_bf16 v[208:211], a[180:183], a[36:39], v[208:211]\n" \
  "s_add_u32 s98, s98, 28928\n" \
  "s_cmp_eq_u32 s98, 86784\n" \
  "s_cselect_b32 s98, 0, s98\n" \
  "v_mfma_f32_16x16x32_bf16 v[212:215], a[180:183], a[44:47], v[212:215]\n" \
  "v_mfma_f32_16x16x32_bf16 v[212:215], a[176:179], a[40:43], v[212:215]\n" \
  "v_mfma_f32_16x16x32_bf16 v[216:219], a[176:179], a[48:51], v[216:219]\n" \
  "v_mfma_f32_16x16x32_bf16 v[216:219], a[180:183], a[52:55], v[216:219]\n" \
  "v_mfma_f32_16x16x32_bf16 v[220:223], a[180:183], a[60:63], v[220:223]\n" \
  "v_mfma_f32_16x16x32_bf16 v[220:223], a[176:179], a[56:59], v[220:223]\n" \
  "s_sub_u32 s96, s96, 1\n" \
  "s_cmp_lg_u32 s96, 0\n" \
  "s_cbranch_scc1 LloopA_%=\n" \
  "s_waitcnt lgkmcnt(0)\n" \
  "v_mfma_f32_16x16x32_bf16 v[0:3], a[128:131], a[64:67], v[0:3]\n" \
  "ds_read_b64_tr_b16 a[176:177], v227 offset:0\n" \
  "v_mfma_f32_16x16x32_bf16 v[0:3], a[132:135], a[68:71], v[0:3]\n" \
  "ds_read_b64_tr_b16 a[178:179], v227 offset:1792\n" \
  "v_mfma_f32_16x16x32_bf16 v[4:7], a[132:135], a[76:79], v[4:7]\n" \
  "ds_read_b64_tr_b16 a[180:181], v227 offset:7168\n" \
  "v_mfma_f32_16x16x32_bf16 v[4:7], a[128:131], a[72:75], v[4:7]\n" \
  "ds_read_b64_tr_b16 a[182:183], v227 offset:8960\n" \
  "v_mfma_f32_16x16x32_bf16 v[8:11], a[128:131], a[80:83], v[8:11]\n" \
  "v_mfma_f32_16x16x32_bf16 v[8:11], a[132:135], a[84:87], v[8:11]\n" \
  "v_mfma_f32_16x16x32_bf16 v[12:15], a[132:135], a[92:95], v[12:15]\n" \
  "s_add_u32 s97, s97, 28928\n" \
  "s_cmp_eq_u32 s97, 86784\n" \
  "s_cselect_b32 s99, s68, s69\n" \
  "s_cselect_b32 s97, 0, s97\n" \
  "v_mfma_f32_16x16x32_bf16 v[12:15], a[128:131], a[88:91], v[12:15]\n" \
  "v_mfma_f32_16x16x32_bf16 v[16:19], a[128:131], a[96:99], v[16:19]\n" \
  "v_add_u32 v226, s99, v226\n" \
  "v_add_u32 v227, s99, v227\n" \
  "v_mfma_f32_16x16x32_bf16 v[16:19], a[132:135], a[100:103], v[16:19]\n" \
  "s_waitcnt lgkmcnt(0)\n" \
  "s_waitcnt vmcnt(9)\n" \
  "s_barrier\n" \
  "v_mfma_f32_16x16x32_bf16 v[20:23], a[132:135], a[108:111], v[20:23]\n" \
  "ds_read_b128 a[0:3], v224 offset:0\n" \
  "v_mfma_f32_16x16x32_bf16 v[20:23], a[128:131], a[104:107], v[20:23]\n" \
  "s_add_u32 m0, s88, 32768\n" \
  "v_mfma_f32_16x16x32_bf16 v[24:27], a[128:131], a[112:115], v[24:27]\n" \
  "buffer_load_dwordx4 v230, s[8:11], 0 offen sc1 lds\n" \
  "v_mfma_f32_16x16x32_bf16 v[24:27], a[132:135], a[116:119], v[24:27]\n" \
  "v_mfma_f32_16x16x32_bf16 v[28:31], a[132:135], a[124:127], v[28:31]\n" \
  "ds_read_b128 a[4:7], v225 offset:0\n" \
  "v_mfma_f32_16x16x32_bf16 v[28:31], a[128:131], a[120:123], v[28:31]\n" \
  "v_mfma_f32_16x16x32_bf16 v[32:35], a[136:139], a[64:67], v[32:35]\n" \
  "ds_read_b64_tr_b16 a[128:129], v226 offset:0\n" \
  "s_add_u32 m0, m0, 2048\n" \
  "v_mfma_f32_16x16x32_bf16 v[32:35], a[140:143], a[68:71], v[32:35]\n" \
  "buffer_load_dwordx4 v230, s[8:11], s72 offen sc1 lds\n" \
  "v_mfma_f32_16x16x32_bf16 v[36:39], a[140:143], a[76:79], v[36:39]\n" \
  "ds_read_b128 a[8:11], v224 offset:2048\n" \
  "ds_read_b64_tr_b16 a[130:131], v226 offset:1792\n" \
  "v_mfma_f32_16x16x32_bf16 v[36:39], a[136:139], a[72:75], v[36:39]\n" \
  "v_mfma_f32_16x16x32_bf16 v[40:43], a[136:139], a[80:83], v[40:43]\n" \
  "ds_read_b64_tr_b16 a[132:133], v226 offset:7168\n" \
  "v_mfma_f32_16x16x32_bf16 v[40:43], a[140:143], a[84:87], v[40:43]\n" \
  "s_add_u32 m0, m0, 2048\n" \
  "v_mfma_f32_16x16x32_bf16 v[44:47], a[140:143], a[92:95], v[44:47]\n" \
  "buffer_load_dwordx4 v230, s[8:11], s73 offen sc1 lds\n" \
  "ds_read_b64_tr_b16 a[134:135], v226 offset:8960\n" \
  "v_mfma_f32_16x16x32_bf16 v[44:47], a[136:139], a[88:91], v[44:47]\n" \
  "ds_read_b128 a[12:15], v225 offset:2048\n" \
  "v_mfma_f32_16x16x32_bf16 v[48:51], a[136:139], a[96:99], v[48:51]\n" \
  "v_mfma_f32_16x16x32_bf16 v[48:51], a[140:143], a[100:103], v[48:51]\n" \
  "v_mfma_f32_16x16x32_bf16 v[52:55], a[140:143], a[108:111], v[52:55]\n" \
  "s_add_u32 m0, m0, 2048\n" \
  "v_mfma_f32_16x16x32_bf16 v[52:55], a[136:139], a[104:107], v[52:55]\n" \
  "buffer_load_dwordx4 v230, s[8:11], s74 offen sc1 lds\n" \
  "ds_read_b128 a[16:19], v224 offset:4096\n" \
  "v_mfma_f32_16x16x32_bf16 v[56:59], a[136:139], a[112:115], v[56:59]\n" \
  "v_mfma_f32_16x16x32_bf16 v[56:59], a[140:143], a[116:119], v[56:59]\n" \
  "v_mfma_f32_16x16x32_bf16 v[60:63], a[140:143], a[124:127], v[60:63]\n" \
  "v_mfma_f32_16x16x32_bf16 v[60:63], a[136:139], a[120:123], v[60:63]\n" \
  "ds_read_b128 a[20:23], v225 offset:4096\n" \
  "s_add_u32 m0, m0, 2048\n" \
  "v_mfma_f32_16x16x32_bf16 v[64:67], a[144:147], a[64:67], v[64:67]\n" \
  "buffer_load_dwordx4 v230, s[8:11], s75 offen sc1 lds\n" \
  "ds_read_b64_tr_b16 a[136:137], v226 offset:14352\n" \
  "v_mfma_f32_16x16x32_bf16 v[64:67], a[148:151], a[68:71], v[64:67]\n" \
  "v_mfma_f32_16x16x32_bf16 v[68:71], a[148:151], a[76:79], v[68:71]\n" \
  "ds_read_b64_tr_b16 a[138:139], v226 offset:16144\n" \
  "v_mfma_f32_16x16x32_bf16 v[68:71], a[144:147], a[72:75], v[68:71]\n" \
  "v_mfma_f32_16x16x32_bf16 v[72:75], a[144:147], a[80:83], v[72:75]\n" \
  "ds_read_b128 a[24:27], v224 offset:6144\n" \
  "ds_read_b64_tr_b16 a[140:141], v226 offset:21520\n" \
  "s_add_u32 m0, m0, 2048\n" \
  "v_mfma_f32_16x16x32_bf16 v[72:75], a[148:151], a[84:87], v[72:75]\n" \
  "buffer_load_dwordx4 v230, s[8:11], s76 offen sc1 lds\n" \
  "v_mfma_f32_16x16x32_bf16 v[76:79], a[148:151], a[92:95], v[76:79]\n" \
  "ds_read_b64_tr_b16 a[142:143], v226 offset:23312\n" \
  "v_mfma_f32_16x16x32_bf16 v[76:79], a[144:147], a[88:91], v[76:79]\n" \
  "v_mfma_f32_16x16x32_bf16 v[80:83], a[144:147], a[96:99], v[80:83]\n" \
  "ds_read_b128 a[28:31], v225 offset:6144\n" \
  "v_mfma_f32_16x16x32_bf16 v[80:83], a[148:151], a[100:103], v[80:83]\n" \
  "s_add_u32 m0, m0, 2048\n" \
  "v_mfma_f32_16x16x32_bf16 v[84:87], a[148:151], a[108:111], v[84:87]\n" \
  "buffer_load_dwordx4 v230, s[8:11], s77 offen sc1 lds\n" \
  "v_mfma_f32_16x16x32_bf16 v[84:87], a[144:147], a[104:107], v[84:87]\n" \
  "v_mfma_f32_16x16x32_bf16 v[88:91], a[144:147], a[112:115], v[88:91]\n" \
  "v_mfma_f32_16x16x32_bf16 v[88:91], a[148:151], a[116:119], v[88:91]\n" \
  "ds_read_b128 a[32:35], v224 offset:8192\n" \
  "v_mfma_f32_16x16x32_bf16 v[92:95], a[148:151], a[124:127], v[92:95]\n" \
  "s_add_u32 m0, m0, 2048\n" \
  "v_mfma_f32_16x16x32_bf16 v[92:95], a[144:147], a[120:123], v[92:95]\n" \
  "buffer_load_dwordx4 v230, s[8:11], s78 offen sc1 lds\n" \
  "v_mfma_f32_16x16x32_bf16 v[96:99], a[152:155], a[64:67], v[96:99]\n" \
  "ds_read_b64_tr_b16 a[144:145], v226 offset:32\n" \
  "v_mfma_f32_16x16x32_bf16 v[96:99], a[156:159], a[68:71], v[96:99]\n" \
  "ds_read_b128 a[36:39], v225 offset:8192\n" \
  "v_mfma_f32_16x16x32_bf16 v[100:103], a[156:159], a[76:79], v[100:103]\n" \
  "ds_read_b64_tr_b16 a[146:147], v226 offset:1824\n" \
  "v_mfma_f32_16x16x32_bf16 v[100:103], a[152:155], a[72:75], v[100:103]\n" \
  "v_mfma_f32_16x16x32_bf16 v[104:107], a[152:155], a[80:83], v[104:107]\n" \
  "ds_read_b64_tr_b16 a[148:149], v226 offset:7200\n" \
  "v_mfma_f32_16x16x32_bf16 v[104:107], a[156:159], a[84:87], v[104:107]\n" \
  "ds_read_b128 a[40:43], v224 offset:10240\n" \
  "v_mfma_f32_16x16x32_bf16 v[108:111], a[156:159], a[92:95], v[108:111]\n" \
  "ds_read_b64_tr_b16 a[150:151], v226 offset:8992\n" \
  "s_add_u32 s8, s8, 128\n" \
  "s_addc_u32 s9, s9, 0\n" \
  "v_mfma_f32_16x16x32_bf16 v[108:111], a[152:155], a[88:91], v[108:111]\n" \
  "v_mfma_f32_16x16x32_bf16 v[112:115], a[152:155], a[96:99], v[112:115]\n" \
  "v_mfma_f32_16x16x32_bf16 v[112:115], a[156:159], a[100:103], v[112:115]\n" \
  "v_mfma_f32_16x16x32_bf16 v[116:119], a[156:159], a[108:111], v[116:119]\n" \
  "ds_read_b128 a[44:47], v225 offset:10240\n" \
  "v_mfma_f32_16x16x32_bf16 v[116:119], a[152:155], a[104:107], v[116:119]\n" \
  "v_mfma_f32_16x16x32_bf16 v[120:123], a[152:155], a[112:115], v[120:123]\n" \
  "v_accvgpr_read_b32 v252, a213\n" \
  "s_mov_b32 m0, 162560\n" \
  "s_nop 0\n" \
  "global_load_lds_dword v252, s[62:63]\n" \
  "v_mfma_f32_16x16x32_bf16 v[120:123], a[156:159], a[116:119], v[120:123]\n" \
  "v_mfma_f32_16x16x32_bf16 v[124:127], a[156:159], a[124:127], v[124:127]\n" \
  "ds_read_b128 a[48:51], v224 offset:12288\n" \
  "v_mfma_f32_16x16x32_bf16 v[124:127], a[152:155], a[120:123], v[124:127]\n" \
  "v_mfma_f32_16x16x32_bf16 v[128:131], a[160:163], a[64:67], v[128:131]\n" \
  "ds_read_b64_tr_b16 a[152:153], v226 offset:14384\n" \
  "v_accvgpr_read_b32 v250, a205\n" \
  "v_accvgpr_read_b32 v251, a206\n" \
  "v_mfma_f32_16x16x32_bf16 v[128:131], a[164:167], a[68:71], v[128:131]\n" \
  "v_mfma_f32_16x16x32_bf16 v[132:135], a[164:167], a[76:79], v[132:135]\n" \
  "ds_read_b128 a[52:55], v225 offset:12288\n" \
  "ds_read_b64_tr_b16 a[154:155], v226 offset:16176\n" \
  "v_accvgpr_read_b32 v236, a207\n" \
  "v_accvgpr_read_b32 v237, a211\n" \
  "v_mfma_f32_16x16x32_bf16 v[132:135], a[160:163], a[72:75], v[132:135]\n" \
  "v_mfma_f32_16x16x32_bf16 v[136:139], a[160:163], a[80:83], v[136:139]\n" \
  "ds_read_b64_tr_b16 a[156:157], v226 offset:21552\n" \
  "s_mov_b32 s85, 114688\n" \
  "v_mfma_f32_16x16x32_bf16 v[136:139], a[164:167], a[84:87], v[136:139]\n" \
  "v_mfma_f32_16x16x32_bf16 v[140:143], a[164:167], a[92:95], v[140:143]\n" \
  "ds_read_b64_tr_b16 a[158:159], v226 offset:23344\n" \
  "v_mfma_f32_16x16x32_bf16 v[140:143], a[160:163], a[88:91], v[140:143]\n" \
  "ds_read_b128 a[56:59], v224 offset:14336\n" \
  "v_mfma_f32_16x16x32_bf16 v[144:147], a[160:163], a[96:99], v[144:147]\n" \
  "v_mfma_f32_16x16x32_bf16 v[144:147], a[164:167], a[100:103], v[144:147]\n" \
  "v_mfma_f32_16x16x32_bf16 v[148:151], a[164:167], a[108:111], v[148:151]\n" \
  "v_mfma_f32_16x16x32_bf16 v[148:151], a[160:163], a[104:107], v[148:151]\n" \
  "ds_read_b128 a[60:63], v225 offset:14336\n" \
  "v_mfma_f32_16x16x32_bf16 v[152:155], a[160:163], a[112:115], v[152:155]\n" \
  "v_mfma_f32_16x16x32_bf16 v[152:155], a[164:167], a[116:119], v[152:155]\n" \
  "v_mfma_f32_16x16x32_bf16 v[156:159], a[164:167], a[124:127], v[156:159]\n" \
  "v_mfma_f32_16x16x32_bf16 v[156:159], a[160:163], a[120:123], v[156:159]\n" \
  "v_mfma_f32_16x16x32_bf16 v[160:163], a[168:171], a[64:67], v[160:163]\n" \
  "ds_read_b64_tr_b16 a[160:161], v226 offset:64\n" \
  "v_mfma_f32_16x16x32_bf16 v[160:163], a[172:175], a[68:71], v[160:163]\n" \
  "v_mfma_f32_16x16x32_bf16 v[164:167], a[172:175], a[76:79], v[164:167]\n" \
  "ds_read_b64_tr_b16 a[162:163], v226 offset:1856\n" \
  "v_mfma_f32_16x16x32_bf16 v[164:167], a[168:171], a[72:75], v[164:167]\n" \
  "v_mfma_f32_16x16x32_bf16 v[168:171], a[168:171], a[80:83], v[168:171]\n" \
  "ds_read_b64_tr_b16 a[164:165], v226 offset:7232\n" \
  "v_mfma_f32_16x16x32_bf16 v[168:171], a[172:175], a[84:87], v[168:171]\n" \
  "v_mfma_f32_16x16x32_bf16 v[172:175], a[172:175], a[92:95], v[172:175]\n" \
  "ds_read_b64_tr_b16 a[166:167], v226 offset:9024\n" \
  "v_mfma_f32_16x16x32_bf16 v[172:175], a[168:171], a[88:91], v[172:175]\n" \
  "v_mfma_f32_16x16x32_bf16 v[176:179], a[168:171], a[96:99], v[176:179]\n" \
  "v_mfma_f32_16x16x32_bf16 v[176:179], a[172:175], a[100:103], v[176:179]\n" \
  "v_mfma_f32_16x16x32_bf16 v[180:183], a[172:175], a[108:111], v[180:183]\n" \
  "v_mfma_f32_16x16x32_bf16 v[180:183], a[168:171], a[104:107], v[180:183]\n" \
  "v_mfma_f32_16x16x32_bf16 v[184:187], a[168:171], a[112:115], v[184:187]\n" \
  "v_mfma_f32_16x16x32_bf16 v[184:187], a[172:175], a[116:119], v[184:187]\n" \
  "v_mfma_f32_16x16x32_bf16 v[188:191], a[172:175], a[124:127], v[188:191]\n" \
  "v_mfma_f32_16x16x32_bf16 v[188:191], a[168:171], a[120:123], v[188:191]\n" \
  "v_mfma_f32_16x16x32_bf16 v[192:195], a[176:179], a[64:67], v[192:195]\n" \
  "ds_read_b64_tr_b16 a[168:169], v226 offset:14416\n" \
  "v_mfma_f32_16x16x32_bf16 v[192:195], a[180:183], a[68:71], v[192:195]\n" \
  "v_mfma_f32_16x16x32_bf16 v[196:199], a[180:183], a[76:79], v[196:199]\n" \
  "ds_read_b64_tr_b16 a[170:171], v226 offset:16208\n" \
  "v_mfma_f32_16x16x32_bf16 v[196:199], a[176:179], a[72:75], v[196:199]\n" \
  "v_mfma_f32_16x16x32_bf16 v[200:203], a[176:179], a[80:83], v[200:203]\n" \
  "ds_read_b64_tr_b16 a[172:173], v226 offset:21584\n" \
  "v_accvgpr_read_b32 v253, a212\n" \
  "buffer_load_dwordx2 v[252:253], v253, s[52:55], s71 offen sc1\n" \
  "v_mfma_f32_16x16x32_bf16 v[200:203], a[180:183], a[84:87], v[200:203]\n" \
  "v_mfma_f32_16x16x32_bf16 v[204:207], a[180:183], a[92:95], v[204:207]\n" \
  "ds_read_b64_tr_b16 a[174:175], v226 offset:23376\n" \
  "v_mfma_f32_16x16x32_bf16 v[204:207], a[176:179], a[88:91], v[204:207]\n" \
  "v_mfma_f32_16x16x32_bf16 v[208:211], a[176:179], a[96:99], v[208:211]\n" \
  "v_mfma_f32_16x16x32_bf16 v[208:211], a[180:183], a[100:103], v[208:211]\n" \
  "v_mfma_f32_16x16x32_bf16 v[212:215], a[180:183], a[108:111], v[212:215]\n" \
  "v_mfma_f32_16x16x32_bf16 v[212:215], a[176:179], a[104:107], v[212:215]\n" \
  "v_mfma_f32_16x16x32_bf16 v[216:219], a[176:179], a[112:115], v[216:219]\n" \
  "v_mfma_f32_16x16x32_bf16 v[216:219], a[180:183], a[116:119], v[216:219]\n" \
  "v_mfma_f32_16x16x32_bf16 v[220:223], a[180:183], a[124:127], v[220:223]\n" \
  "v_mfma_f32_16x16x32_bf16 v[220:223], a[176:179], a[120:123], v[220:223]\n" \
  "s_waitcnt lgkmcnt(0)\n" \
  "v_mfma_f32_16x16x32_bf16 v[0:3], a[128:131], a[0:3], v[0:3]\n" \
  "ds_read_b64_tr_b16 a[176:177], v227 offset:0\n" \
  "v_mfma_f32_16x16x32_bf16 v[0:3], a[132:135], a[4:7], v[0:3]\n" \
  "ds_read_b64_tr_b16 a[178:179], v227 offset:1792\n" \
  "v_mfma_f32_16x16x32_bf16 v[4:7], a[132:135], a[12:15], v[4:7]\n" \
  "ds_read_b64_tr_b16 a[180:181], v227 offset:7168\n" \
  "v_mfma_f32_16x16x32_bf16 v[4:7], a[128:131], a[8:11], v[4:7]\n" \
  "ds_read_b64_tr_b16 a[182:183], v227 offset:8960\n" \
  "v_mfma_f32_16x16x32_bf16 v[8:11], a[128:131], a[16:19], v[8:11]\n" \
  "v_mfma_f32_16x16x32_bf16 v[8:11], a[132:135], a[20:23], v[8:11]\n" \
  "v_mfma_f32_16x16x32_bf16 v[12:15], a[132:135], a[28:31], v[12:15]\n" \
  "s_add_u32 s97, s97, 28928\n" \
  "s_cmp_eq_u32 s97, 86784\n" \
  "s_cselect_b32 s99, s68, s69\n" \
  "s_cselect_b32 s97, 0, s97\n" \
  "v_mfma_f32_16x16x32_bf16 v[12:15], a[128:131], a[24:27], v[12:15]\n" \
  "v_mfma_f32_16x16x32_bf16 v[16:19], a[128:131], a[32:35], v[16:19]\n" \
  "v_add_u32 v226, s99, v226\n" \
  "v_add_u32 v227, s99, v227\n" \
  "v_mfma_f32_16x16x32_bf16 v[16:19], a[132:135], a[36:39], v[16:19]\n" \
  "s_waitcnt lgkmcnt(0)\n" \
  "s_waitcnt vmcnt(2)\n" \
  "s_barrier\n" \
  "s_waitcnt vmcnt(0)\n" \
  "v_xor_b32 v252, v252, v254\n" \
  "v_xor_b32 v253, v253, v255\n" \
  "v_or_b32 v252, v252, v253\n" \
  "v_cmp_ne_u32 vcc, 0, v252\n" \
  "s_nop 4\n" \
  "s_cbranch_vccnz Lslow1_%=\n" \
  "Lres1_%=:\n" \
  "v_mfma_f32_16x16x32_bf16 v[20:23], a[132:135], a[44:47], v[20:23]\n" \
  "ds_read_b128 a[64:67], v224 offset:32768\n" \
  "v_mfma_f32_16x16x32_bf16 v[20:23], a[128:131], a[40:43], v[20:23]\n" \
  "s_add_u32 m0, s90, 0\n" \
  "v_mfma_f32_16x16x32_bf16 v[24:27], a[128:131], a[48:51], v[24:27]\n" \
  "buffer_load_dwordx4 v248, s[16:19], 0 offen sc1 lds\n" \
  "v_mfma_f32_16x16x32_bf16 v[24:27], a[132:135], a[52:55], v[24:27]\n" \
  "v_mfma_f32_16x16x32_bf16 v[28:31], a[132:135], a[60:63], v[28:31]\n" \
  "ds_read_b128 a[68:71], v225 offset:32768\n" \
  "v_mfma_f32_16x16x32_bf16 v[28:31], a[128:131], a[56:59], v[28:31]\n" \
  "v_mfma_f32_16x16x32_bf16 v[32:35], a[136:139], a[0:3], v[32:35]\n" \
  "ds_read_b64_tr_b16 a[128:129], v226 offset:0\n" \
  "s_add_u32 m0, m0, 2048\n" \
  "v_mfma_f32_16x16x32_bf16 v[32:35], a[140:143], a[4:7], v[32:35]\n" \
  "buffer_load_dwordx4 v248, s[16:19], s79 offen sc1 lds\n" \
  "v_mfma_f32_16x16x32_bf16 v[36:39], a[140:143], a[12:15], v[36:39]\n" \
  "ds_read_b128 a[72:75], v224 offset:34816\n" \
  "ds_read_b64_tr_b16 a[130:131], v226 offset:1792\n" \
  "v_mfma_f32_16x16x32_bf16 v[36:39], a[136:139], a[8:11], v[36:39]\n" \
  "v_mfma_f32_16x16x32_bf16 v[40:43], a[136:139], a[16:19], v[40:43]\n" \
  "ds_read_b64_tr_b16 a[132:133], v226 offset:7168\n" \
  "v_mfma_f32_16x16x32_bf16 v[40:43], a[140:143], a[20:23], v[40:43]\n" \
  "s_add_u32 m0, m0, 2048\n" \
  "v_mfma_f32_16x16x32_bf16 v[44:47], a[140:143], a[28:31], v[44:47]\n" \
  "buffer_load_dwordx4 v248, s[16:19], s80 offen sc1 lds\n" \
  "ds_read_b64_tr_b16 a[134:135], v226 offset:8960\n" \
  "v_mfma_f32_16x16x32_bf16 v[44:47], a[136:139], a[24:27], v[44:47]\n" \
  "ds_read_b128 a[76:79], v225 offset:34816\n" \
  "v_mfma_f32_16x16x32_bf16 v[48:51], a[136:139], a[32:35], v[48:51]\n" \
  "v_mfma_f32_16x16x32_bf16 v[48:51], a[140:143], a[36:39], v[48:51]\n" \
  "v_mfma_f32_16x16x32_bf16 v[52:55], a[140:143], a[44:47], v[52:55]\n" \
  "s_add_u32 m0, m0, 2048\n" \
  "v_mfma_f32_16x16x32_bf16 v[52:55], a[136:139], a[40:43], v[52:55]\n" \
  "buffer_load_dwordx4 v248, s[16:19], s81 offen sc1 lds\n" \
  "ds_read_b128 a[80:83], v224 offset:36864\n" \
  "v_mfma_f32_16x16x32_bf16 v[56:59], a[136:139], a[48:51], v[56:59]\n" \
  "v_mfma_f32_16x16x32_bf16 v[56:59], a[140:143], a[52:55], v[56:59]\n" \
  "v_mfma_f32_16x16x32_bf16 v[60:63], a[140:143], a[60:63], v[60:63]\n" \
  "v_mfma_f32_16x16x32_bf16 v[60:63], a[136:139], a[56:59], v[60:63]\n" \
  "ds_read_b128 a[84:87], v225 offset:36864\n" \
  "s_add_u32 m0, s91, 0\n" \
  "v_mfma_f32_16x16x32_bf16 v[64:67], a[144:147], a[0:3], v[64:67]\n" \
  "buffer_load_dwordx4 v249, s[20:23], 0 offen lds\n" \
  "ds_read_b64_tr_b16 a[136:137], v226 offset:14352\n" \
  "v_mfma_f32_16x16x32_bf16 v[64:67], a[148:151], a[4:7], v[64:67]\n" \
  "v_mfma_f32_16x16x32_bf16 v[68:71], a[148:151], a[12:15], v[68:71]\n" \
  "ds_read_b64_tr_b16 a[138:139], v226 offset:16144\n" \
  "v_mfma_f32_16x16x32_bf16 v[68:71], a[144:147], a[8:11], v[68:71]\n" \
  "v_mfma_f32_16x16x32_bf16 v[72:75], a[144:147], a[16:19], v[72:75]\n" \
  "ds_read_b128 a[88:91], v224 offset:38912\n" \
  "ds_read_b64_tr_b16 a[140:141], v226 offset:21520\n" \
  "s_add_u32 m0, m0, s82\n" \
  "v_mfma_f32_16x16x32_bf16 v[72:75], a[148:151], a[20:23], v[72:75]\n" \
  "buffer_load_dwordx4 v249, s[20:23], s72 offen lds\n" \
  "v_mfma_f32_16x16x32_bf16 v[76:79], a[148:151], a[28:31], v[76:79]\n" \
  "ds_read_b64_tr_b16 a[142:143], v226 offset:23312\n" \
  "v_mfma_f32_16x16x32_bf16 v[76:79], a[144:147], a[24:27], v[76:79]\n" \
  "v_mfma_f32_16x16x32_bf16 v[80:83], a[144:147], a[32:35], v[80:83]\n" \
  "ds_read_b128 a[92:95], v225 offset:38912\n" \
  "v_mfma_f32_16x16x32_bf16 v[80:83], a[148:151], a[36:39], v[80:83]\n" \
  "s_add_u32 m0, m0, s82\n" \
  "v_mfma_f32_16x16x32_bf16 v[84:87], a[148:151], a[44:47], v[84:87]\n" \
  "buffer_load_dwordx4 v249, s[20:23], s73 offen lds\n" \
  "v_mfma_f32_16x16x32_bf16 v[84:87], a[144:147], a[40:43], v[84:87]\n" \
  "v_mfma_f32_16x16x32_bf16 v[88:91], a[144:147], a[48:51], v[88:91]\n" \
  "v_mfma_f32_16x16x32_bf16 v[88:91], a[148:151], a[52:55], v[88:91]\n" \
  "ds_read_b128 a[96:99], v224 offset:40960\n" \
  "v_mfma_f32_16x16x32_bf16 v[92:95], a[148:151], a[60:63], v[92:95]\n" \
  "s_add_u32 m0, m0, s82\n" \
  "v_mfma_f32_16x16x32_bf16 v[92:95], a[144:147], a[56:59], v[92:95]\n" \
  "buffer_load_dwordx4 v249, s[20:23], s74 offen lds\n" \
  "v_mfma_f32_16x16x32_bf16 v[96:99], a[152:155], a[0:3], v[96:99]\n" \
  "ds_read_b64_tr_b16 a[144:145], v226 offset:32\n" \
  "v_mfma_f32_16x16x32_bf16 v[96:99], a[156:159], a[4:7], v[96:99]\n" \
  "ds_read_b128 a[100:103], v225 offset:40960\n" \
  "v_mfma_f32_16x16x32_bf16 v[100:103], a[156:159], a[12:15], v[100:103]\n" \
  "ds_read_b64_tr_b16 a[146:147], v226 offset:1824\n" \
  "v_mfma_f32_16x16x32_bf16 v[100:103], a[152:155], a[8:11], v[100:103]\n" \
  "v_mfma_f32_16x16x32_bf16 v[104:107], a[152:155], a[16:19], v[104:107]\n" \
  "ds_read_b64_tr_b16 a[148:149], v226 offset:7200\n" \
  "v_mfma_f32_16x16x32_bf16 v[104:107], a[156:159], a[20:23], v[104:107]\n" \
  "ds_read_b128 a[104:107], v224 offset:43008\n" \
  "s_sub_u32 s16, s16, 128\n" \
  "s_subb_u32 s17, s17, 0\n" \
  "v_mfma_f32_16x16x32_bf16 v[108:111], a[156:159], a[28:31], v[108:111]\n" \
  "ds_read_b64_tr_b16 a[150:151], v226 offset:8992\n" \
  "s_sub_u32 s20, s20, 128\n" \
  "s_subb_u32 s21, s21, 0\n" \
  "v_mfma_f32_16x16x32_bf16 v[108:111], a[152:155], a[24:27], v[108:111]\n" \
  "v_mfma_f32_16x16x32_bf16 v[112:115], a[152:155], a[32:35], v[112:115]\n" \
  "v_mfma_f32_16x16x32_bf16 v[112:115], a[156:159], a[36:39], v[112:115]\n" \
  "v_mfma_f32_16x16x32_bf16 v[116:119], a[156:159], a[44:47], v[116:119]\n" \
  "ds_read_b128 a[108:111], v225 offset:43008\n" \
  "v_mfma_f32_16x16x32_bf16 v[116:119], a[152:155], a[40:43], v[116:119]\n" \
  "s_add_u32 m0, s90, 98304\n" \
  "v_mfma_f32_16x16x32_bf16 v[120:123], a[152:155], a[48:51], v[120:123]\n" \
  "buffer_load_dwordx4 v248, s[16:19], 0 offen sc1 lds\n" \
  "v_mfma_f32_16x16x32_bf16 v[120:123], a[156:159], a[52:55], v[120:123]\n" \
  "v_mfma_f32_16x16x32_bf16 v[124:127], a[156:159], a[60:63], v[124:127]\n" \
  "ds_read_b128 a[112:115], v224 offset:45056\n" \
  "v_mfma_f32_16x16x32_bf16 v[124:127], a[152:155], a[56:59], v[124:127]\n" \
  "v_mfma_f32_16x16x32_bf16 v[128:131], a[160:163], a[0:3], v[128:131]\n" \
  "ds_read_b64_tr_b16 a[152:153], v226 offset:14384\n" \
  "s_add_u32 m0, m0, 2048\n" \
  "v_mfma_f32_16x16x32_bf16 v[128:131], a[164:167], a[4:7], v[128:131]\n" \
  "buffer_load_dwordx4 v248, s[16:19], s79 offen sc1 lds\n" \
  "v_mfma_f32_16x16x32_bf16 v[132:135], a[164:167], a[12:15], v[132:135]\n" \
  "ds_read_b128 a[116:119], v225 offset:45056\n" \
  "ds_read_b64_tr_b16 a[154:155], v226 offset:16176\n" \
  "v_mfma_f32_16x16x32_bf16 v[132:135], a[160:163], a[8:11], v[132:135]\n" \
  "v_mfma_f32_16x16x32_bf16 v[136:139], a[160:163], a[16:19], v[136:139]\n" \
  "ds_read_b64_tr_b16 a[156:157], v226 offset:21552\n" \
  "v_mfma_f32_16x16x32_bf16 v[136:139], a[164:167], a[20:23], v[136:139]\n" \
  "s_add_u32 m0, m0, 2048\n" \
  "v_mfma_f32_16x16x32_bf16 v[140:143], a[164:167], a[28:31], v[140:143]\n" \
  "buffer_load_dwordx4 v248, s[16:19], s80 offen sc1 lds\n" \
  "ds_read_b64_tr_b16 a[158:159], v226 offset:23344\n" \
  "v_mfma_f32_16x16x32_bf16 v[140:143], a[160:163], a[24:27], v[140:143]\n" \
  "ds_read_b128 a[120:123], v224 offset:47104\n" \
  "v_mfma_f32_16x16x32_bf16 v[144:147], a[160:163], a[32:35], v[144:147]\n" \
  "v_mfma_f32_16x16x32_bf16 v[144:147], a[164:167], a[36:39], v[144:147]\n" \
  "v_mfma_f32_16x16x32_bf16 v[148:151], a[164:167], a[44:47], v[148:151]\n" \
  "s_add_u32 m0, m0, 2048\n" \
  "v_mfma_f32_16x16x32_bf16 v[148:151], a[160:163], a[40:43], v[148:151]\n" \
  "buffer_load_dwordx4 v248, s[16:19], s81 offen sc1 lds\n" \
  "ds_read_b128 a[124:127], v225 offset:47104\n" \
  "v_mfma_f32_16x16x32_bf16 v[152:155], a[160:163], a[48:51], v[152:155]\n" \
  "v_mfma_f32_16x16x32_bf16 v[152:155], a[164:167], a[52:55], v[152:155]\n" \
  "v_mfma_f32_16x16x32_bf16 v[156:159], a[164:167], a[60:63], v[156:159]\n" \
  "v_mfma_f32_16x16x32_bf16 v[156:159], a[160:163], a[56:59], v[156:159]\n" \
  "s_add_u32 m0, s91, 98304\n" \
  "v_mfma_f32_16x16x32_bf16 v[160:163], a[168:171], a[0:3], v[160:163]\n" \
  "buffer_load_dwordx4 v249, s[20:23], 0 offen lds\n" \
  "ds_read_b64_tr_b16 a[160:161], v226 offset:64\n" \
  "v_mfma_f32_16x16x32_bf16 v[160:163], a[172:175], a[4:7], v[160:163]\n" \
  "v_mfma_f32_16x16x32_bf16 v[164:167], a[172:175], a[12:15], v[164:167]\n" \
  "ds_read_b64_tr_b16 a[162:163], v226 offset:1856\n" \
  "v_mfma_f32_16x16x32_bf16 v[164:167], a[168:171], a[8:11], v[164:167]\n" \
  "v_mfma_f32_16x16x32_bf16 v[168:171], a[168:171], a[16:19], v[168:171]\n" \
  "ds_read_b64_tr_b16 a[164:165], v226 offset:7232\n" \
  "s_add_u32 m0, m0, s82\n" \
  "v_mfma_f32_16x16x32_bf16 v[168:171], a[172:175], a[20:23], v[168:171]\n" \
  "buffer_load_dwordx4 v249, s[20:23], s72 offen lds\n" \
  "v_mfma_f32_16x16x32_bf16 v[172:175], a[172:175], a[28:31], v[172:175]\n" \
  "ds_read_b64_tr_b16 a[166:167], v226 offset:9024\n" \
  "v_mfma_f32_16x16x32_bf16 v[172:175], a[168:171], a[24:27], v[172:175]\n" \
  "v_mfma_f32_16x16x32_bf16 v[176:179], a[168:171], a[32:35], v[176:179]\n" \
  "v_mfma_f32_16x16x32_bf16 v[176:179], a[172:175], a[36:39], v[176:179]\n" \
  "s_add_u32 m0, m0, s82\n" \
  "v_mfma_f32_16x16x32_bf16 v[180:183], a[172:175], a[44:47], v[180:183]\n" \
  "buffer_load_dwordx4 v249, s[20:23], s73 offen lds\n" \
  "v_mfma_f32_16x16x32_bf16 v[180:183], a[168:171], a[40:43], v[180:183]\n" \
  "v_mfma_f32_16x16x32_bf16 v[184:187], a[168:171], a[48:51], v[184:187]\n" \
  "v_mfma_f32_16x16x32_bf16 v[184:187], a[172:175], a[52:55], v[184:187]\n" \
  "v_mfma_f32_16x16x32_bf16 v[188:191], a[172:175], a[60:63], v[188:191]\n" \
  "s_add_u32 m0, m0, s82\n" \
  "v_mfma_f32_16x16x32_bf16 v[188:191], a[168:171], a[56:59], v[188:191]\n" \
  "buffer_load_dwordx4 v249, s[20:23], s74 offen lds\n" \
  "v_mfma_f32_16x16x32_bf16 v[192:195], a[176:179], a[0:3], v[192:195]\n" \
  "ds_read_b64_tr_b16 a[168:169], v226 offset:14416\n" \
  "v_mfma_f32_16x16x32_bf16 v[192:195], a[180:183], a[4:7], v[192:195]\n" \
  "v_mfma_f32_16x16x32_bf16 v[196:199], a[180:183], a[12:15], v[196:199]\n" \
  "ds_read_b64_tr_b16 a[170:171], v226 offset:16208\n" \
  "v_mfma_f32_16x16x32_bf16 v[196:199], a[176:179], a[8:11], v[196:199]\n" \
  "v_mfma_f32_16x16x32_bf16 v[200:203], a[176:179], a[16:19], v[200:203]\n" \
  "ds_read_b64_tr_b16 a[172:173], v226 offset:21584\n" \
  "v_mfma_f32_16x16x32_bf16 v[200:203], a[180:183], a[20:23], v[200:203]\n" \
  "s_sub_u32 s16, s16, 128\n" \
  "s_subb_u32 s17, s17, 0\n" \
  "v_mfma_f32_16x16x32_bf16 v[204:207], a[180:183], a[28:31], v[204:207]\n" \
  "ds_read_b64_tr_b16 a[174:175], v226 offset:23376\n" \
  "s_sub_u32 s20, s20, 128\n" \
  "s_subb_u32 s21, s21, 0\n" \
  "v_mfma_f32_16x16x32_bf16 v[204:207], a[176:179], a[24:27], v[204:207]\n" \
  "v_mfma_f32_16x16x32_bf16 v[208:211], a[176:179], a[32:35], v[208:211]\n" \
  "v_mfma_f32_16x16x32_bf16 v[208:211], a[180:183], a[36:39], v[208:211]\n" \
  "v_mfma_f32_16x16x32_bf16 v[212:215], a[180:183], a[44:47], v[212:215]\n" \
  "v_mfma_f32_16x16x32_bf16 v[212:215], a[176:179], a[40:43], v[212:215]\n" \
  "v_mfma_f32_16x16x32_bf16 v[216:219], a[176:179], a[48:51], v[216:219]\n" \
  "v_mfma_f32_16x16x32_bf16 v[216:219], a[180:183], a[52:55], v[216:219]\n" \
  "v_mfma_f32_16x16x32_bf16 v[220:223], a[180:183], a[60:63], v[220:223]\n" \
  "v_mfma_f32_16x16x32_bf16 v[220:223], a[176:179], a[56:59], v[220:223]\n" \
  "s_waitcnt lgkmcnt(0)\n" \
  "v_mfma_f32_16x16x32_bf16 v[0:3], a[128:131], a[64:67], v[0:3]\n" \
  "ds_read_b64_tr_b16 a[176:177], v227 offset:0\n" \
  "v_mfma_f32_16x16x32_bf16 v[0:3], a[132:135], a[68:71], v[0:3]\n" \
  "ds_read_b64_tr_b16 a[178:179], v227 offset:1792\n" \
  "v_mfma_f32_16x16x32_bf16 v[4:7], a[132:135], a[76:79], v[4:7]\n" \
  "ds_read_b64_tr_b16 a[180:181], v227 offset:7168\n" \
  "s_add_u32 s96, s94, 0\n" \
  "v_mfma_f32_16x16x32_bf16 v[4:7], a[128:131], a[72:75], v[4:7]\n" \
  "ds_read_b64_tr_b16 a[182:183], v227 offset:8960\n" \
  "s_add_u32 s97, s94, 16384\n" \
  "v_mfma_f32_16x16x32_bf16 v[8:11], a[128:131], a[80:83], v[8:11]\n" \
  "s_add_u32 s98, s94, 131072\n" \
  "v_mfma_f32_16x16x32_bf16 v[8:11], a[132:135], a[84:87], v[8:11]\n" \
  "s_add_u32 s99, s94, 147456\n" \
  "v_mfma_f32_16x16x32_bf16 v[12:15], a[132:135], a[92:95], v[12:15]\n" \
  "v_mfma_f32_16x16x32_bf16 v[12:15], a[128:131], a[88:91], v[12:15]\n" \
  "v_mfma_f32_16x16x32_bf16 v[16:19], a[128:131], a[96:99], v[16:19]\n" \
  "v_mfma_f32_16x16x32_bf16 v[16:19], a[132:135], a[100:103], v[16:19]\n" \
  "s_waitcnt lgkmcnt(0)\n" \
  "s_waitcnt vmcnt(8)\n" \
  "s_barrier\n" \
  "v_mfma_f32_16x16x32_bf16 v[20:23], a[132:135], a[108:111], v[20:23]\n" \
  "ds_read_b128 a[32:35], v244 offset:0\n" \
  "v_mfma_f32_16x16x32_bf16 v[20:23], a[128:131], a[104:107], v[20:23]\n" \
  "s_add_u32 m0, s90, 32768\n" \
  "v_mfma_f32_16x16x32_bf16 v[24:27], a[128:131], a[112:115], v[24:27]\n" \
  "buffer_load_dwordx4 v248, s[16:19], 0 offen sc1 lds\n" \
  "v_mfma_f32_16x16x32_bf16 v[24:27], a[132:135], a[116:119], v[24:27]\n" \
  "ds_read_b128 a[36:39], v245 offset:0\n" \
  "v_mfma_f32_16x16x32_bf16 v[28:31], a[132:135], a[124:127], v[28:31]\n" \
  "v_mfma_f32_16x16x32_bf16 v[28:31], a[128:131], a[120:123], v[28:31]\n" \
  "v_mfma_f32_16x16x32_bf16 v[32:35], a[136:139], a[64:67], v[32:35]\n" \
  "ds_read_b128 a[0:3], v240 offset:0\n" \
  "s_add_u32 m0, m0, 2048\n" \
  "v_mfma_f32_16x16x32_bf16 v[32:35], a[140:143], a[68:71], v[32:35]\n" \
  "buffer_load_dwordx4 v248, s[16:19], s79 offen sc1 lds\n" \
  "v_mfma_f32_16x16x32_bf16 v[36:39], a[140:143], a[76:79], v[36:39]\n" \
  "v_mfma_f32_16x16x32_bf16 v[36:39], a[136:139], a[72:75], v[36:39]\n" \
  "v_mfma_f32_16x16x32_bf16 v[40:43], a[136:139], a[80:83], v[40:43]\n" \
  "ds_read_b128 a[4:7], v241 offset:0\n" \
  "v_mfma_f32_16x16x32_bf16 v[40:43], a[140:143], a[84:87], v[40:43]\n" \
  "s_add_u32 m0, m0, 2048\n" \
  "v_mfma_f32_16x16x32_bf16 v[44:47], a[140:143], a[92:95], v[44:47]\n" \
  "buffer_load_dwordx4 v248, s[16:19], s80 offen sc1 lds\n" \
  "v_mfma_f32_16x16x32_bf16 v[44:47], a[136:139], a[88:91], v[44:47]\n" \
  "ds_read_b128 a[8:11], v240 offset:2048\n" \
  "v_mfma_f32_16x16x32_bf16 v[48:51], a[136:139], a[96:99], v[48:51]\n" \
  "v_mfma_f32_16x16x32_bf16 v[48:51], a[140:143], a[100:103], v[48:51]\n" \
  "v_mfma_f32_16x16x32_bf16 v[52:55], a[140:143], a[108:111], v[52:55]\n" \
  "ds_read_b128 a[12:15], v241 offset:2048\n" \
  "s_add_u32 m0, m0, 2048\n" \
  "v_mfma_f32_16x16x32_bf16 v[52:55], a[136:139], a[104:107], v[52:55]\n" \
  "buffer_load_dwordx4 v248, s[16:19], s81 offen sc1 lds\n" \
  "v_mfma_f32_16x16x32_bf16 v[56:59], a[136:139], a[112:115], v[56:59]\n" \
  "v_mfma_f32_16x16x32_bf16 v[56:59], a[140:143], a[116:119], v[56:59]\n" \
  "v_mfma_f32_16x16x32_bf16 v[60:63], a[140:143], a[124:127], v[60:63]\n" \
  "ds_read_b128 a[16:19], v240 offset:4096\n" \
  "v_mfma_f32_16x16x32_bf16 v[60:63], a[136:139], a[120:123], v[60:63]\n" \
  "s_add_u32 m0, s91, 32768\n" \
  "v_mfma_f32_16x16x32_bf16 v[64:67], a[144:147], a[64:67], v[64:67]\n" \
  "buffer_load_dwordx4 v249, s[20:23], 0 offen lds\n" \
  "v_mfma_f32_16x16x32_bf16 v[64:67], a[148:151], a[68:71], v[64:67]\n" \
  "ds_read_b128 a[20:23], v241 offset:4096\n" \
  "v_mfma_f32_16x16x32_bf16 v[68:71], a[148:151], a[76:79], v[68:71]\n" \
  "v_mfma_f32_16x16x32_bf16 v[68:71], a[144:147], a[72:75], v[68:71]\n" \
  "v_mfma_f32_16x16x32_bf16 v[72:75], a[144:147], a[80:83], v[72:75]\n" \
  "s_add_u32 m0, m0, s82\n" \
  "v_mfma_f32_16x16x32_bf16 v[72:75], a[148:151], a[84:87], v[72:75]\n" \
  "buffer_load_dwordx4 v249, s[20:23], s72 offen lds\n" \
  "ds_read_b128 a[24:27], v240 offset:6144\n" \
  "v_mfma_f32_16x16x32_bf16 v[76:79], a[148:151], a[92:95], v[76:79]\n" \
  "v_mfma_f32_16x16x32_bf16 v[76:79], a[144:147], a[88:91], v[76:79]\n" \
  "v_mfma_f32_16x16x32_bf16 v[80:83], a[144:147], a[96:99], v[80:83]\n" \
  "ds_read_b128 a[28:31], v241 offset:6144\n" \
  "v_mfma_f32_16x16x32_bf16 v[80:83], a[148:151], a[100:103], v[80:83]\n" \
  "s_add_u32 m0, m0, s82\n" \
  "v_mfma_f32_16x16x32_bf16 v[84:87], a[148:151], a[108:111], v[84:87]\n" \
  "buffer_load_dwordx4 v249, s[20:23], s73 offen lds\n" \
  "v_mfma_f32_16x16x32_bf16 v[84:87], a[144:147], a[104:107], v[84:87]\n" \
  "ds_read_b128 a[40:43], v244 offset:2048\n" \
  "v_mfma_f32_16x16x32_bf16 v[88:91], a[144:147], a[112:115], v[88:91]\n" \
  "v_mfma_f32_16x16x32_bf16 v[88:91], a[148:151], a[116:119], v[88:91]\n" \
  "v_mfma_f32_16x16x32_bf16 v[92:95], a[148:151], a[124:127], v[92:95]\n" \
  "s_add_u32 m0, m0, s82\n" \
  "v_mfma_f32_16x16x32_bf16 v[92:95], a[144:147], a[120:123], v[92:95]\n" \
  "buffer_load_dwordx4 v249, s[20:23], s74 offen lds\n" \
  "ds_read_b128 a[44:47], v245 offset:2048\n" \
  "v_mfma_f32_16x16x32_bf16 v[96:99], a[152:155], a[64:67], v[96:99]\n" \
  "v_mfma_f32_16x16x32_bf16 v[96:99], a[156:159], a[68:71], v[96:99]\n" \
  "v_mfma_f32_16x16x32_bf16 v[100:103], a[156:159], a[76:79], v[100:103]\n" \
  "ds_read_b128 a[48:51], v244 offset:4096\n" \
  "v_mfma_f32_16x16x32_bf16 v[100:103], a[152:155], a[72:75], v[100:103]\n" \
  "v_mfma_f32_16x16x32_bf16 v[104:107], a[152:155], a[80:83], v[104:107]\n" \
  "v_mfma_f32_16x16x32_bf16 v[104:107], a[156:159], a[84:87], v[104:107]\n" \
  "ds_read_b128 a[52:55], v245 offset:4096\n" \
  "s_sub_u32 s16, s16, 128\n" \
  "s_subb_u32 s17, s17, 0\n" \
  "v_mfma_f32_16x16x32_bf16 v[108:111], a[156:159], a[92:95], v[108:111]\n" \
  "s_sub_u32 s20, s20, 128\n" \
  "s_subb_u32 s21, s21, 0\n" \
  "v_mfma_f32_16x16x32_bf16 v[108:111], a[152:155], a[88:91], v[108:111]\n" \
  "v_mfma_f32_16x16x32_bf16 v[112:115], a[152:155], a[96:99], v[112:115]\n" \
  "v_mfma_f32_16x16x32_bf16 v[112:115], a[156:159], a[100:103], v[112:115]\n" \
  "ds_read_b128 a[56:59], v244 offset:6144\n" \
  "v_mfma_f32_16x16x32_bf16 v[116:119], a[156:159], a[108:111], v[116:119]\n" \
  "v_mfma_f32_16x16x32_bf16 v[116:119], a[152:155], a[104:107], v[116:119]\n" \
  "s_add_u32 m0, s90, 65536\n" \
  "v_mfma_f32_16x16x32_bf16 v[120:123], a[152:155], a[112:115], v[120:123]\n" \
  "buffer_load_dwordx4 v248, s[16:19], 0 offen sc1 lds\n" \
  "ds_read_b128 a[60:63], v245 offset:6144\n" \
  "v_mfma_f32_16x16x32_bf16 v[120:123], a[156:159], a[116:119], v[120:123]\n" \
  "v_mfma_f32_16x16x32_bf16 v[124:127], a[156:159], a[124:127], v[124:127]\n" \
  "v_mfma_f32_16x16x32_bf16 v[124:127], a[152:155], a[120:123], v[124:127]\n" \
  "v_mfma_f32_16x16x32_bf16 v[128:131], a[160:163], a[64:67], v[128:131]\n" \
  "s_add_u32 m0, m0, 2048\n" \
  "v_mfma_f32_16x16x32_bf16 v[128:131], a[164:167], a[68:71], v[128:131]\n" \
  "buffer_load_dwordx4 v248, s[16:19], s79 offen sc1 lds\n" \
  "v_mfma_f32_16x16x32_bf16 v[132:135], a[164:167], a[76:79], v[132:135]\n" \
  "v_mul_f32 v0, s86, v0\n" \
  "v_mul_f32 v1, s86, v1\n" \
  "v_mfma_f32_16x16x32_bf16 v[132:135], a[160:163], a[72:75], v[132:135]\n" \
  "v_exp_f32 v0, v0\n" \
  "v_mfma_f32_16x16x32_bf16 v[136:139], a[160:163], a[80:83], v[136:139]\n" \
  "v_mul_f32 v2, s86, v2\n" \
  "v_mfma_f32_16x16x32_bf16 v[136:139], a[164:167], a[84:87], v[136:139]\n" \
  "s_add_u32 m0, m0, 2048\n" \
  "v_exp_f32 v1, v1\n" \
  "v_mfma_f32_16x16x32_bf16 v[140:143], a[164:167], a[92:95], v[140:143]\n" \
  "buffer_load_dwordx4 v248, s[16:19], s80 offen sc1 lds\n" \
  "v_mfma_f32_16x16x32_bf16 v[140:143], a[160:163], a[88:91], v[140:143]\n" \
  "v_add_f32 v0, 1.0, v0\n" \
  "v_mul_f32 v3, s86, v3\n" \
  "v_mfma_f32_16x16x32_bf16 v[144:147], a[160:163], a[96:99], v[144:147]\n" \
  "v_exp_f32 v2, v2\n" \
  "v_add_f32 v1, 1.0, v1\n" \
  "v_mfma_f32_16x16x32_bf16 v[144:147], a[164:167], a[100:103], v[144:147]\n" \
  "v_rcp_f32 v0, v0\n" \
  "v_mul_f32 v64, s86, v64\n" \
  "v_mfma_f32_16x16x32_bf16 v[148:151], a[164:167], a[108:111], v[148:151]\n" \
  "s_add_u32 m0, m0, 2048\n" \
  "v_exp_f32 v3, v3\n" \
  "v_mfma_f32_16x16x32_bf16 v[148:151], a[160:163], a[104:107], v[148:151]\n" \
  "buffer_load_dwordx4 v248, s[16:19], s81 offen sc1 lds\n" \
  "v_mfma_f32_16x16x32_bf16 v[152:155], a[160:163], a[112:115], v[152:155]\n" \
  "v_add_f32 v2, 1.0, v2\n" \
  "v_rcp_f32 v1, v1\n" \
  "v_mfma_f32_16x16x32_bf16 v[152:155], a[164:167], a[116:119], v[152:155]\n" \
  "v_mul_f32 v0, v0, v32\n" \
  "v_mul_f32 v65, s86, v65\n" \
  "v_mfma_f32_16x16x32_bf16 v[156:159], a[164:167], a[124:127], v[156:159]\n" \
  "v_exp_f32 v64, v64\n" \
  "v_add_f32 v3, 1.0, v3\n" \
  "v_mfma_f32_16x16x32_bf16 v[156:159], a[160:163], a[120:123], v[156:159]\n" \
  "s_add_u32 m0, s91, 65536\n" \
  "v_rcp_f32 v2, v2\n" \
  "v_mfma_f32_16x16x32_bf16 v[160:163], a[168:171], a[64:67], v[160:163]\n" \
  "buffer_load_dwordx4 v249, s[20:23], 0 offen lds\n" \
  "v_mfma_f32_16x16x32_bf16 v[160:163], a[172:175], a[68:71], v[160:163]\n" \
  "v_mul_f32 v1, v1, v33\n" \
  "v_mul_f32 v66, s86, v66\n" \
  "v_mfma_f32_16x16x32_bf16 v[164:167], a[172:175], a[76:79], v[164:167]\n" \
  "v_exp_f32 v65, v65\n" \
  "v_add_f32 v64, 1.0, v64\n" \
  "v_mfma_f32_16x16x32_bf16 v[164:167], a[168:171], a[72:75], v[164:167]\n" \
  "v_rcp_f32 v3, v3\n" \
  "v_mul_f32 v2, v2, v34\n" \
  "v_mfma_f32_16x16x32_bf16 v[168:171], a[168:171], a[80:83], v[168:171]\n" \
  "s_add_u32 m0, m0, s82\n" \
  "v_mul_f32 v67, s86, v67\n" \
  "v_mfma_f32_16x16x32_bf16 v[168:171], a[172:175], a[84:87], v[168:171]\n" \
  "buffer_load_dwordx4 v249, s[20:23], s72 offen lds\n" \
  "v_mfma_f32_16x16x32_bf16 v[172:175], a[172:175], a[92:95], v[172:175]\n" \
  "v_exp_f32 v66, v66\n" \
  "v_add_f32 v65, 1.0, v65\n" \
  "v_mfma_f32_16x16x32_bf16 v[172:175], a[168:171], a[88:91], v[172:175]\n" \
  "v_rcp_f32 v64, v64\n" \
  "v_mul_f32 v3, v3, v35\n" \
  "v_mfma_f32_16x16x32_bf16 v[176:179], a[168:171], a[96:99], v[176:179]\n" \
  "v_exp_f32 v67, v67\n" \
  "v_mfma_f32_16x16x32_bf16 v[176:179], a[172:175], a[100:103], v[176:179]\n" \
  "s_add_u32 m0, m0, s82\n" \
  "v_add_f32 v66, 1.0, v66\n" \
  "v_mfma_f32_16x16x32_bf16 v[180:183], a[172:175], a[108:111], v[180:183]\n" \
  "buffer_load_dwordx4 v249, s[20:23], s73 offen lds\n" \
  "v_mfma_f32_16x16x32_bf16 v[180:183], a[168:171], a[104:107], v[180:183]\n" \
  "v_rcp_f32 v65, v65\n" \
  "v_mul_f32 v64, v64, v96\n" \
  "v_mfma_f32_16x16x32_bf16 v[184:187], a[168:171], a[112:115], v[184:187]\n" \
  "v_add_f32 v67, 1.0, v67\n" \
  "v_rcp_f32 v66, v66\n" \
  "v_mfma_f32_16x16x32_bf16 v[184:187], a[172:175], a[116:119], v[184:187]\n" \
  "v_mul_f32 v65, v65, v97\n" \
  "v_mfma_f32_16x16x32_bf16 v[188:191], a[172:175], a[124:127], v[188:191]\n" \
  "s_add_u32 m0, m0, s82\n" \
  "v_rcp_f32 v67, v67\n" \
  "v_mfma_f32_16x16x32_bf16 v[188:191], a[168:171], a[120:123], v[188:191]\n" \
  "buffer_load_dwordx4 v249, s[20:23], s74 offen lds\n" \
  "v_mfma_f32_16x16x32_bf16 v[192:195], a[176:179], a[64:67], v[192:195]\n" \
  "v_mul_f32 v66, v66, v98\n" \
  "v_mul_f32 v67, v67, v99\n" \
  "v_cvt_pk_bf16_f32 v32, v0, v1\n" \
  "v_mfma_f32_16x16x32_bf16 v[192:195], a[180:183], a[68:71], v[192:195]\n" \
  "v_cvt_pk_bf16_f32 v33, v2, v3\n" \
  "v_cvt_pk_bf16_f32 v34, v64, v65\n" \
  "v_cvt_pk_bf16_f32 v35, v66, v67\n" \
  "v_mfma_f32_16x16x32_bf16 v[196:199], a[180:183], a[76:79], v[196:199]\n" \
  "s_mov_b32 s69, 0\n" \
  "v_mfma_f32_16x16x32_bf16 v[196:199], a[176:179], a[72:75], v[196:199]\n" \
  "buffer_store_dwordx4 v[32:35], v250, s[24:27], s69 offen sc1\n" \
  "v_mfma_f32_16x16x32_bf16 v[200:203], a[176:179], a[80:83], v[200:203]\n" \
  "v_mul_f32 v4, s86, v4\n" \
  "v_mul_f32 v5, s86, v5\n" \
  "v_mfma_f32_16x16x32_bf16 v[200:203], a[180:183], a[84:87], v[200:203]\n" \
  "s_sub_u32 s16, s16, 128\n" \
  "s_subb_u32 s17, s17, 0\n" \
  "v_mfma_f32_16x16x32_bf16 v[204:207], a[180:183], a[92:95], v[204:207]\n" \
  "s_sub_u32 s20, s20, 128\n" \
  "s_subb_u32 s21, s21, 0\n" \
  "v_mfma_f32_16x16x32_bf16 v[204:207], a[176:179], a[88:91], v[204:207]\n" \
  "v_exp_f32 v4, v4\n" \
  "v_mul_f32 v6, s86, v6\n" \
  "v_mfma_f32_16x16x32_bf16 v[208:211], a[176:179], a[96:99], v[208:211]\n" \
  "v_exp_f32 v5, v5\n" \
  "v_add_f32 v4, 1.0, v4\n" \
  "v_mfma_f32_16x16x32_bf16 v[208:211], a[180:183], a[100:103], v[208:211]\n" \
  "v_mul_f32 v7, s86, v7\n" \
  "v_mfma_f32_16x16x32_bf16 v[212:215], a[180:183], a[108:111], v[212:215]\n" \
  "v_exp_f32 v6, v6\n" \
  "v_add_f32 v5, 1.0, v5\n" \
  "v_mfma_f32_16x16x32_bf16 v[212:215], a[176:179], a[104:107], v[212:215]\n" \
  "v_rcp_f32 v4, v4\n" \
  "v_mul_f32 v68, s86, v68\n" \
  "v_mfma_f32_16x16x32_bf16 v[216:219], a[176:179], a[112:115], v[216:219]\n" \
  "v_exp_f32 v7, v7\n" \
  "v_mfma_f32_16x16x32_bf16 v[216:219], a[180:183], a[116:119], v[216:219]\n" \
  "v_add_f32 v6, 1.0, v6\n" \
  "v_mfma_f32_16x16x32_bf16 v[220:223], a[180:183], a[124:127], v[220:223]\n" \
  "v_rcp_f32 v5, v5\n" \
  "v_mul_f32 v4, v4, v36\n" \
  "v_mfma_f32_16x16x32_bf16 v[220:223], a[176:179], a[120:123], v[220:223]\n" \
  "v_mul_f32 v69, s86, v69\n" \
  "v_exp_f32 v68, v68\n" \
  "s_waitcnt lgkmcnt(0)\n" \
  "s_waitcnt vmcnt(17)\n" \
  "s_barrier\n" \
  "v_mfma_f32_16x16x32_bf16 a[128:131], a[0:3], a[32:35], 0\n" \
  "s_add_u32 m0, s90, 0\n" \
  "ds_read_b128 a[96:99], v246 offset:32768\n" \
  "v_add_f32 v7, 1.0, v7\n" \
  "v_mfma_f32_16x16x32_bf16 a[128:131], a[4:7], a[36:39], a[128:131]\n" \
  "buffer_load_dwordx4 v248, s[16:19], 0 offen sc1 lds\n" \
  "v_mfma_f32_16x16x32_bf16 a[132:135], a[12:15], a[36:39], 0\n" \
  "ds_read_b128 a[100:103], v247 offset:32768\n" \
  "v_rcp_f32 v6, v6\n" \
  "v_mul_f32 v5, v5, v37\n" \
  "v_mfma_f32_16x16x32_bf16 a[132:135], a[8:11], a[32:35], a[132:135]\n" \
  "s_add_u32 m0, m0, 2048\n" \
  "ds_read_b128 a[64:67], v242 offset:32768\n" \
  "v_mul_f32 v70, s86, v70\n" \
  "v_mfma_f32_16x16x32_bf16 a[136:139], a[16:19], a[32:35], 0\n" \
  "buffer_load_dwordx4 v248, s[16:19], s79 offen sc1 lds\n" \
  "v_mfma_f32_16x16x32_bf16 a[136:139], a[20:23], a[36:39], a[136:139]\n" \
  "ds_read_b128 a[68:71], v243 offset:32768\n" \
  "v_exp_f32 v69, v69\n" \
  "v_add_f32 v68, 1.0, v68\n" \
  "v_mfma_f32_16x16x32_bf16 a[140:143], a[28:31], a[36:39], 0\n" \
  "ds_read_b128 a[72:75], v242 offset:34816\n" \
  "v_rcp_f32 v7, v7\n" \
  "v_mul_f32 v6, v6, v38\n" \
  "v_mfma_f32_16x16x32_bf16 a[140:143], a[24:27], a[32:35], a[140:143]\n" \
  "s_add_u32 m0, m0, 2048\n" \
  "ds_read_b128 a[76:79], v243 offset:34816\n" \
  "v_mul_f32 v71, s86, v71\n" \
  "v_mfma_f32_16x16x32_bf16 a[144:147], a[0:3], a[40:43], 0\n" \
  "buffer_load_dwordx4 v248, s[16:19], s80 offen sc1 lds\n" \
  "v_mfma_f32_16x16x32_bf16 a[144:147], a[4:7], a[44:47], a[144:147]\n" \
  "ds_read_b128 a[80:83], v242 offset:36864\n" \
  "v_exp_f32 v70, v70\n" \
  "v_add_f32 v69, 1.0, v69\n" \
  "v_mfma_f32_16x16x32_bf16 a[148:151], a[12:15], a[44:47], 0\n" \
  "s_add_u32 m0, m0, 2048\n" \
  "ds_read_b128 a[84:87], v243 offset:36864\n" \
  "v_rcp_f32 v68, v68\n" \
  "v_mfma_f32_16x16x32_bf16 a[148:151], a[8:11], a[40:43], a[148:151]\n" \
  "buffer_load_dwordx4 v248, s[16:19], s81 offen sc1 lds\n" \
  "v_mul_f32 v7, v7, v39\n" \
  "v_mfma_f32_16x16x32_bf16 a[152:155], a[16:19], a[40:43], 0\n" \
  "ds_read_b128 a[88:91], v242 offset:38912\n" \
  "v_exp_f32 v71, v71\n" \
  "v_add_f32 v70, 1.0, v70\n" \
  "v_mfma_f32_16x16x32_bf16 a[152:155], a[20:23], a[44:47], a[152:155]\n" \
  "ds_read_b128 a[92:95], v243 offset:38912\n" \
  "v_rcp_f32 v69, v69\n" \
  "v_mul_f32 v68, v68, v100\n" \
  "v_mfma_f32_16x16x32_bf16 a[156:159], a[28:31], a[44:47], 0\n" \
  "s_add_u32 m0, s91, 0\n" \
  "ds_read_b128 a[104:107], v246 offset:34816\n" \
  "v_add_f32 v71, 1.0, v71\n" \
  "v_mfma_f32_16x16x32_bf16 a[156:159], a[24:27], a[40:43], a[156:159]\n" \
  "buffer_load_dwordx4 v249, s[20:23], 0 offen lds\n" \
  "v_mfma_f32_16x16x32_bf16 a[160:163], a[0:3], a[48:51], 0\n" \
  "ds_read_b128 a[108:111], v247 offset:34816\n" \
  "v_rcp_f32 v70, v70\n" \
  "v_mul_f32 v69, v69, v101\n" \
  "v_mfma_f32_16x16x32_bf16 a[160:163], a[4:7], a[52:55], a[160:163]\n" \
  "s_add_u32 m0, m0, s82\n" \
  "v_rcp_f32 v71, v71\n" \
  "v_mul_f32 v70, v70, v102\n" \
  "v_mfma_f32_16x16x32_bf16 a[164:167], a[12:15], a[52:55], 0\n" \
  "buffer_load_dwordx4 v249, s[20:23], s72 offen lds\n" \
  "v_mul_f32 v71, v71, v103\n" \
  "v_mfma_f32_16x16x32_bf16 a[164:167], a[8:11], a[48:51], a[164:167]\n" \
  "ds_read_b128 a[112:115], v246 offset:36864\n" \
  "v_cvt_pk_bf16_f32 v36, v4, v5\n" \
  "v_cvt_pk_bf16_f32 v37, v6, v7\n" \
  "v_cvt_pk_bf16_f32 v38, v68, v69\n" \
  "v_mfma_f32_16x16x32_bf16 a[168:171], a[16:19], a[48:51], 0\n" \
  "ds_read_b128 a[116:119], v247 offset:36864\n" \
  "v_cvt_pk_bf16_f32 v39, v70, v71\n" \
  "s_mov_b32 s69, 114688\n" \
  "v_mfma_f32_16x16x32_bf16 a[168:171], a[20:23], a[52:55], a[168:171]\n" \
  "s_add_u32 m0, m0, s82\n" \
  "buffer_store_dwordx4 v[36:39], v250, s[24:27], s69 offen sc1\n" \
  "v_mfma_f32_16x16x32_bf16 a[172:175], a[28:31], a[52:55], 0\n" \
  "buffer_load_dwordx4 v249, s[20:23], s73 offen lds\n" \
  "v_mul_f32 v8, s86, v8\n" \
  "v_mfma_f32_16x16x32_bf16 a[172:175], a[24:27], a[48:51], a[172:175]\n" \
  "ds_read_b128 a[120:123], v246 offset:38912\n" \
  "v_mul_f32 v9, s86, v9\n" \
  "v_exp_f32 v8, v8\n" \
  "v_mfma_f32_16x16x32_bf16 a[176:179], a[0:3], a[56:59], 0\n" \
  "s_add_u32 m0, m0, s82\n" \
  "v_mul_f32 v10, s86, v10\n" \
  "v_exp_f32 v9, v9\n" \
  "v_mfma_f32_16x16x32_bf16 a[176:179], a[4:7], a[60:63], a[176:179]\n" \
  "buffer_load_dwordx4 v249, s[20:23], s74 offen lds\n" \
  "v_add_f32 v8, 1.0, v8\n" \
  "v_mfma_f32_16x16x32_bf16 a[180:183], a[12:15], a[60:63], 0\n" \
  "ds_read_b128 a[124:127], v247 offset:38912\n" \
  "v_mul_f32 v11, s86, v11\n" \
  "v_exp_f32 v10, v10\n" \
  "v_mfma_f32_16x16x32_bf16 a[180:183], a[8:11], a[56:59], a[180:183]\n" \
  "v_add_f32 v9, 1.0, v9\n" \
  "v_rcp_f32 v8, v8\n" \
  "v_mfma_f32_16x16x32_bf16 a[184:187], a[16:19], a[56:59], 0\n" \
  "s_sub_u32 s16, s16, 128\n" \
  "s_subb_u32 s17, s17, 0\n" \
  "v_mul_f32 v72, s86, v72\n" \
  "v_mfma_f32_16x16x32_bf16 a[184:187], a[20:23], a[60:63], a[184:187]\n" \
  "v_exp_f32 v11, v11\n" \
  "v_add_f32 v10, 1.0, v10\n" \
  "v_mfma_f32_16x16x32_bf16 a[188:191], a[28:31], a[60:63], 0\n" \
  "s_sub_u32 s20, s20, 128\n" \
  "s_subb_u32 s21, s21, 0\n" \
  "v_rcp_f32 v9, v9\n" \
  "v_mfma_f32_16x16x32_bf16 a[188:191], a[24:27], a[56:59], a[188:191]\n" \
  "v_mul_f32 v8, v8, v40\n" \
  "v_mul_f32 v73, s86, v73\n" \
  "s_waitcnt lgkmcnt(0)\n" \
  "s_waitcnt vmcnt(18)\n" \
  "s_barrier\n" \
  "v_mfma_f32_16x16x32_bf16 a[128:131], a[64:67], a[96:99], a[128:131]\n" \
  "s_add_u32 m0, s90, 98304\n" \
  "ds_read_b128 a[32:35], v244 offset:32768\n" \
  "v_exp_f32 v72, v72\n" \
  "v_mfma_f32_16x16x32_bf16 a[128:131], a[68:71], a[100:103], a[128:131]\n" \
  "buffer_load_dwordx4 v248, s[16:19], 0 offen sc1 lds\n" \
  "v_add_f32 v11, 1.0, v11\n" \
  "v_mfma_f32_16x16x32_bf16 a[132:135], a[76:79], a[100:103], a[132:135]\n" \
  "ds_read_b128 a[36:39], v245 offset:32768\n" \
  "v_rcp_f32 v10, v10\n" \
  "v_mul_f32 v9, v9, v41\n" \
  "v_mfma_f32_16x16x32_bf16 a[132:135], a[72:75], a[96:99], a[132:135]\n" \
  "s_add_u32 m0, m0, 2048\n" \
  "ds_read_b128 a[0:3], v240 offset:32768\n" \
  "v_mul_f32 v74, s86, v74\n" \
  "v_mfma_f32_16x16x32_bf16 a[136:139], a[80:83], a[96:99], a[136:139]\n" \
  "buffer_load_dwordx4 v248, s[16:19], s79 offen sc1 lds\n" \
  "v_mfma_f32_16x16x32_bf16 a[136:139], a[84:87], a[100:103], a[136:139]\n" \
  "ds_read_b128 a[4:7], v241 offset:32768\n" \
  "v_exp_f32 v73, v73\n" \
  "v_add_f32 v72, 1.0, v72\n" \
  "v_mfma_f32_16x16x32_bf16 a[140:143], a[92:95], a[100:103], a[140:143]\n" \
  "ds_read_b128 a[8:11], v240 offset:34816\n" \
  "v_rcp_f32 v11, v11\n" \
  "v_mul_f32 v10, v10, v42\n" \
  "v_mfma_f32_16x16x32_bf16 a[140:143], a[88:91], a[96:99], a[140:143]\n" \
  "s_add_u32 m0, m0, 2048\n" \
  "ds_read_b128 a[12:15], v241 offset:34816\n" \
  "v_mul_f32 v75, s86, v75\n" \
  "v_mfma_f32_16x16x32_bf16 a[144:147], a[64:67], a[104:107], a[144:147]\n" \
  "buffer_load_dwordx4 v248, s[16:19], s80 offen sc1 lds\n" \
  "v_mfma_f32_16x16x32_bf16 a[144:147], a[68:71], a[108:111], a[144:147]\n" \
  "ds_read_b128 a[16:19], v240 offset:36864\n" \
  "v_exp_f32 v74, v74\n" \
  "v_add_f32 v73, 1.0, v73\n" \
  "v_mfma_f32_16x16x32_bf16 a[148:151], a[76:79], a[108:111], a[148:151]\n" \
  "s_add_u32 m0, m0, 2048\n" \
  "ds_read_b128 a[20:23], v241 offset:36864\n" \
  "v_rcp_f32 v72, v72\n" \
  "v_mfma_f32_16x16x32_bf16 a[148:151], a[72:75], a[104:107], a[148:151]\n" \
  "buffer_load_dwordx4 v248, s[16:19], s81 offen sc1 lds\n" \
  "v_mul_f32 v11, v11, v43\n" \
  "v_mfma_f32_16x16x32_bf16 a[152:155], a[80:83], a[104:107], a[152:155]\n" \
  "ds_read_b128 a[24:27], v240 offset:38912\n" \
  "v_exp_f32 v75, v75\n" \
  "v_add_f32 v74, 1.0, v74\n" \
  "v_mfma_f32_16x16x32_bf16 a[152:155], a[84:87], a[108:111], a[152:155]\n" \
  "ds_read_b128 a[28:31], v241 offset:38912\n" \
  "v_rcp_f32 v73, v73\n" \
  "v_mul_f32 v72, v72, v104\n" \
  "v_mfma_f32_16x16x32_bf16 a[156:159], a[92:95], a[108:111], a[156:159]\n" \
  "s_add_u32 m0, s91, 98304\n" \
  "ds_read_b128 a[40:43], v244 offset:34816\n" \
  "v_add_f32 v75, 1.0, v75\n" \
  "v_mfma_f32_16x16x32_bf16 a[156:159], a[88:91], a[104:107], a[156:159]\n" \
  "buffer_load_dwordx4 v249, s[20:23], 0 offen lds\n" \
  "v_mfma_f32_16x16x32_bf16 a[160:163], a[64:67], a[112:115], a[160:163]\n" \
  "ds_read_b128 a[44:47], v245 offset:34816\n" \
  "v_rcp_f32 v74, v74\n" \
  "v_mul_f32 v73, v73, v105\n" \
  "v_mfma_f32_16x16x32_bf16 a[160:163], a[68:71], a[116:119], a[160:163]\n" \
  "s_add_u32 m0, m0, s82\n" \
  "v_rcp_f32 v75, v75\n" \
  "v_mul_f32 v74, v74, v106\n" \
  "v_mfma_f32_16x16x32_bf16 a[164:167], a[76:79], a[116:119], a[164:167]\n" \
  "buffer_load_dwordx4 v249, s[20:23], s72 offen lds\n" \
  "v_mul_f32 v75, v75, v107\n" \
  "v_mfma_f32_16x16x32_bf16 a[164:167], a[72:75], a[112:115], a[164:167]\n" \
  "ds_read_b128 a[48:51], v244 offset:36864\n" \
  "v_cvt_pk_bf16_f32 v40, v8, v9\n" \
  "v_cvt_pk_bf16_f32 v41, v10, v11\n" \
  "v_cvt_pk_bf16_f32 v42, v72, v73\n" \
  "v_mfma_f32_16x16x32_bf16 a[168:171], a[80:83], a[112:115], a[168:171]\n" \
  "ds_read_b128 a[52:55], v245 offset:36864\n" \
  "v_cvt_pk_bf16_f32 v43, v74, v75\n" \
  "s_mov_b32 s69, 229376\n" \
  "v_mfma_f32_16x16x32_bf16 a[168:171], a[84:87], a[116:119], a[168:171]\n" \
  "s_add_u32 m0, m0, s82\n" \
  "buffer_store_dwordx4 v[40:43], v250, s[24:27], s69 offen sc1\n" \
  "v_mfma_f32_16x16x32_bf16 a[172:175], a[92:95], a[116:119], a[172:175]\n" \
  "buffer_load_dwordx4 v249, s[20:23], s73 offen lds\n" \
  "v_mul_f32 v12, s86, v12\n" \
  "v_mfma_f32_16x16x32_bf16 a[172:175], a[88:91], a[112:115], a[172:175]\n" \
  "ds_read_b128 a[56:59], v244 offset:38912\n" \
  "v_mul_f32 v13, s86, v13\n" \
  "v_exp_f32 v12, v12\n" \
  "v_mfma_f32_16x16x32_bf16 a[176:179], a[64:67], a[120:123], a[176:179]\n" \
  "s_add_u32 m0, m0, s82\n" \
  "v_mul_f32 v14, s86, v14\n" \
  "v_exp_f32 v13, v13\n" \
  "v_mfma_f32_16x16x32_bf16 a[176:179], a[68:71], a[124:127], a[176:179]\n" \
  "buffer_load_dwordx4 v249, s[20:23], s74 offen lds\n" \
  "v_add_f32 v12, 1.0, v12\n" \
  "v_mfma_f32_16x16x32_bf16 a[180:183], a[76:79], a[124:127], a[180:183]\n" \
  "ds_read_b128 a[60:63], v245 offset:38912\n" \
  "v_mul_f32 v15, s86, v15\n" \
  "v_exp_f32 v14, v14\n" \
  "v_mfma_f32_16x16x32_bf16 a[180:183], a[72:75], a[120:123], a[180:183]\n" \
  "v_add_f32 v13, 1.0, v13\n" \
  "v_rcp_f32 v12, v12\n" \
  "v_mfma_f32_16x16x32_bf16 a[184:187], a[80:83], a[120:123], a[184:187]\n" \
  "s_sub_u32 s16, s16, 128\n" \
  "s_subb_u32 s17, s17, 0\n" \
  "v_mul_f32 v76, s86, v76\n" \
  "v_mfma_f32_16x16x32_bf16 a[184:187], a[84:87], a[124:127], a[184:187]\n" \
  "v_exp_f32 v15, v15\n" \
  "v_add_f32 v14, 1.0, v14\n" \
  "v_mfma_f32_16x16x32_bf16 a[188:191], a[92:95], a[124:127], a[188:191]\n" \
  "s_sub_u32 s20, s20, 128\n" \
  "s_subb_u32 s21, s21, 0\n" \
  "v_rcp_f32 v13, v13\n" \
  "v_mfma_f32_16x16x32_bf16 a[188:191], a[88:91], a[120:123], a[188:191]\n" \
  "v_mul_f32 v12, v12, v44\n" \
  "v_mul_f32 v77, s86, v77\n" \
  "s_waitcnt lgkmcnt(0)\n" \
  "s_waitcnt vmcnt(19)\n" \
  "s_barrier\n" \
  "v_mfma_f32_16x16x32_bf16 a[128:131], a[0:3], a[32:35], a[128:131]\n" \
  "s_add_u32 m0, s90, 32768\n" \
  "ds_read_b128 a[96:99], v246 offset:0\n" \
  "v_exp_f32 v76, v76\n" \
  "v_mfma_f32_16x16x32_bf16 a[128:131], a[4:7], a[36:39], a[128:131]\n" \
  "buffer_load_dwordx4 v248, s[16:19], 0 offen sc1 lds\n" \
  "v_add_f32 v15, 1.0, v15\n" \
  "v_mfma_f32_16x16x32_bf16 a[132:135], a[12:15], a[36:39], a[132:135]\n" \
  "ds_read_b128 a[100:103], v247 offset:0\n" \
  "v_rcp_f32 v14, v14\n" \
  "v_mul_f32 v13, v13, v45\n" \
  "v_mfma_f32_16x16x32_bf16 a[132:135], a[8:11], a[32:35], a[132:135]\n" \
  "s_add_u32 m0, m0, 2048\n" \
  "ds_read_b128 a[64:67], v242 offset:0\n" \
  "v_mul_f32 v78, s86, v78\n" \
  "v_mfma_f32_16x16x32_bf16 a[136:139], a[16:19], a[32:35], a[136:139]\n" \
  "buffer_load_dwordx4 v248, s[16:19], s79 offen sc1 lds\n" \
  "v_mfma_f32_16x16x32_bf16 a[136:139], a[20:23], a[36:39], a[136:139]\n" \
  "ds_read_b128 a[68:71], v243 offset:0\n" \
  "v_exp_f32 v77, v77\n" \
  "v_add_f32 v76, 1.0, v76\n" \
  "v_mfma_f32_16x16x32_bf16 a[140:143], a[28:31], a[36:39], a[140:143]\n" \
  "ds_read_b128 a[72:75], v242 offset:2048\n" \
  "v_rcp_f32 v15, v15\n" \
  "v_mul_f32 v14, v14, v46\n" \
  "v_mfma_f32_16x16x32_bf16 a[140:143], a[24:27], a[32:35], a[140:143]\n" \
  "s_add_u32 m0, m0, 2048\n" \
  "ds_read_b128 a[76:79], v243 offset:2048\n" \
  "v_mul_f32 v79, s86, v79\n" \
  "v_mfma_f32_16x16x32_bf16 a[144:147], a[0:3], a[40:43], a[144:147]\n" \
  "buffer_load_dwordx4 v248, s[16:19], s80 offen sc1 lds\n" \
  "v_mfma_f32_16x16x32_bf16 a[144:147], a[4:7], a[44:47], a[144:147]\n" \
  "ds_read_b128 a[80:83], v242 offset:4096\n" \
  "v_exp_f32 v78, v78\n" \
  "v_add_f32 v77, 1.0, v77\n" \
  "v_mfma_f32_16x16x32_bf16 a[148:151], a[12:15], a[44:47], a[148:151]\n" \
  "s_add_u32 m0, m0, 2048\n" \
  "ds_read_b128 a[84:87], v243 offset:4096\n" \
  "v_rcp_f32 v76, v76\n" \
  "v_mfma_f32_16x16x32_bf16 a[148:151], a[8:11], a[40:43], a[148:151]\n" \
  "buffer_load_dwordx4 v248, s[16:19], s81 offen sc1 lds\n" \
  "v_mul_f32 v15, v15, v47\n" \
  "v_mfma_f32_16x16x32_bf16 a[152:155], a[16:19], a[40:43], a[152:155]\n" \
  "ds_read_b128 a[88:91], v242 offset:6144\n" \
  "v_exp_f32 v79, v79\n" \
  "v_add_f32 v78, 1.0, v78\n" \
  "v_mfma_f32_16x16x32_bf16 a[152:155], a[20:23], a[44:47], a[152:155]\n" \
  "ds_read_b128 a[92:95], v243 offset:6144\n" \
  "v_rcp_f32 v77, v77\n" \
  "v_mul_f32 v76, v76, v108\n" \
  "v_mfma_f32_16x16x32_bf16 a[156:159], a[28:31], a[44:47], a[156:159]\n" \
  "s_add_u32 m0, s91, 32768\n" \
  "ds_read_b128 a[104:107], v246 offset:2048\n" \
  "v_add_f32 v79, 1.0, v79\n" \
  "v_mfma_f32_16x16x32_bf16 a[156:159], a[24:27], a[40:43], a[156:159]\n" \
  "buffer_load_dwordx4 v249, s[20:23], 0 offen lds\n" \
  "v_mfma_f32_16x16x32_bf16 a[160:163], a[0:3], a[48:51], a[160:163]\n" \
  "ds_read_b128 a[108:111], v247 offset:2048\n" \
  "v_rcp_f32 v78, v78\n" \
  "v_mul_f32 v77, v77, v109\n" \
  "v_mfma_f32_16x16x32_bf16 a[160:163], a[4:7], a[52:55], a[160:163]\n" \
  "s_add_u32 m0, m0, s82\n" \
  "v_rcp_f32 v79, v79\n" \
  "v_mul_f32 v78, v78, v110\n" \
  "v_mfma_f32_16x16x32_bf16 a[164:167], a[12:15], a[52:55], a[164:167]\n" \
  "buffer_load_dwordx4 v249, s[20:23], s72 offen lds\n" \
  "v_mul_f32 v79, v79, v111\n" \
  "v_mfma_f32_16x16x32_bf16 a[164:167], a[8:11], a[48:51], a[164:167]\n" \
  "ds_read_b128 a[112:115], v246 offset:4096\n" \
  "v_cvt_pk_bf16_f32 v44, v12, v13\n" \
  "v_cvt_pk_bf16_f32 v45, v14, v15\n" \
  "v_cvt_pk_bf16_f32 v46, v76, v77\n" \
  "v_mfma_f32_16x16x32_bf16 a[168:171], a[16:19], a[48:51], a[168:171]\n" \
  "ds_read_b128 a[116:119], v247 offset:4096\n" \
  "v_cvt_pk_bf16_f32 v47, v78, v79\n" \
  "s_mov_b32 s69, 344064\n" \
  "v_mfma_f32_16x16x32_bf16 a[168:171], a[20:23], a[52:55], a[168:171]\n" \
  "s_add_u32 m0, m0, s82\n" \
  "buffer_store_dwordx4 v[44:47], v250, s[24:27], s69 offen sc1\n" \
  "v_mfma_f32_16x16x32_bf16 a[172:175], a[28:31], a[52:55], a[172:175]\n" \
  "buffer_load_dwordx4 v249, s[20:23], s73 offen lds\n" \
  "v_mul_f32 v16, s86, v16\n" \
  "v_mfma_f32_16x16x32_bf16 a[172:175], a[24:27], a[48:51], a[172:175]\n" \
  "ds_read_b128 a[120:123], v246 offset:6144\n" \
  "v_mul_f32 v17, s86, v17\n" \
  "v_exp_f32 v16, v16\n" \
  "v_mfma_f32_16x16x32_bf16 a[176:179], a[0:3], a[56:59], a[176:179]\n" \
  "s_add_u32 m0, m0, s82\n" \
  "v_mul_f32 v18, s86, v18\n" \
  "v_exp_f32 v17, v17\n" \
  "v_mfma_f32_16x16x32_bf16 a[176:179], a[4:7], a[60:63], a[176:179]\n" \
  "buffer_load_dwordx4 v249, s[20:23], s74 offen lds\n" \
  "v_add_f32 v16, 1.0, v16\n" \
  "v_mfma_f32_16x16x32_bf16 a[180:183], a[12:15], a[60:63], a[180:183]\n" \
  "ds_read_b128 a[124:127], v247 offset:6144\n" \
  "v_mul_f32 v19, s86, v19\n" \
  "v_exp_f32 v18, v18\n" \
  "v_mfma_f32_16x16x32_bf16 a[180:183], a[8:11], a[56:59], a[180:183]\n" \
  "v_add_f32 v17, 1.0, v17\n" \
  "v_rcp_f32 v16, v16\n" \
  "v_mfma_f32_16x16x32_bf16 a[184:187], a[16:19], a[56:59], a[184:187]\n" \
  "s_sub_u32 s16, s16, 128\n" \
  "s_subb_u32 s17, s17, 0\n" \
  "v_mul_f32 v80, s86, v80\n" \
  "v_mfma_f32_16x16x32_bf16 a[184:187], a[20:23], a[60:63], a[184:187]\n" \
  "v_exp_f32 v19, v19\n" \
  "v_add_f32 v18, 1.0, v18\n" \
  "v_mfma_f32_16x16x32_bf16 a[188:191], a[28:31], a[60:63], a[188:191]\n" \
  "s_sub_u32 s20, s20, 128\n" \
  "s_subb_u32 s21, s21, 0\n" \
  "v_rcp_f32 v17, v17\n" \
  "v_mfma_f32_16x16x32_bf16 a[188:191], a[24:27], a[56:59], a[188:191]\n" \
  "v_mul_f32 v16, v16, v48\n" \
  "v_mul_f32 v81, s86, v81\n" \
  "s_waitcnt lgkmcnt(0)\n" \
  "s_waitcnt vmcnt(18)\n" \
  "s_barrier\n" \
  "v_mfma_f32_16x16x32_bf16 a[128:131], a[64:67], a[96:99], a[128:131]\n" \
  "s_add_u32 m0, s90, 65536\n" \
  "ds_read_b128 a[32:35], v244 offset:0\n" \
  "v_exp_f32 v80, v80\n" \
  "v_mfma_f32_16x16x32_bf16 a[128:131], a[68:71], a[100:103], a[128:131]\n" \
  "buffer_load_dwordx4 v248, s[16:19], 0 offen sc1 lds\n" \
  "v_add_f32 v19, 1.0, v19\n" \
  "v_mfma_f32_16x16x32_bf16 a[132:135], a[76:79], a[100:103], a[132:135]\n" \
  "ds_read_b128 a[36:39], v245 offset:0\n" \
  "v_rcp_f32 v18, v18\n" \
  "v_mul_f32 v17, v17, v49\n" \
  "v_mfma_f32_16x16x32_bf16 a[132:135], a[72:75], a[96:99], a[132:135]\n" \
  "s_add_u32 m0, m0, 2048\n" \
  "ds_read_b128 a[0:3], v240 offset:0\n" \
  "v_mul_f32 v82, s86, v82\n" \
  "v_mfma_f32_16x16x32_bf16 a[136:139], a[80:83], a[96:99], a[136:139]\n" \
  "buffer_load_dwordx4 v248, s[16:19], s79 offen sc1 lds\n" \
  "v_mfma_f32_16x16x32_bf16 a[136:139], a[84:87], a[100:103], a[136:139]\n" \
  "ds_read_b128 a[4:7], v241 offset:0\n" \
  "v_exp_f32 v81, v81\n" \
  "v_add_f32 v80, 1.0, v80\n" \
  "v_mfma_f32_16x16x32_bf16 a[140:143], a[92:95], a[100:103], a[140:143]\n" \
  "ds_read_b128 a[8:11], v240 offset:2048\n" \
  "v_rcp_f32 v19, v19\n" \
  "v_mul_f32 v18, v18, v50\n" \
  "v_mfma_f32_16x16x32_bf16 a[140:143], a[88:91], a[96:99], a[140:143]\n" \
  "s_add_u32 m0, m0, 2048\n" \
  "ds_read_b128 a[12:15], v241 offset:2048\n" \
  "v_mul_f32 v83, s86, v83\n" \
  "v_mfma_f32_16x16x32_bf16 a[144:147], a[64:67], a[104:107], a[144:147]\n" \
  "buffer_load_dwordx4 v248, s[16:19], s80 offen sc1 lds\n" \
  "v_mfma_f32_16x16x32_bf16 a[144:147], a[68:71], a[108:111], a[144:147]\n" \
  "ds_read_b128 a[16:19], v240 offset:4096\n" \
  "v_exp_f32 v82, v82\n" \
  "v_add_f32 v81, 1.0, v81\n" \
  "v_mfma_f32_16x16x32_bf16 a[148:151], a[76:79], a[108:111], a[148:151]\n" \
  "s_add_u32 m0, m0, 2048\n" \
  "ds_read_b128 a[20:23], v241 offset:4096\n" \
  "v_rcp_f32 v80, v80\n" \
  "v_mfma_f32_16x16x32_bf16 a[148:151], a[72:75], a[104:107], a[148:151]\n" \
  "buffer_load_dwordx4 v248, s[16:19], s81 offen sc1 lds\n" \
  "v_mul_f32 v19, v19, v51\n" \
  "v_mfma_f32_16x16x32_bf16 a[152:155], a[80:83], a[104:107], a[152:155]\n" \
  "ds_read_b128 a[24:27], v240 offset:6144\n" \
  "v_exp_f32 v83, v83\n" \
  "v_add_f32 v82, 1.0, v82\n" \
  "v_mfma_f32_16x16x32_bf16 a[152:155], a[84:87], a[108:111], a[152:155]\n" \
  "ds_read_b128 a[28:31], v241 offset:6144\n" \
  "v_rcp_f32 v81, v81\n" \
  "v_mul_f32 v80, v80, v112\n" \
  "v_mfma_f32_16x16x32_bf16 a[156:159], a[92:95], a[108:111], a[156:159]\n" \
  "s_add_u32 m0, s91, 65536\n" \
  "ds_read_b128 a[40:43], v244 offset:2048\n" \
  "v_add_f32 v83, 1.0, v83\n" \
  "v_mfma_f32_16x16x32_bf16 a[156:159], a[88:91], a[104:107], a[156:159]\n" \
  "buffer_load_dwordx4 v249, s[20:23], 0 offen lds\n" \
  "v_mfma_f32_16x16x32_bf16 a[160:163], a[64:67], a[112:115], a[160:163]\n" \
  "ds_read_b128 a[44:47], v245 offset:2048\n" \
  "v_rcp_f32 v82, v82\n" \
  "v_mul_f32 v81, v81, v113\n" \
  "v_mfma_f32_16x16x32_bf16 a[160:163], a[68:71], a[116:119], a[160:163]\n" \
  "s_add_u32 m0, m0, s82\n" \
  "v_rcp_f32 v83, v83\n" \
  "v_mul_f32 v82, v82, v114\n" \
  "v_mfma_f32_16x16x32_bf16 a[164:167], a[76:79], a[116:119], a[164:167]\n" \
  "buffer_load_dwordx4 v249, s[20:23], s72 offen lds\n" \
  "v_mul_f32 v83, v83, v115\n" \
  "v_mfma_f32_16x16x32_bf16 a[164:167], a[72:75], a[112:115], a[164:167]\n" \
  "ds_read_b128 a[48:51], v244 offset:4096\n" \
  "v_cvt_pk_bf16_f32 v48, v16, v17\n" \
  "v_cvt_pk_bf16_f32 v49, v18, v19\n" \
  "v_cvt_pk_bf16_f32 v50, v80, v81\n" \
  "v_mfma_f32_16x16x32_bf16 a[168:171], a[80:83], a[112:115], a[168:171]\n" \
  "ds_read_b128 a[52:55], v245 offset:4096\n" \
  "v_cvt_pk_bf16_f32 v51, v82, v83\n" \
  "s_mov_b32 s69, 458752\n" \
  "v_mfma_f32_16x16x32_bf16 a[168:171], a[84:87], a[116:119], a[168:171]\n" \
  "s_add_u32 m0, m0, s82\n" \
  "buffer_store_dwordx4 v[48:51], v250, s[24:27], s69 offen sc1\n" \
  "v_mfma_f32_16x16x32_bf16 a[172:175], a[92:95], a[116:119], a[172:175]\n" \
  "buffer_load_dwordx4 v249, s[20:23], s73 offen lds\n" \
  "v_mul_f32 v20, s86, v20\n" \
  "v_mfma_f32_16x16x32_bf16 a[172:175], a[88:91], a[112:115], a[172:175]\n" \
  "ds_read_b128 a[56:59], v244 offset:6144\n" \
  "v_mul_f32 v21, s86, v21\n" \
  "v_exp_f32 v20, v20\n" \
  "v_mfma_f32_16x16x32_bf16 a[176:179], a[64:67], a[120:123], a[176:179]\n" \
  "s_add_u32 m0, m0, s82\n" \
  "v_mul_f32 v22, s86, v22\n" \
  "v_exp_f32 v21, v21\n" \
  "v_mfma_f32_16x16x32_bf16 a[176:179], a[68:71], a[124:127], a[176:179]\n" \
  "buffer_load_dwordx4 v249, s[20:23], s74 offen lds\n" \
  "v_add_f32 v20, 1.0, v20\n" \
  "v_mfma_f32_16x16x32_bf16 a[180:183], a[76:79], a[124:127], a[180:183]\n" \
  "ds_read_b128 a[60:63], v245 offset:6144\n" \
  "v_mul_f32 v23, s86, v23\n" \
  "v_exp_f32 v22, v22\n" \
  "v_mfma_f32_16x16x32_bf16 a[180:183], a[72:75], a[120:123], a[180:183]\n" \
  "v_add_f32 v21, 1.0, v21\n" \
  "v_rcp_f32 v20, v20\n" \
  "v_mfma_f32_16x16x32_bf16 a[184:187], a[80:83], a[120:123], a[184:187]\n" \
  "s_sub_u32 s16, s16, 128\n" \
  "s_subb_u32 s17, s17, 0\n" \
  "v_mul_f32 v84, s86, v84\n" \
  "v_mfma_f32_16x16x32_bf16 a[184:187], a[84:87], a[124:127], a[184:187]\n" \
  "v_exp_f32 v23, v23\n" \
  "v_add_f32 v22, 1.0, v22\n" \
  "v_mfma_f32_16x16x32_bf16 a[188:191], a[92:95], a[124:127], a[188:191]\n" \
  "s_sub_u32 s20, s20, 128\n" \
  "s_subb_u32 s21, s21, 0\n" \
  "v_rcp_f32 v21, v21\n" \
  "v_mfma_f32_16x16x32_bf16 a[188:191], a[88:91], a[120:123], a[188:191]\n" \
  "v_mul_f32 v20, v20, v52\n" \
  "v_mul_f32 v85, s86, v85\n" \
  "s_waitcnt lgkmcnt(0)\n" \
  "s_waitcnt vmcnt(18)\n" \
  "s_barrier\n" \
  "v_mfma_f32_16x16x32_bf16 a[128:131], a[0:3], a[32:35], a[128:131]\n" \
  "s_add_u32 m0, s90, 0\n" \
  "ds_read_b128 a[96:99], v246 offset:32768\n" \
  "v_exp_f32 v84, v84\n" \
  "v_mfma_f32_16x16x32_bf16 a[128:131], a[4:7], a[36:39], a[128:131]\n" \
  "buffer_load_dwordx4 v248, s[16:19], 0 offen sc1 lds\n" \
  "v_add_f32 v23, 1.0, v23\n" \
  "v_mfma_f32_16x16x32_bf16 a[132:135], a[12:15], a[36:39], a[132:135]\n" \
  "ds_read_b128 a[100:103], v247 offset:32768\n" \
  "v_rcp_f32 v22, v22\n" \
  "v_mul_f32 v21, v21, v53\n" \
  "v_mfma_f32_16x16x32_bf16 a[132:135], a[8:11], a[32:35], a[132:135]\n" \
  "s_add_u32 m0, m0, 2048\n" \
  "ds_read_b128 a[64:67], v242 offset:32768\n" \
  "v_mul_f32 v86, s86, v86\n" \
  "v_mfma_f32_16x16x32_bf16 a[136:139], a[16:19], a[32:35], a[136:139]\n" \
  "buffer_load_dwordx4 v248, s[16:19], s79 offen sc1 lds\n" \
  "v_mfma_f32_16x16x32_bf16 a[136:139], a[20:23], a[36:39], a[136:139]\n" \
  "ds_read_b128 a[68:71], v243 offset:32768\n" \
  "v_exp_f32 v85, v85\n" \
  "v_add_f32 v84, 1.0, v84\n" \
  "v_mfma_f32_16x16x32_bf16 a[140:143], a[28:31], a[36:39], a[140:143]\n" \
  "ds_read_b128 a[72:75], v242 offset:34816\n" \
  "v_rcp_f32 v23, v23\n" \
  "v_mul_f32 v22, v22, v54\n" \
  "v_mfma_f32_16x16x32_bf16 a[140:143], a[24:27], a[32:35], a[140:143]\n" \
  "s_add_u32 m0, m0, 2048\n" \
  "ds_read_b128 a[76:79], v243 offset:34816\n" \
  "v_mul_f32 v87, s86, v87\n" \
  "v_mfma_f32_16x16x32_bf16 a[144:147], a[0:3], a[40:43], a[144:147]\n" \
  "buffer_load_dwordx4 v248, s[16:19], s80 offen sc1 lds\n" \
  "v_mfma_f32_16x16x32_bf16 a[144:147], a[4:7], a[44:47], a[144:147]\n" \
  "ds_read_b128 a[80:83], v242 offset:36864\n" \
  "v_exp_f32 v86, v86\n" \
  "v_add_f32 v85, 1.0, v85\n" \
  "v_mfma_f32_16x16x32_bf16 a[148:151], a[12:15], a[44:47], a[148:151]\n" \
  "s_add_u32 m0, m0, 2048\n" \
  "ds_read_b128 a[84:87], v243 offset:36864\n" \
  "v_rcp_f32 v84, v84\n" \
  "v_mfma_f32_16x16x32_bf16 a[148:151], a[8:11], a[40:43], a[148:151]\n" \
  "buffer_load_dwordx4 v248, s[16:19], s81 offen sc1 lds\n" \
  "v_mul_f32 v23, v23, v55\n" \
  "v_mfma_f32_16x16x32_bf16 a[152:155], a[16:19], a[40:43], a[152:155]\n" \
  "ds_read_b128 a[88:91], v242 offset:38912\n" \
  "v_exp_f32 v87, v87\n" \
  "v_add_f32 v86, 1.0, v86\n" \
  "v_mfma_f32_16x16x32_bf16 a[152:155], a[20:23], a[44:47], a[152:155]\n" \
  "ds_read_b128 a[92:95], v243 offset:38912\n" \
  "v_rcp_f32 v85, v85\n" \
  "v_mul_f32 v84, v84, v116\n" \
  "v_mfma_f32_16x16x32_bf16 a[156:159], a[28:31], a[44:47], a[156:159]\n" \
  "s_add_u32 m0, s91, 0\n" \
  "ds_read_b128 a[104:107], v246 offset:34816\n" \
  "v_add_f32 v87, 1.0, v87\n" \
  "v_mfma_f32_16x16x32_bf16 a[156:159], a[24:27], a[40:43], a[156:159]\n" \
  "buffer_load_dwordx4 v249, s[20:23], 0 offen lds\n" \
  "v_mfma_f32_16x16x32_bf16 a[160:163], a[0:3], a[48:51], a[160:163]\n" \
  "ds_read_b128 a[108:111], v247 offset:34816\n" \
  "v_rcp_f32 v86, v86\n" \
  "v_mul_f32 v85, v85, v117\n" \
  "v_mfma_f32_16x16x32_bf16 a[160:163], a[4:7], a[52:55], a[160:163]\n" \
  "s_add_u32 m0, m0, s82\n" \
  "v_rcp_f32 v87, v87\n" \
  "v_mul_f32 v86, v86, v118\n" \
  "v_mfma_f32_16x16x32_bf16 a[164:167], a[12:15], a[52:55], a[164:167]\n" \
  "buffer_load_dwordx4 v249, s[20:23], s72 offen lds\n" \
  "v_mul_f32 v87, v87, v119\n" \
  "v_mfma_f32_16x16x32_bf16 a[164:167], a[8:11], a[48:51], a[164:167]\n" \
  "ds_read_b128 a[112:115], v246 offset:36864\n" \
  "v_cvt_pk_bf16_f32 v52, v20, v21\n" \
  "v_cvt_pk_bf16_f32 v53, v22, v23\n" \
  "v_cvt_pk_bf16_f32 v54, v84, v85\n" \
  "v_mfma_f32_16x16x32_bf16 a[168:171], a[16:19], a[48:51], a[168:171]\n" \
  "ds_read_b128 a[116:119], v247 offset:36864\n" \
  "v_cvt_pk_bf16_f32 v55, v86, v87\n" \
  "s_mov_b32 s69, 573440\n" \
  "v_mfma_f32_16x16x32_bf16 a[168:171], a[20:23], a[52:55], a[168:171]\n" \
  "s_add_u32 m0, m0, s82\n" \
  "buffer_store_dwordx4 v[52:55], v250, s[24:27], s69 offen sc1\n" \
  "v_mfma_f32_16x16x32_bf16 a[172:175], a[28:31], a[52:55], a[172:175]\n" \
  "buffer_load_dwordx4 v249, s[20:23], s73 offen lds\n" \
  "v_mul_f32 v24, s86, v24\n" \
  "v_mfma_f32_16x16x32_bf16 a[172:175], a[24:27], a[48:51], a[172:175]\n" \
  "ds_read_b128 a[120:123], v246 offset:38912\n" \
  "v_mul_f32 v25, s86, v25\n" \
  "v_exp_f32 v24, v24\n" \
  "v_mfma_f32_16x16x32_bf16 a[176:179], a[0:3], a[56:59], a[176:179]\n" \
  "s_add_u32 m0, m0, s82\n" \
  "v_mul_f32 v26, s86, v26\n" \
  "v_exp_f32 v25, v25\n" \
  "v_mfma_f32_16x16x32_bf16 a[176:179], a[4:7], a[60:63], a[176:179]\n" \
  "buffer_load_dwordx4 v249, s[20:23], s74 offen lds\n" \
  "v_add_f32 v24, 1.0, v24\n" \
  "v_mfma_f32_16x16x32_bf16 a[180:183], a[12:15], a[60:63], a[180:183]\n" \
  "ds_read_b128 a[124:127], v247 offset:38912\n" \
  "v_mul_f32 v27, s86, v27\n" \
  "v_exp_f32 v26, v26\n" \
  "v_mfma_f32_16x16x32_bf16 a[180:183], a[8:11], a[56:59], a[180:183]\n" \
  "v_add_f32 v25, 1.0, v25\n" \
  "v_rcp_f32 v24, v24\n" \
  "v_mfma_f32_16x16x32_bf16 a[184:187], a[16:19], a[56:59], a[184:187]\n" \
  "s_sub_u32 s16, s16, 128\n" \
  "s_subb_u32 s17, s17, 0\n" \
  "v_mul_f32 v88, s86, v88\n" \
  "v_mfma_f32_16x16x32_bf16 a[184:187], a[20:23], a[60:63], a[184:187]\n" \
  "v_exp_f32 v27, v27\n" \
  "v_add_f32 v26, 1.0, v26\n" \
  "v_mfma_f32_16x16x32_bf16 a[188:191], a[28:31], a[60:63], a[188:191]\n" \
  "s_sub_u32 s20, s20, 128\n" \
  "s_subb_u32 s21, s21, 0\n" \
  "v_rcp_f32 v25, v25\n" \
  "v_mfma_f32_16x16x32_bf16 a[188:191], a[24:27], a[56:59], a[188:191]\n" \
  "v_mul_f32 v24, v24, v56\n" \
  "v_mul_f32 v89, s86, v89\n" \
  "s_waitcnt lgkmcnt(0)\n" \
  "s_waitcnt vmcnt(18)\n" \
  "s_barrier\n" \
  "v_mfma_f32_16x16x32_bf16 a[128:131], a[64:67], a[96:99], a[128:131]\n" \
  "s_add_u32 m0, s90, 98304\n" \
  "ds_read_b128 a[32:35], v244 offset:32768\n" \
  "v_exp_f32 v88, v88\n" \
  "v_mfma_f32_16x16x32_bf16 a[128:131], a[68:71], a[100:103], a[128:131]\n" \
  "buffer_load_dwordx4 v248, s[16:19], 0 offen sc1 lds\n" \
  "v_add_f32 v27, 1.0, v27\n" \
  "v_mfma_f32_16x16x32_bf16 a[132:135], a[76:79], a[100:103], a[132:135]\n" \
  "ds_read_b128 a[36:39], v245 offset:32768\n" \
  "v_rcp_f32 v26, v26\n" \
  "v_mul_f32 v25, v25, v57\n" \
  "v_mfma_f32_16x16x32_bf16 a[132:135], a[72:75], a[96:99], a[132:135]\n" \
  "s_add_u32 m0, m0, 2048\n" \
  "ds_read_b128 a[0:3], v240 offset:32768\n" \
  "v_mul_f32 v90, s86, v90\n" \
  "v_mfma_f32_16x16x32_bf16 a[136:139], a[80:83], a[96:99], a[136:139]\n" \
  "buffer_load_dwordx4 v248, s[16:19], s79 offen sc1 lds\n" \
  "v_mfma_f32_16x16x32_bf16 a[136:139], a[84:87], a[100:103], a[136:139]\n" \
  "ds_read_b128 a[4:7], v241 offset:32768\n" \
  "v_exp_f32 v89, v89\n" \
  "v_add_f32 v88, 1.0, v88\n" \
  "v_mfma_f32_16x16x32_bf16 a[140:143], a[92:95], a[100:103], a[140:143]\n" \
  "ds_read_b128 a[8:11], v240 offset:34816\n" \
  "v_rcp_f32 v27, v27\n" \
  "v_mul_f32 v26, v26, v58\n" \
  "v_mfma_f32_16x16x32_bf16 a[140:143], a[88:91], a[96:99], a[140:143]\n" \
  "s_add_u32 m0, m0, 2048\n" \
  "ds_read_b128 a[12:15], v241 offset:34816\n" \
  "v_mul_f32 v91, s86, v91\n" \
  "v_mfma_f32_16x16x32_bf16 a[144:147], a[64:67], a[104:107], a[144:147]\n" \
  "buffer_load_dwordx4 v248, s[16:19], s80 offen sc1 lds\n" \
  "v_mfma_f32_16x16x32_bf16 a[144:147], a[68:71], a[108:111], a[144:147]\n" \
  "ds_read_b128 a[16:19], v240 offset:36864\n" \
  "v_exp_f32 v90, v90\n" \
  "v_add_f32 v89, 1.0, v89\n" \
  "v_mfma_f32_16x16x32_bf16 a[148:151], a[76:79], a[108:111], a[148:151]\n" \
  "s_add_u32 m0, m0, 2048\n" \
  "ds_read_b128 a[20:23], v241 offset:36864\n" \
  "v_rcp_f32 v88, v88\n" \
  "v_mfma_f32_16x16x32_bf16 a[148:151], a[72:75], a[104:107], a[148:151]\n" \
  "buffer_load_dwordx4 v248, s[16:19], s81 offen sc1 lds\n" \
  "v_mul_f32 v27, v27, v59\n" \
  "v_mfma_f32_16x16x32_bf16 a[152:155], a[80:83], a[104:107], a[152:155]\n" \
  "ds_read_b128 a[24:27], v240 offset:38912\n" \
  "v_exp_f32 v91, v91\n" \
  "v_add_f32 v90, 1.0, v90\n" \
  "v_mfma_f32_16x16x32_bf16 a[152:155], a[84:87], a[108:111], a[152:155]\n" \
  "ds_read_b128 a[28:31], v241 offset:38912\n" \
  "v_rcp_f32 v89, v89\n" \
  "v_mul_f32 v88, v88, v120\n" \
  "v_mfma_f32_16x16x32_bf16 a[156:159], a[92:95], a[108:111], a[156:159]\n" \
  "s_add_u32 m0, s91, 98304\n" \
  "ds_read_b128 a[40:43], v244 offset:34816\n" \
  "v_add_f32 v91, 1.0, v91\n" \
  "v_mfma_f32_16x16x32_bf16 a[156:159], a[88:91], a[104:107], a[156:159]\n" \
  "buffer_load_dwordx4 v249, s[20:23], 0 offen lds\n" \
  "v_mfma_f32_16x16x32_bf16 a[160:163], a[64:67], a[112:115], a[160:163]\n" \
  "ds_read_b128 a[44:47], v245 offset:34816\n" \
  "v_rcp_f32 v90, v90\n" \
  "v_mul_f32 v89, v89, v121\n" \
  "v_mfma_f32_16x16x32_bf16 a[160:163], a[68:71], a[116:119], a[160:163]\n" \
  "s_add_u32 m0, m0, s82\n" \
  "v_rcp_f32 v91, v91\n" \
  "v_mul_f32 v90, v90, v122\n" \
  "v_mfma_f32_16x16x32_bf16 a[164:167], a[76:79], a[116:119], a[164:167]\n" \
  "buffer_load_dwordx4 v249, s[20:23], s72 offen lds\n" \
  "v_mul_f32 v91, v91, v123\n" \
  "v_mfma_f32_16x16x32_bf16 a[164:167], a[72:75], a[112:115], a[164:167]\n" \
  "ds_read_b128 a[48:51], v244 offset:36864\n" \
  "v_cvt_pk_bf16_f32 v56, v24, v25\n" \
  "v_cvt_pk_bf16_f32 v57, v26, v27\n" \
  "v_cvt_pk_bf16_f32 v58, v88, v89\n" \
  "v_mfma_f32_16x16x32_bf16 a[168:171], a[80:83], a[112:115], a[168:171]\n" \
  "ds_read_b128 a[52:55], v245 offset:36864\n" \
  "v_cvt_pk_bf16_f32 v59, v90, v91\n" \
  "s_mov_b32 s69, 688128\n" \
  "v_mfma_f32_16x16x32_bf16 a[168:171], a[84:87], a[116:119], a[168:171]\n" \
  "s_add_u32 m0, m0, s82\n" \
  "buffer_store_dwordx4 v[56:59], v250, s[24:27], s69 offen sc1\n" \
  "v_mfma_f32_16x16x32_bf16 a[172:175], a[92:95], a[116:119], a[172:175]\n" \
  "buffer_load_dwordx4 v249, s[20:23], s73 offen lds\n" \
  "v_mul_f32 v28, s86, v28\n" \
  "v_mfma_f32_16x16x32_bf16 a[172:175], a[88:91], a[112:115], a[172:175]\n" \
  "ds_read_b128 a[56:59], v244 offset:38912\n" \
  "v_mul_f32 v29, s86, v29\n" \
  "v_exp_f32 v28, v28\n" \
  "v_mfma_f32_16x16x32_bf16 a[176:179], a[64:67], a[120:123], a[176:179]\n" \
  "s_add_u32 m0, m0, s82\n" \
  "v_mul_f32 v30, s86, v30\n" \
  "v_exp_f32 v29, v29\n" \
  "v_mfma_f32_16x16x32_bf16 a[176:179], a[68:71], a[124:127], a[176:179]\n" \
  "buffer_load_dwordx4 v249, s[20:23], s74 offen lds\n" \
  "v_add_f32 v28, 1.0, v28\n" \
  "v_mfma_f32_16x16x32_bf16 a[180:183], a[76:79], a[124:127], a[180:183]\n" \
  "ds_read_b128 a[60:63], v245 offset:38912\n" \
  "v_mul_f32 v31, s86, v31\n" \
  "v_exp_f32 v30, v30\n" \
  "v_mfma_f32_16x16x32_bf16 a[180:183], a[72:75], a[120:123], a[180:183]\n" \
  "v_add_f32 v29, 1.0, v29\n" \
  "v_rcp_f32 v28, v28\n" \
  "v_mfma_f32_16x16x32_bf16 a[184:187], a[80:83], a[120:123], a[184:187]\n" \
  "s_sub_u32 s16, s16, 128\n" \
  "s_subb_u32 s17, s17, 0\n" \
  "v_mul_f32 v92, s86, v92\n" \
  "v_mfma_f32_16x16x32_bf16 a[184:187], a[84:87], a[124:127], a[184:187]\n" \
  "v_exp_f32 v31, v31\n" \
  "v_add_f32 v30, 1.0, v30\n" \
  "v_mfma_f32_16x16x32_bf16 a[188:191], a[92:95], a[124:127], a[188:191]\n" \
  "s_sub_u32 s20, s20, 128\n" \
  "s_subb_u32 s21, s21, 0\n" \
  "v_rcp_f32 v29, v29\n" \
  "v_mfma_f32_16x16x32_bf16 a[188:191], a[88:91], a[120:123], a[188:191]\n" \
  "v_mul_f32 v28, v28, v60\n" \
  "v_mul_f32 v93, s86, v93\n" \
  "s_waitcnt lgkmcnt(0)\n" \
  "s_waitcnt vmcnt(18)\n" \
  "s_barrier\n" \
  "v_mfma_f32_16x16x32_bf16 a[128:131], a[0:3], a[32:35], a[128:131]\n" \
  "s_add_u32 m0, s90, 32768\n" \
  "ds_read_b128 a[96:99], v246 offset:0\n" \
  "v_exp_f32 v92, v92\n" \
  "v_mfma_f32_16x16x32_bf16 a[128:131], a[4:7], a[36:39], a[128:131]\n" \
  "buffer_load_dwordx4 v248, s[16:19], 0 offen sc1 lds\n" \
  "v_add_f32 v31, 1.0, v31\n" \
  "v_mfma_f32_16x16x32_bf16 a[132:135], a[12:15], a[36:39], a[132:135]\n" \
  "ds_read_b128 a[100:103], v247 offset:0\n" \
  "v_rcp_f32 v30, v30\n" \
  "v_mul_f32 v29, v29, v61\n" \
  "v_mfma_f32_16x16x32_bf16 a[132:135], a[8:11], a[32:35], a[132:135]\n" \
  "s_add_u32 m0, m0, 2048\n" \
  "ds_read_b128 a[64:67], v242 offset:0\n" \
  "v_mul_f32 v94, s86, v94\n" \
  "v_mfma_f32_16x16x32_bf16 a[136:139], a[16:19], a[32:35], a[136:139]\n" \
  "buffer_load_dwordx4 v248, s[16:19], s79 offen sc1 lds\n" \
  "v_mfma_f32_16x16x32_bf16 a[136:139], a[20:23], a[36:39], a[136:139]\n" \
  "ds_read_b128 a[68:71], v243 offset:0\n" \
  "v_exp_f32 v93, v93\n" \
  "v_add_f32 v92, 1.0, v92\n" \
  "v_mfma_f32_16x16x32_bf16 a[140:143], a[28:31], a[36:39], a[140:143]\n" \
  "ds_read_b128 a[72:75], v242 offset:2048\n" \
  "v_rcp_f32 v31, v31\n" \
  "v_mul_f32 v30, v30, v62\n" \
  "v_mfma_f32_16x16x32_bf16 a[140:143], a[24:27], a[32:35], a[140:143]\n" \
  "s_add_u32 m0, m0, 2048\n" \
  "ds_read_b128 a[76:79], v243 offset:2048\n" \
  "v_mul_f32 v95, s86, v95\n" \
  "v_mfma_f32_16x16x32_bf16 a[144:147], a[0:3], a[40:43], a[144:147]\n" \
  "buffer_load_dwordx4 v248, s[16:19], s80 offen sc1 lds\n" \
  "v_mfma_f32_16x16x32_bf16 a[144:147], a[4:7], a[44:47], a[144:147]\n" \
  "ds_read_b128 a[80:83], v242 offset:4096\n" \
  "v_exp_f32 v94, v94\n" \
  "v_add_f32 v93, 1.0, v93\n" \
  "v_mfma_f32_16x16x32_bf16 a[148:151], a[12:15], a[44:47], a[148:151]\n" \
  "s_add_u32 m0, m0, 2048\n" \
  "ds_read_b128 a[84:87], v243 offset:4096\n" \
  "v_rcp_f32 v92, v92\n" \
  "v_mfma_f32_16x16x32_bf16 a[148:151], a[8:11], a[40:43], a[148:151]\n" \
  "buffer_load_dwordx4 v248, s[16:19], s81 offen sc1 lds\n" \
  "v_mul_f32 v31, v31, v63\n" \
  "v_mfma_f32_16x16x32_bf16 a[152:155], a[16:19], a[40:43], a[152:155]\n" \
  "ds_read_b128 a[88:91], v242 offset:6144\n" \
  "v_exp_f32 v95, v95\n" \
  "v_add_f32 v94, 1.0, v94\n" \
  "v_mfma_f32_16x16x32_bf16 a[152:155], a[20:23], a[44:47], a[152:155]\n" \
  "ds_read_b128 a[92:95], v243 offset:6144\n" \
  "v_rcp_f32 v93, v93\n" \
  "v_mul_f32 v92, v92, v124\n" \
  "v_mfma_f32_16x16x32_bf16 a[156:159], a[28:31], a[44:47], a[156:159]\n" \
  "s_add_u32 m0, s91, 32768\n" \
  "ds_read_b128 a[104:107], v246 offset:2048\n" \
  "v_add_f32 v95, 1.0, v95\n" \
  "v_mfma_f32_16x16x32_bf16 a[156:159], a[24:27], a[40:43], a[156:159]\n" \
  "buffer_load_dwordx4 v249, s[20:23], 0 offen lds\n" \
  "v_mfma_f32_16x16x32_bf16 a[160:163], a[0:3], a[48:51], a[160:163]\n" \
  "ds_read_b128 a[108:111], v247 offset:2048\n" \
  "v_rcp_f32 v94, v94\n" \
  "v_mul_f32 v93, v93, v125\n" \
  "v_mfma_f32_16x16x32_bf16 a[160:163], a[4:7], a[52:55], a[160:163]\n" \
  "s_add_u32 m0, m0, s82\n" \
  "v_rcp_f32 v95, v95\n" \
  "v_mul_f32 v94, v94, v126\n" \
  "v_mfma_f32_16x16x32_bf16 a[164:167], a[12:15], a[52:55], a[164:167]\n" \
  "buffer_load_dwordx4 v249, s[20:23], s72 offen lds\n" \
  "v_mul_f32 v95, v95, v127\n" \
  "v_mfma_f32_16x16x32_bf16 a[164:167], a[8:11], a[48:51], a[164:167]\n" \
  "ds_read_b128 a[112:115], v246 offset:4096\n" \
  "v_cvt_pk_bf16_f32 v60, v28, v29\n" \
  "v_cvt_pk_bf16_f32 v61, v30, v31\n" \
  "v_cvt_pk_bf16_f32 v62, v92, v93\n" \
  "v_mfma_f32_16x16x32_bf16 a[168:171], a[16:19], a[48:51], a[168:171]\n" \
  "ds_read_b128 a[116:119], v247 offset:4096\n" \
  "v_cvt_pk_bf16_f32 v63, v94, v95\n" \
  "s_mov_b32 s69, 802816\n" \
  "v_mfma_f32_16x16x32_bf16 a[168:171], a[20:23], a[52:55], a[168:171]\n" \
  "s_add_u32 m0, m0, s82\n" \
  "buffer_store_dwordx4 v[60:63], v250, s[24:27], s69 offen sc1\n" \
  "v_mfma_f32_16x16x32_bf16 a[172:175], a[28:31], a[52:55], a[172:175]\n" \
  "buffer_load_dwordx4 v249, s[20:23], s73 offen lds\n" \
  "v_accvgpr_read_b32 v224, a192\n" \
  "v_mfma_f32_16x16x32_bf16 a[172:175], a[24:27], a[48:51], a[172:175]\n" \
  "ds_read_b128 a[120:123], v246 offset:6144\n" \
  "v_add_u32 v225, 65536, v224\n" \
  "v_accvgpr_read_b32 v226, a193\n" \
  "v_add_u32 v230, 65536, v226\n" \
  "v_mfma_f32_16x16x32_bf16 a[176:179], a[0:3], a[56:59], a[176:179]\n" \
  "s_add_u32 m0, m0, s82\n" \
  "v_accvgpr_read_b32 v227, a194\n" \
  "v_add_u32 v231, 65536, v227\n" \
  "v_accvgpr_read_b32 v228, a195\n" \
  "v_mfma_f32_16x16x32_bf16 a[176:179], a[4:7], a[60:63], a[176:179]\n" \
  "buffer_load_dwordx4 v249, s[20:23], s74 offen lds\n" \
  "v_add_u32 v232, 65536, v228\n" \
  "v_mfma_f32_16x16x32_bf16 a[180:183], a[12:15], a[60:63], a[180:183]\n" \
  "ds_read_b128 a[124:127], v247 offset:6144\n" \
  "v_accvgpr_read_b32 v229, a196\n" \
  "v_add_u32 v233, 65536, v229\n" \
  "v_accvgpr_read_b32 v234, a197\n" \
  "v_mfma_f32_16x16x32_bf16 a[180:183], a[8:11], a[56:59], a[180:183]\n" \
  "v_accvgpr_read_b32 v235, a198\n" \
  "v_mul_f32 v128, s86, v128\n" \
  "v_mul_f32 v129, s86, v129\n" \
  "v_mfma_f32_16x16x32_bf16 a[184:187], a[16:19], a[56:59], a[184:187]\n" \
  "s_sub_u32 s16, s16, 128\n" \
  "s_subb_u32 s17, s17, 0\n" \
  "v_exp_f32 v128, v128\n" \
  "v_mfma_f32_16x16x32_bf16 a[184:187], a[20:23], a[60:63], a[184:187]\n" \
  "v_mul_f32 v130, s86, v130\n" \
  "v_mfma_f32_16x16x32_bf16 a[188:191], a[28:31], a[60:63], a[188:191]\n" \
  "s_sub_u32 s20, s20, 128\n" \
  "s_subb_u32 s21, s21, 0\n" \
  "v_exp_f32 v129, v129\n" \
  "v_mfma_f32_16x16x32_bf16 a[188:191], a[24:27], a[56:59], a[188:191]\n" \
  "v_add_f32 v128, 1.0, v128\n" \
  "v_mul_f32 v131, s86, v131\n" \
  "v_exp_f32 v130, v130\n" \
  "s_waitcnt lgkmcnt(0)\n" \
  "s_waitcnt vmcnt(18)\n" \
  "s_barrier\n" \
  "v_mfma_f32_16x16x32_bf16 a[128:131], a[64:67], a[96:99], a[128:131]\n" \
  "s_add_u32 m0, s90, 65536\n" \
  "ds_read_b128 a[32:35], v244 offset:0\n" \
  "v_add_f32 v129, 1.0, v129\n" \
  "v_mfma_f32_16x16x32_bf16 a[128:131], a[68:71], a[100:103], a[128:131]\n" \
  "buffer_load_dwordx4 v248, s[16:19], 0 offen sc1 lds\n" \
  "v_mfma_f32_16x16x32_bf16 a[132:135], a[76:79], a[100:103], a[132:135]\n" \
  "ds_read_b128 a[36:39], v245 offset:0\n" \
  "v_rcp_f32 v128, v128\n" \
  "v_mul_f32 v132, s86, v132\n" \
  "v_mfma_f32_16x16x32_bf16 a[132:135], a[72:75], a[96:99], a[132:135]\n" \
  "s_add_u32 m0, m0, 2048\n" \
  "ds_read_b128 a[0:3], v240 offset:0\n" \
  "v_exp_f32 v131, v131\n" \
  "v_mfma_f32_16x16x32_bf16 a[136:139], a[80:83], a[96:99], a[136:139]\n" \
  "buffer_load_dwordx4 v248, s[16:19], s79 offen sc1 lds\n" \
  "v_add_f32 v130, 1.0, v130\n" \
  "v_mfma_f32_16x16x32_bf16 a[136:139], a[84:87], a[100:103], a[136:139]\n" \
  "ds_read_b128 a[4:7], v241 offset:0\n" \
  "v_rcp_f32 v129, v129\n" \
  "v_mul_f32 v128, v128, v160\n" \
  "v_mfma_f32_16x16x32_bf16 a[140:143], a[92:95], a[100:103], a[140:143]\n" \
  "ds_read_b128 a[8:11], v240 offset:2048\n" \
  "v_mul_f32 v133, s86, v133\n" \
  "v_exp_f32 v132, v132\n" \
  "v_mfma_f32_16x16x32_bf16 a[140:143], a[88:91], a[96:99], a[140:143]\n" \
  "s_add_u32 m0, m0, 2048\n" \
  "ds_read_b128 a[12:15], v241 offset:2048\n" \
  "v_add_f32 v131, 1.0, v131\n" \
  "v_mfma_f32_16x16x32_bf16 a[144:147], a[64:67], a[104:107], a[144:147]\n" \
  "buffer_load_dwordx4 v248, s[16:19], s80 offen sc1 lds\n" \
  "v_mfma_f32_16x16x32_bf16 a[144:147], a[68:71], a[108:111], a[144:147]\n" \
  "ds_read_b128 a[16:19], v240 offset:4096\n" \
  "v_rcp_f32 v130, v130\n" \
  "v_mul_f32 v129, v129, v161\n" \
  "v_mfma_f32_16x16x32_bf16 a[148:151], a[76:79], a[108:111], a[148:151]\n" \
  "s_add_u32 m0, m0, 2048\n" \
  "ds_read_b128 a[20:23], v241 offset:4096\n" \
  "v_mul_f32 v134, s86, v134\n" \
  "v_mfma_f32_16x16x32_bf16 a[148:151], a[72:75], a[104:107], a[148:151]\n" \
  "buffer_load_dwordx4 v248, s[16:19], s81 offen sc1 lds\n" \
  "v_mfma_f32_16x16x32_bf16 a[152:155], a[80:83], a[104:107], a[152:155]\n" \
  "ds_read_b128 a[24:27], v240 offset:6144\n" \
  "v_exp_f32 v133, v133\n" \
  "v_add_f32 v132, 1.0, v132\n" \
  "v_mfma_f32_16x16x32_bf16 a[152:155], a[84:87], a[108:111], a[152:155]\n" \
  "ds_read_b128 a[28:31], v241 offset:6144\n" \
  "v_rcp_f32 v131, v131\n" \
  "v_mul_f32 v130, v130, v162\n" \
  "v_mfma_f32_16x16x32_bf16 a[156:159], a[92:95], a[108:111], a[156:159]\n" \
  "s_add_u32 m0, s91, 65536\n" \
  "ds_read_b128 a[40:43], v244 offset:2048\n" \
  "v_mul_f32 v135, s86, v135\n" \
  "v_mfma_f32_16x16x32_bf16 a[156:159], a[88:91], a[104:107], a[156:159]\n" \
  "buffer_load_dwordx4 v249, s[20:23], 0 offen lds\n" \
  "v_mfma_f32_16x16x32_bf16 a[160:163], a[64:67], a[112:115], a[160:163]\n" \
  "ds_read_b128 a[44:47], v245 offset:2048\n" \
  "v_exp_f32 v134, v134\n" \
  "v_add_f32 v133, 1.0, v133\n" \
  "v_mfma_f32_16x16x32_bf16 a[160:163], a[68:71], a[116:119], a[160:163]\n" \
  "s_add_u32 m0, m0, s82\n" \
  "v_rcp_f32 v132, v132\n" \
  "v_mul_f32 v131, v131, v163\n" \
  "v_mfma_f32_16x16x32_bf16 a[164:167], a[76:79], a[116:119], a[164:167]\n" \
  "buffer_load_dwordx4 v249, s[20:23], s72 offen lds\n" \
  "v_mfma_f32_16x16x32_bf16 a[164:167], a[72:75], a[112:115], a[164:167]\n" \
  "ds_read_b128 a[48:51], v244 offset:4096\n" \
  "v_exp_f32 v135, v135\n" \
  "v_add_f32 v134, 1.0, v134\n" \
  "v_mfma_f32_16x16x32_bf16 a[168:171], a[80:83], a[112:115], a[168:171]\n" \
  "ds_read_b128 a[52:55], v245 offset:4096\n" \
  "v_rcp_f32 v133, v133\n" \
  "v_mul_f32 v132, v132, v164\n" \
  "v_mfma_f32_16x16x32_bf16 a[168:171], a[84:87], a[116:119], a[168:171]\n" \
  "s_add_u32 m0, m0, s82\n" \
  "v_add_f32 v135, 1.0, v135\n" \
  "v_mfma_f32_16x16x32_bf16 a[172:175], a[92:95], a[116:119], a[172:175]\n" \
  "buffer_load_dwordx4 v249, s[20:23], s73 offen lds\n" \
  "v_mfma_f32_16x16x32_bf16 a[172:175], a[88:91], a[112:115], a[172:175]\n" \
  "ds_read_b128 a[56:59], v244 offset:6144\n" \
  "v_rcp_f32 v134, v134\n" \
  "v_mul_f32 v133, v133, v165\n" \
  "v_mfma_f32_16x16x32_bf16 a[176:179], a[64:67], a[120:123], a[176:179]\n" \
  "s_add_u32 m0, m0, s82\n" \
  "v_rcp_f32 v135, v135\n" \
  "v_mul_f32 v134, v134, v166\n" \
  "v_mfma_f32_16x16x32_bf16 a[176:179], a[68:71], a[124:127], a[176:179]\n" \
  "buffer_load_dwordx4 v249, s[20:23], s74 offen lds\n" \
  "v_mul_f32 v135, v135, v167\n" \
  "v_mfma_f32_16x16x32_bf16 a[180:183], a[76:79], a[124:127], a[180:183]\n" \
  "ds_read_b128 a[60:63], v245 offset:6144\n" \
  "v_cvt_pk_bf16_f32 v160, v128, v129\n" \
  "v_cvt_pk_bf16_f32 v161, v130, v131\n" \
  "v_cvt_pk_bf16_f32 v162, v132, v133\n" \
  "v_mfma_f32_16x16x32_bf16 a[180:183], a[72:75], a[120:123], a[180:183]\n" \
  "v_cvt_pk_bf16_f32 v163, v134, v135\n" \
  "s_mov_b32 s69, 0\n" \
  "v_permlane16_swap_b32 v160, v162\n" \
  "v_mfma_f32_16x16x32_bf16 a[184:187], a[80:83], a[120:123], a[184:187]\n" \
  "s_sub_u32 s16, s16, 128\n" \
  "s_subb_u32 s17, s17, 0\n" \
  "v_permlane16_swap_b32 v161, v163\n" \
  "v_mfma_f32_16x16x32_bf16 a[184:187], a[84:87], a[124:127], a[184:187]\n" \
  "buffer_store_dwordx4 v[160:163], v251, s[24:27], s69 offen sc1\n" \
  "v_mul_f32 v136, s86, v136\n" \
  "v_mfma_f32_16x16x32_bf16 a[188:191], a[92:95], a[124:127], a[188:191]\n" \
  "s_sub_u32 s20, s20, 128\n" \
  "s_subb_u32 s21, s21, 0\n" \
  "v_mul_f32 v137, s86, v137\n" \
  "v_mfma_f32_16x16x32_bf16 a[188:191], a[88:91], a[120:123], a[188:191]\n" \
  "v_exp_f32 v136, v136\n" \
  "v_mul_f32 v138, s86, v138\n" \
  "s_waitcnt lgkmcnt(0)\n" \
  "s_waitcnt vmcnt(18)\n" \
  "s_barrier\n" \
  "v_mfma_f32_16x16x32_bf16 a[128:131], a[0:3], a[32:35], a[128:131]\n" \
  "s_add_u32 m0, s90, 0\n" \
  "ds_read_b128 a[96:99], v246 offset:32768\n" \
  "v_exp_f32 v137, v137\n" \
  "v_mfma_f32_16x16x32_bf16 a[128:131], a[4:7], a[36:39], a[128:131]\n" \
  "buffer_load_dwordx4 v248, s[16:19], 0 offen sc1 lds\n" \
  "v_add_f32 v136, 1.0, v136\n" \
  "v_mfma_f32_16x16x32_bf16 a[132:135], a[12:15], a[36:39], a[132:135]\n" \
  "ds_read_b128 a[100:103], v247 offset:32768\n" \
  "v_mul_f32 v139, s86, v139\n" \
  "v_mfma_f32_16x16x32_bf16 a[132:135], a[8:11], a[32:35], a[132:135]\n" \
  "s_add_u32 m0, m0, 2048\n" \
  "ds_read_b128 a[64:67], v242 offset:32768\n" \
  "v_exp_f32 v138, v138\n" \
  "v_mfma_f32_16x16x32_bf16 a[136:139], a[16:19], a[32:35], a[136:139]\n" \
  "buffer_load_dwordx4 v248, s[16:19], s79 offen sc1 lds\n" \
  "v_add_f32 v137, 1.0, v137\n" \
  "v_mfma_f32_16x16x32_bf16 a[136:139], a[20:23], a[36:39], a[136:139]\n" \
  "ds_read_b128 a[68:71], v243 offset:32768\n" \
  "v_rcp_f32 v136, v136\n" \
  "v_mul_f32 v140, s86, v140\n" \
  "v_mfma_f32_16x16x32_bf16 a[140:143], a[28:31], a[36:39], a[140:143]\n" \
  "ds_read_b128 a[72:75], v242 offset:34816\n" \
  "v_mfma_f32_16x16x32_bf16 a[140:143], a[24:27], a[32:35], a[140:143]\n" \
  "s_add_u32 m0, m0, 2048\n" \
  "ds_read_b128 a[76:79], v243 offset:34816\n" \
  "v_exp_f32 v139, v139\n" \
  "v_mfma_f32_16x16x32_bf16 a[144:147], a[0:3], a[40:43], a[144:147]\n" \
  "buffer_load_dwordx4 v248, s[16:19], s80 offen sc1 lds\n" \
  "v_add_f32 v138, 1.0, v138\n" \
  "v_mfma_f32_16x16x32_bf16 a[144:147], a[4:7], a[44:47], a[144:147]\n" \
  "ds_read_b128 a[80:83], v242 offset:36864\n" \
  "v_rcp_f32 v137, v137\n" \
  "v_mul_f32 v136, v136, v168\n" \
  "v_mfma_f32_16x16x32_bf16 a[148:151], a[12:15], a[44:47], a[148:151]\n" \
  "s_add_u32 m0, m0, 2048\n" \
  "ds_read_b128 a[84:87], v243 offset:36864\n" \
  "v_mul_f32 v141, s86, v141\n" \
  "v_mfma_f32_16x16x32_bf16 a[148:151], a[8:11], a[40:43], a[148:151]\n" \
  "buffer_load_dwordx4 v248, s[16:19], s81 offen sc1 lds\n" \
  "v_mfma_f32_16x16x32_bf16 a[152:155], a[16:19], a[40:43], a[152:155]\n" \
  "ds_read_b128 a[88:91], v242 offset:38912\n" \
  "v_exp_f32 v140, v140\n" \
  "v_add_f32 v139, 1.0, v139\n" \
  "v_mfma_f32_16x16x32_bf16 a[152:155], a[20:23], a[44:47], a[152:155]\n" \
  "ds_read_b128 a[92:95], v243 offset:38912\n" \
  "v_rcp_f32 v138, v138\n" \
  "v_mfma_f32_16x16x32_bf16 a[156:159], a[28:31], a[44:47], a[156:159]\n" \
  "s_add_u32 m0, s91, 0\n" \
  "ds_read_b128 a[104:107], v246 offset:34816\n" \
  "v_mul_f32 v137, v137, v169\n" \
  "v_mul_f32 v142, s86, v142\n" \
  "v_mfma_f32_16x16x32_bf16 a[156:159], a[24:27], a[40:43], a[156:159]\n" \
  "buffer_load_dwordx4 v249, s[20:23], 0 offen lds\n" \
  "v_mfma_f32_16x16x32_bf16 a[160:163], a[0:3], a[48:51], a[160:163]\n" \
  "ds_read_b128 a[108:111], v247 offset:34816\n" \
  "v_exp_f32 v141, v141\n" \
  "v_add_f32 v140, 1.0, v140\n" \
  "v_mfma_f32_16x16x32_bf16 a[160:163], a[4:7], a[52:55], a[160:163]\n" \
  "s_add_u32 m0, m0, s82\n" \
  "v_rcp_f32 v139, v139\n" \
  "v_mfma_f32_16x16x32_bf16 a[164:167], a[12:15], a[52:55], a[164:167]\n" \
  "buffer_load_dwordx4 v249, s[20:23], s72 offen lds\n" \
  "v_mul_f32 v138, v138, v170\n" \
  "v_mfma_f32_16x16x32_bf16 a[164:167], a[8:11], a[48:51], a[164:167]\n" \
  "ds_read_b128 a[112:115], v246 offset:36864\n" \
  "v_mul_f32 v143, s86, v143\n" \
  "v_mfma_f32_16x16x32_bf16 a[168:171], a[16:19], a[48:51], a[168:171]\n" \
  "ds_read_b128 a[116:119], v247 offset:36864\n" \
  "v_exp_f32 v142, v142\n" \
  "v_add_f32 v141, 1.0, v141\n" \
  "v_mfma_f32_16x16x32_bf16 a[168:171], a[20:23], a[52:55], a[168:171]\n" \
  "s_add_u32 m0, m0, s82\n" \
  "v_mfma_f32_16x16x32_bf16 a[172:175], a[28:31], a[52:55], a[172:175]\n" \
  "buffer_load_dwordx4 v249, s[20:23], s73 offen lds\n" \
  "v_mfma_f32_16x16x32_bf16 a[172:175], a[24:27], a[48:51], a[172:175]\n" \
  "ds_read_b128 a[120:123], v246 offset:38912\n" \
  "v_rcp_f32 v140, v140\n" \
  "v_mul_f32 v139, v139, v171\n" \
  "v_mfma_f32_16x16x32_bf16 a[176:179], a[0:3], a[56:59], a[176:179]\n" \
  "s_add_u32 m0, m0, s82\n" \
  "v_exp_f32 v143, v143\n" \
  "v_mfma_f32_16x16x32_bf16 a[176:179], a[4:7], a[60:63], a[176:179]\n" \
  "buffer_load_dwordx4 v249, s[20:23], s74 offen lds\n" \
  "v_add_f32 v142, 1.0, v142\n" \
  "v_mfma_f32_16x16x32_bf16 a[180:183], a[12:15], a[60:63], a[180:183]\n" \
  "ds_read_b128 a[124:127], v247 offset:38912\n" \
  "v_rcp_f32 v141, v141\n" \
  "v_mfma_f32_16x16x32_bf16 a[180:183], a[8:11], a[56:59], a[180:183]\n" \
  "v_mul_f32 v140, v140, v172\n" \
  "v_add_f32 v143, 1.0, v143\n" \
  "v_mfma_f32_16x16x32_bf16 a[184:187], a[16:19], a[56:59], a[184:187]\n" \
  "s_sub_u32 s16, s16, 128\n" \
  "s_subb_u32 s17, s17, 0\n" \
  "v_rcp_f32 v142, v142\n" \
  "v_mfma_f32_16x16x32_bf16 a[184:187], a[20:23], a[60:63], a[184:187]\n" \
  "v_mul_f32 v141, v141, v173\n" \
  "v_mfma_f32_16x16x32_bf16 a[188:191], a[28:31], a[60:63], a[188:191]\n" \
  "s_sub_u32 s20, s20, 128\n" \
  "s_subb_u32 s21, s21, 0\n" \
  "v_rcp_f32 v143, v143\n" \
  "v_mfma_f32_16x16x32_bf16 a[188:191], a[24:27], a[56:59], a[188:191]\n" \
  "v_mul_f32 v142, v142, v174\n" \
  "v_mul_f32 v143, v143, v175\n" \
  "s_waitcnt lgkmcnt(0)\n" \
  "s_waitcnt vmcnt(17)\n" \
  "s_barrier\n" \
  "v_mfma_f32_16x16x32_bf16 a[128:131], a[64:67], a[96:99], a[128:131]\n" \
  "s_add_u32 m0, s90, 98304\n" \
  "ds_read_b128 a[32:35], v244 offset:32768\n" \
  "v_cvt_pk_bf16_f32 v168, v136, v137\n" \
  "v_mfma_f32_16x16x32_bf16 a[128:131], a[68:71], a[100:103], a[128:131]\n" \
  "buffer_load_dwordx4 v248, s[16:19], 0 offen sc1 lds\n" \
  "v_cvt_pk_bf16_f32 v169, v138, v139\n" \
  "v_mfma_f32_16x16x32_bf16 a[132:135], a[76:79], a[100:103], a[132:135]\n" \
  "ds_read_b128 a[36:39], v245 offset:32768\n" \
  "buffer_store_dwordx2 v[254:255], v237, s[52:55], s0 offen sc1\n" \
  "s_add_u32 s0, s0, 8192\n" \
  "v_mfma_f32_16x16x32_bf16 a[132:135], a[72:75], a[96:99], a[132:135]\n" \
  "s_add_u32 m0, m0, 2048\n" \
  "ds_read_b128 a[0:3], v240 offset:32768\n" \
  "v_cvt_pk_bf16_f32 v170, v140, v141\n" \
  "v_cvt_pk_bf16_f32 v171, v142, v143\n" \
  "v_mfma_f32_16x16x32_bf16 a[136:139], a[80:83], a[96:99], a[136:139]\n" \
  "buffer_load_dwordx4 v248, s[16:19], s79 offen sc1 lds\n" \
  "s_mov_b32 s69, 229376\n" \
  "v_mfma_f32_16x16x32_bf16 a[136:139], a[84:87], a[100:103], a[136:139]\n" \
  "ds_read_b128 a[4:7], v241 offset:32768\n" \
  "v_permlane16_swap_b32 v168, v170\n" \
  "v_permlane16_swap_b32 v169, v171\n" \
  "v_mfma_f32_16x16x32_bf16 a[140:143], a[92:95], a[100:103], a[140:143]\n" \
  "ds_read_b128 a[8:11], v240 offset:34816\n" \
  "buffer_store_dwordx4 v[168:171], v251, s[24:27], s69 offen sc1\n" \
  "v_mfma_f32_16x16x32_bf16 a[140:143], a[88:91], a[96:99], a[140:143]\n" \
  "s_add_u32 m0, m0, 2048\n" \
  "ds_read_b128 a[12:15], v241 offset:34816\n" \
  "v_mul_f32 v144, s86, v144\n" \
  "v_mul_f32 v145, s86, v145\n" \
  "v_mfma_f32_16x16x32_bf16 a[144:147], a[64:67], a[104:107], a[144:147]\n" \
  "buffer_load_dwordx4 v248, s[16:19], s80 offen sc1 lds\n" \
  "v_mfma_f32_16x16x32_bf16 a[144:147], a[68:71], a[108:111], a[144:147]\n" \
  "ds_read_b128 a[16:19], v240 offset:36864\n" \
  "v_exp_f32 v144, v144\n" \
  "v_mul_f32 v146, s86, v146\n" \
  "v_mfma_f32_16x16x32_bf16 a[148:151], a[76:79], a[108:111], a[148:151]\n" \
  "s_add_u32 m0, m0, 2048\n" \
  "ds_read_b128 a[20:23], v241 offset:36864\n" \
  "v_exp_f32 v145, v145\n" \
  "v_mfma_f32_16x16x32_bf16 a[148:151], a[72:75], a[104:107], a[148:151]\n" \
  "buffer_load_dwordx4 v248, s[16:19], s81 offen sc1 lds\n" \
  "v_add_f32 v144, 1.0, v144\n" \
  "v_mfma_f32_16x16x32_bf16 a[152:155], a[80:83], a[104:107], a[152:155]\n" \
  "ds_read_b128 a[24:27], v240 offset:38912\n" \
  "v_mul_f32 v147, s86, v147\n" \
  "v_mfma_f32_16x16x32_bf16 a[152:155], a[84:87], a[108:111], a[152:155]\n" \
  "ds_read_b128 a[28:31], v241 offset:38912\n" \
  "v_exp_f32 v146, v146\n" \
  "v_add_f32 v145, 1.0, v145\n" \
  "v_mfma_f32_16x16x32_bf16 a[156:159], a[92:95], a[108:111], a[156:159]\n" \
  "s_add_u32 m0, s91, 98304\n" \
  "ds_read_b128 a[40:43], v244 offset:34816\n" \
  "v_rcp_f32 v144, v144\n" \
  "v_mfma_f32_16x16x32_bf16 a[156:159], a[88:91], a[104:107], a[156:159]\n" \
  "buffer_load_dwordx4 v249, s[20:23], 0 offen lds\n" \
  "v_mul_f32 v148, s86, v148\n" \
  "v_mfma_f32_16x16x32_bf16 a[160:163], a[64:67], a[112:115], a[160:163]\n" \
  "ds_read_b128 a[44:47], v245 offset:34816\n" \
  "v_exp_f32 v147, v147\n" \
  "v_mfma_f32_16x16x32_bf16 a[160:163], a[68:71], a[116:119], a[160:163]\n" \
  "s_add_u32 m0, m0, s82\n" \
  "v_add_f32 v146, 1.0, v146\n" \
  "v_mfma_f32_16x16x32_bf16 a[164:167], a[76:79], a[116:119], a[164:167]\n" \
  "buffer_load_dwordx4 v249, s[20:23], s72 offen lds\n" \
  "v_mfma_f32_16x16x32_bf16 a[164:167], a[72:75], a[112:115], a[164:167]\n" \
  "ds_read_b128 a[48:51], v244 offset:36864\n" \
  "v_rcp_f32 v145, v145\n" \
  "v_mul_f32 v144, v144, v176\n" \
  "v_mfma_f32_16x16x32_bf16 a[168:171], a[80:83], a[112:115], a[168:171]\n" \
  "ds_read_b128 a[52:55], v245 offset:36864\n" \
  "v_mul_f32 v149, s86, v149\n" \
  "v_mfma_f32_16x16x32_bf16 a[168:171], a[84:87], a[116:119], a[168:171]\n" \
  "s_add_u32 m0, m0, s82\n" \
  "v_exp_f32 v148, v148\n" \
  "v_add_f32 v147, 1.0, v147\n" \
  "v_mfma_f32_16x16x32_bf16 a[172:175], a[92:95], a[116:119], a[172:175]\n" \
  "buffer_load_dwordx4 v249, s[20:23], s73 offen lds\n" \
  "v_mfma_f32_16x16x32_bf16 a[172:175], a[88:91], a[112:115], a[172:175]\n" \
  "ds_read_b128 a[56:59], v244 offset:38912\n" \
  "v_rcp_f32 v146, v146\n" \
  "v_mul_f32 v145, v145, v177\n" \
  "v_mfma_f32_16x16x32_bf16 a[176:179], a[64:67], a[120:123], a[176:179]\n" \
  "s_add_u32 m0, m0, s82\n" \
  "v_mul_f32 v150, s86, v150\n" \
  "v_mfma_f32_16x16x32_bf16 a[176:179], a[68:71], a[124:127], a[176:179]\n" \
  "buffer_load_dwordx4 v249, s[20:23], s74 offen lds\n" \
  "v_mfma_f32_16x16x32_bf16 a[180:183], a[76:79], a[124:127], a[180:183]\n" \
  "ds_read_b128 a[60:63], v245 offset:38912\n" \
  "v_exp_f32 v149, v149\n" \
  "v_add_f32 v148, 1.0, v148\n" \
  "v_mfma_f32_16x16x32_bf16 a[180:183], a[72:75], a[120:123], a[180:183]\n" \
  "v_rcp_f32 v147, v147\n" \
  "v_mfma_f32_16x16x32_bf16 a[184:187], a[80:83], a[120:123], a[184:187]\n" \
  "s_sub_u32 s16, s16, 128\n" \
  "s_subb_u32 s17, s17, 0\n" \
  "v_mul_f32 v146, v146, v178\n" \
  "v_mul_f32 v151, s86, v151\n" \
  "v_mfma_f32_16x16x32_bf16 a[184:187], a[84:87], a[124:127], a[184:187]\n" \
  "v_mfma_f32_16x16x32_bf16 a[188:191], a[92:95], a[124:127], a[188:191]\n" \
  "s_sub_u32 s20, s20, 128\n" \
  "s_subb_u32 s21, s21, 0\n" \
  "v_exp_f32 v150, v150\n" \
  "v_mfma_f32_16x16x32_bf16 a[188:191], a[88:91], a[120:123], a[188:191]\n" \
  "v_add_f32 v149, 1.0, v149\n" \
  "v_rcp_f32 v148, v148\n" \
  "s_waitcnt lgkmcnt(0)\n" \
  "s_waitcnt vmcnt(19)\n" \
  "s_barrier\n" \
  "v_mfma_f32_16x16x32_bf16 a[128:131], a[0:3], a[32:35], a[128:131]\n" \
  "s_add_u32 m0, s90, 32768\n" \
  "ds_read_b128 a[96:99], v246 offset:0\n" \
  "v_mul_f32 v147, v147, v179\n" \
  "v_mfma_f32_16x16x32_bf16 a[128:131], a[4:7], a[36:39], a[128:131]\n" \
  "buffer_load_dwordx4 v248, s[16:19], 0 offen sc1 lds\n" \
  "v_mfma_f32_16x16x32_bf16 a[132:135], a[12:15], a[36:39], a[132:135]\n" \
  "ds_read_b128 a[100:103], v247 offset:0\n" \
  "v_exp_f32 v151, v151\n" \
  "v_add_f32 v150, 1.0, v150\n" \
  "v_mfma_f32_16x16x32_bf16 a[132:135], a[8:11], a[32:35], a[132:135]\n" \
  "s_add_u32 m0, m0, 2048\n" \
  "ds_read_b128 a[64:67], v242 offset:0\n" \
  "v_rcp_f32 v149, v149\n" \
  "v_mfma_f32_16x16x32_bf16 a[136:139], a[16:19], a[32:35], a[136:139]\n" \
  "buffer_load_dwordx4 v248, s[16:19], s79 offen sc1 lds\n" \
  "v_mul_f32 v148, v148, v180\n" \
  "v_mfma_f32_16x16x32_bf16 a[136:139], a[20:23], a[36:39], a[136:139]\n" \
  "ds_read_b128 a[68:71], v243 offset:0\n" \
  "v_add_f32 v151, 1.0, v151\n" \
  "v_mfma_f32_16x16x32_bf16 a[140:143], a[28:31], a[36:39], a[140:143]\n" \
  "ds_read_b128 a[72:75], v242 offset:2048\n" \
  "v_rcp_f32 v150, v150\n" \
  "v_mul_f32 v149, v149, v181\n" \
  "v_mfma_f32_16x16x32_bf16 a[140:143], a[24:27], a[32:35], a[140:143]\n" \
  "s_add_u32 m0, m0, 2048\n" \
  "ds_read_b128 a[76:79], v243 offset:2048\n" \
  "v_rcp_f32 v151, v151\n" \
  "v_mfma_f32_16x16x32_bf16 a[144:147], a[0:3], a[40:43], a[144:147]\n" \
  "buffer_load_dwordx4 v248, s[16:19], s80 offen sc1 lds\n" \
  "v_mul_f32 v150, v150, v182\n" \
  "v_mfma_f32_16x16x32_bf16 a[144:147], a[4:7], a[44:47], a[144:147]\n" \
  "ds_read_b128 a[80:83], v242 offset:4096\n" \
  "v_mul_f32 v151, v151, v183\n" \
  "v_cvt_pk_bf16_f32 v176, v144, v145\n" \
  "v_mfma_f32_16x16x32_bf16 a[148:151], a[12:15], a[44:47], a[148:151]\n" \
  "s_add_u32 m0, m0, 2048\n" \
  "ds_read_b128 a[84:87], v243 offset:4096\n" \
  "v_cvt_pk_bf16_f32 v177, v146, v147\n" \
  "v_cvt_pk_bf16_f32 v178, v148, v149\n" \
  "v_mfma_f32_16x16x32_bf16 a[148:151], a[8:11], a[40:43], a[148:151]\n" \
  "buffer_load_dwordx4 v248, s[16:19], s81 offen sc1 lds\n" \
  "v_cvt_pk_bf16_f32 v179, v150, v151\n" \
  "v_mfma_f32_16x16x32_bf16 a[152:155], a[16:19], a[40:43], a[152:155]\n" \
  "ds_read_b128 a[88:91], v242 offset:6144\n" \
  "s_mov_b32 s69, 458752\n" \
  "v_permlane16_swap_b32 v176, v178\n" \
  "v_mfma_f32_16x16x32_bf16 a[152:155], a[20:23], a[44:47], a[152:155]\n" \
  "ds_read_b128 a[92:95], v243 offset:6144\n" \
  "v_permlane16_swap_b32 v177, v179\n" \
  "v_mfma_f32_16x16x32_bf16 a[156:159], a[28:31], a[44:47], a[156:159]\n" \
  "s_add_u32 m0, s91, 32768\n" \
  "ds_read_b128 a[104:107], v246 offset:2048\n" \
  "v_mfma_f32_16x16x32_bf16 a[156:159], a[24:27], a[40:43], a[156:159]\n" \
  "buffer_load_dwordx4 v249, s[20:23], 0 offen lds\n" \
  "v_mfma_f32_16x16x32_bf16 a[160:163], a[0:3], a[48:51], a[160:163]\n" \
  "ds_read_b128 a[108:111], v247 offset:2048\n" \
  "buffer_store_dwordx4 v[176:179], v251, s[24:27], s69 offen sc1\n" \
  "v_mfma_f32_16x16x32_bf16 a[160:163], a[4:7], a[52:55], a[160:163]\n" \
  "s_add_u32 m0, m0, s82\n" \
  "v_mul_f32 v152, s86, v152\n" \
  "v_mul_f32 v153, s86, v153\n" \
  "v_mfma_f32_16x16x32_bf16 a[164:167], a[12:15], a[52:55], a[164:167]\n" \
  "buffer_load_dwordx4 v249, s[20:23], s72 offen lds\n" \
  "v_mfma_f32_16x16x32_bf16 a[164:167], a[8:11], a[48:51], a[164:167]\n" \
  "ds_read_b128 a[112:115], v246 offset:4096\n" \
  "v_exp_f32 v152, v152\n" \
  "v_mul_f32 v154, s86, v154\n" \
  "v_mfma_f32_16x16x32_bf16 a[168:171], a[16:19], a[48:51], a[168:171]\n" \
  "ds_read_b128 a[116:119], v247 offset:4096\n" \
  "v_exp_f32 v153, v153\n" \
  "v_mfma_f32_16x16x32_bf16 a[168:171], a[20:23], a[52:55], a[168:171]\n" \
  "s_add_u32 m0, m0, s82\n" \
  "v_add_f32 v152, 1.0, v152\n" \
  "v_mul_f32 v155, s86, v155\n" \
  "v_mfma_f32_16x16x32_bf16 a[172:175], a[28:31], a[52:55], a[172:175]\n" \
  "buffer_load_dwordx4 v249, s[20:23], s73 offen lds\n" \
  "v_mfma_f32_16x16x32_bf16 a[172:175], a[24:27], a[48:51], a[172:175]\n" \
  "ds_read_b128 a[120:123], v246 offset:6144\n" \
  "v_exp_f32 v154, v154\n" \
  "v_add_f32 v153, 1.0, v153\n" \
  "v_mfma_f32_16x16x32_bf16 a[176:179], a[0:3], a[56:59], a[176:179]\n" \
  "s_add_u32 m0, m0, s82\n" \
  "v_rcp_f32 v152, v152\n" \
  "v_mfma_f32_16x16x32_bf16 a[176:179], a[4:7], a[60:63], a[176:179]\n" \
  "buffer_load_dwordx4 v249, s[20:23], s74 offen lds\n" \
  "v_mul_f32 v156, s86, v156\n" \
  "v_mfma_f32_16x16x32_bf16 a[180:183], a[12:15], a[60:63], a[180:183]\n" \
  "ds_read_b128 a[124:127], v247 offset:6144\n" \
  "v_exp_f32 v155, v155\n" \
  "v_mfma_f32_16x16x32_bf16 a[180:183], a[8:11], a[56:59], a[180:183]\n" \
  "v_add_f32 v154, 1.0, v154\n" \
  "v_mfma_f32_16x16x32_bf16 a[184:187], a[16:19], a[56:59], a[184:187]\n" \
  "s_sub_u32 s16, s16, 128\n" \
  "s_subb_u32 s17, s17, 0\n" \
  "v_rcp_f32 v153, v153\n" \
  "v_mfma_f32_16x16x32_bf16 a[184:187], a[20:23], a[60:63], a[184:187]\n" \
  "v_mul_f32 v152, v152, v184\n" \
  "v_mul_f32 v157, s86, v157\n" \
  "v_mfma_f32_16x16x32_bf16 a[188:191], a[28:31], a[60:63], a[188:191]\n" \
  "s_sub_u32 s20, s20, 128\n" \
  "s_subb_u32 s21, s21, 0\n" \
  "v_mfma_f32_16x16x32_bf16 a[188:191], a[24:27], a[56:59], a[188:191]\n" \
  "v_exp_f32 v156, v156\n" \
  "v_add_f32 v155, 1.0, v155\n" \
  "v_accvgpr_read_b32 v253, a212\n" \
  "buffer_load_dwordx2 v[252:253], v253, s[52:55], s1 offen sc1\n" \
  "s_waitcnt lgkmcnt(0)\n" \
  "s_waitcnt vmcnt(20)\n" \
  "s_barrier\n" \
  "v_mfma_f32_16x16x32_bf16 a[128:131], a[64:67], a[96:99], a[128:131]\n" \
  "s_add_u32 m0, s90, 65536\n" \
  "ds_read_b128 a[32:35], v244 offset:0\n" \
  "v_rcp_f32 v154, v154\n" \
  "v_mfma_f32_16x16x32_bf16 a[128:131], a[68:71], a[100:103], a[128:131]\n" \
  "buffer_load_dwordx4 v248, s[16:19], 0 offen sc1 lds\n" \
  "v_mul_f32 v153, v153, v185\n" \
  "v_mfma_f32_16x16x32_bf16 a[132:135], a[76:79], a[100:103], a[132:135]\n" \
  "ds_read_b128 a[36:39], v245 offset:0\n" \
  "v_mul_f32 v158, s86, v158\n" \
  "v_mfma_f32_16x16x32_bf16 a[132:135], a[72:75], a[96:99], a[132:135]\n" \
  "s_add_u32 m0, m0, 2048\n" \
  "ds_read_b128 a[0:3], v240 offset:0\n" \
  "v_exp_f32 v157, v157\n" \
  "v_mfma_f32_16x16x32_bf16 a[136:139], a[80:83], a[96:99], a[136:139]\n" \
  "buffer_load_dwordx4 v248, s[16:19], s79 offen sc1 lds\n" \
  "v_add_f32 v156, 1.0, v156\n" \
  "v_mfma_f32_16x16x32_bf16 a[136:139], a[84:87], a[100:103], a[136:139]\n" \
  "ds_read_b128 a[4:7], v241 offset:0\n" \
  "v_rcp_f32 v155, v155\n" \
  "v_mul_f32 v154, v154, v186\n" \
  "v_mfma_f32_16x16x32_bf16 a[140:143], a[92:95], a[100:103], a[140:143]\n" \
  "ds_read_b128 a[8:11], v240 offset:2048\n" \
  "v_mul_f32 v159, s86, v159\n" \
  "v_mfma_f32_16x16x32_bf16 a[140:143], a[88:91], a[96:99], a[140:143]\n" \
  "s_add_u32 m0, m0, 2048\n" \
  "ds_read_b128 a[12:15], v241 offset:2048\n" \
  "v_exp_f32 v158, v158\n" \
  "v_mfma_f32_16x16x32_bf16 a[144:147], a[64:67], a[104:107], a[144:147]\n" \
  "buffer_load_dwordx4 v248, s[16:19], s80 offen sc1 lds\n" \
  "v_add_f32 v157, 1.0, v157\n" \
  "v_mfma_f32_16x16x32_bf16 a[144:147], a[68:71], a[108:111], a[144:147]\n" \
  "ds_read_b128 a[16:19], v240 offset:4096\n" \
  "v_rcp_f32 v156, v156\n" \
  "v_mfma_f32_16x16x32_bf16 a[148:151], a[76:79], a[108:111], a[148:151]\n" \
  "s_add_u32 m0, m0, 2048\n" \
  "ds_read_b128 a[20:23], v241 offset:4096\n" \
  "v_mul_f32 v155, v155, v187\n" \
  "v_mfma_f32_16x16x32_bf16 a[148:151], a[72:75], a[104:107], a[148:151]\n" \
  "buffer_load_dwordx4 v248, s[16:19], s81 offen sc1 lds\n" \
  "v_mfma_f32_16x16x32_bf16 a[152:155], a[80:83], a[104:107], a[152:155]\n" \
  "ds_read_b128 a[24:27], v240 offset:6144\n" \
  "v_exp_f32 v159, v159\n" \
  "v_add_f32 v158, 1.0, v158\n" \
  "v_mfma_f32_16x16x32_bf16 a[152:155], a[84:87], a[108:111], a[152:155]\n" \
  "ds_read_b128 a[28:31], v241 offset:6144\n" \
  "v_rcp_f32 v157, v157\n" \
  "v_mfma_f32_16x16x32_bf16 a[156:159], a[92:95], a[108:111], a[156:159]\n" \
  "s_add_u32 m0, s91, 65536\n" \
  "ds_read_b128 a[40:43], v244 offset:2048\n" \
  "v_mul_f32 v156, v156, v188\n" \
  "v_add_f32 v159, 1.0, v159\n" \
  "v_mfma_f32_16x16x32_bf16 a[156:159], a[88:91], a[104:107], a[156:159]\n" \
  "buffer_load_dwordx4 v249, s[20:23], 0 offen lds\n" \
  "v_mfma_f32_16x16x32_bf16 a[160:163], a[64:67], a[112:115], a[160:163]\n" \
  "ds_read_b128 a[44:47], v245 offset:2048\n" \
  "v_rcp_f32 v158, v158\n" \
  "v_mul_f32 v157, v157, v189\n" \
  "v_mfma_f32_16x16x32_bf16 a[160:163], a[68:71], a[116:119], a[160:163]\n" \
  "s_add_u32 m0, m0, s82\n" \
  "v_rcp_f32 v159, v159\n" \
  "v_mfma_f32_16x16x32_bf16 a[164:167], a[76:79], a[116:119], a[164:167]\n" \
  "buffer_load_dwordx4 v249, s[20:23], s72 offen lds\n" \
  "v_mul_f32 v158, v158, v190\n" \
  "v_mfma_f32_16x16x32_bf16 a[164:167], a[72:75], a[112:115], a[164:167]\n" \
  "ds_read_b128 a[48:51], v244 offset:4096\n" \
  "v_mul_f32 v159, v159, v191\n" \
  "v_cvt_pk_bf16_f32 v184, v152, v153\n" \
  "v_mfma_f32_16x16x32_bf16 a[168:171], a[80:83], a[112:115], a[168:171]\n" \
  "ds_read_b128 a[52:55], v245 offset:4096\n" \
  "v_cvt_pk_bf16_f32 v185, v154, v155\n" \
  "v_cvt_pk_bf16_f32 v186, v156, v157\n" \
  "v_mfma_f32_16x16x32_bf16 a[168:171], a[84:87], a[116:119], a[168:171]\n" \
  "s_add_u32 m0, m0, s82\n" \
  "v_cvt_pk_bf16_f32 v187, v158, v159\n" \
  "v_mfma_f32_16x16x32_bf16 a[172:175], a[92:95], a[116:119], a[172:175]\n" \
  "buffer_load_dwordx4 v249, s[20:23], s73 offen lds\n" \
  "s_mov_b32 s69, 688128\n" \
  "v_mfma_f32_16x16x32_bf16 a[172:175], a[88:91], a[112:115], a[172:175]\n" \
  "ds_read_b128 a[56:59], v244 offset:6144\n" \
  "v_permlane16_swap_b32 v184, v186\n" \
  "v_permlane16_swap_b32 v185, v187\n" \
  "v_mfma_f32_16x16x32_bf16 a[176:179], a[64:67], a[120:123], a[176:179]\n" \
  "s_add_u32 m0, m0, s82\n" \
  "buffer_store_dwordx4 v[184:187], v251, s[24:27], s69 offen sc1\n" \
  "v_mfma_f32_16x16x32_bf16 a[176:179], a[68:71], a[124:127], a[176:179]\n" \
  "buffer_load_dwordx4 v249, s[20:23], s74 offen lds\n" \
  "v_permlane32_swap_b32 v192, v196\n" \
  "v_mfma_f32_16x16x32_bf16 a[180:183], a[76:79], a[124:127], a[180:183]\n" \
  "ds_read_b128 a[60:63], v245 offset:6144\n" \
  "v_permlane32_swap_b32 v193, v197\n" \
  "v_permlane32_swap_b32 v194, v198\n" \
  "v_mfma_f32_16x16x32_bf16 a[180:183], a[72:75], a[120:123], a[180:183]\n" \
  "v_permlane32_swap_b32 v195, v199\n" \
  "v_mfma_f32_16x16x32_bf16 a[184:187], a[80:83], a[120:123], a[184:187]\n" \
  "s_add_u32 s16, s16, s6\n" \
  "s_addc_u32 s17, s17, 0\n" \
  "v_permlane32_swap_b32 v200, v204\n" \
  "v_permlane32_swap_b32 v201, v205\n" \
  "v_mfma_f32_16x16x32_bf16 a[184:187], a[84:87], a[124:127], a[184:187]\n" \
  "v_permlane32_swap_b32 v202, v206\n" \
  "v_permlane32_swap_b32 v203, v207\n" \
  "v_mfma_f32_16x16x32_bf16 a[188:191], a[92:95], a[124:127], a[188:191]\n" \
  "v_mul_f32 v192, s86, v192\n" \
  "v_mfma_f32_16x16x32_bf16 a[188:191], a[88:91], a[120:123], a[188:191]\n" \
  "v_mul_f32 v193, s86, v193\n" \
  "s_waitcnt lgkmcnt(0)\n" \
  "s_waitcnt vmcnt(19)\n" \
  "s_barrier\n" \
  "s_waitcnt vmcnt(9)\n" \
  "v_xor_b32 v252, v252, v254\n" \
  "v_xor_b32 v253, v253, v255\n" \
  "v_or_b32 v252, v252, v253\n" \
  "v_cmp_ne_u32 vcc, 0, v252\n" \
  "s_nop 4\n" \
  "s_cbranch_vccnz Lslow2_%=\n" \
  "Lres2_%=:\n" \
  "s_add_u32 s1, s1, 8192\n" \
  "v_mfma_f32_16x16x32_bf16 a[128:131], a[0:3], a[32:35], a[128:131]\n" \
  "s_add_u32 m0, s93, 0\n" \
  "ds_read_b128 a[96:99], v246 offset:32768\n" \
  "v_exp_f32 v192, v192\n" \
  "v_mfma_f32_16x16x32_bf16 a[128:131], a[4:7], a[36:39], a[128:131]\n" \
  "buffer_load_dwordx4 v235, s[32:35], s96 offen lds\n" \
  "v_mul_f32 v194, s86, v194\n" \
  "v_mfma_f32_16x16x32_bf16 a[132:135], a[12:15], a[36:39], a[132:135]\n" \
  "ds_read_b128 a[100:103], v247 offset:32768\n" \
  "v_exp_f32 v193, v193\n" \
  "v_add_f32 v192, 1.0, v192\n" \
  "v_mfma_f32_16x16x32_bf16 a[132:135], a[8:11], a[32:35], a[132:135]\n" \
  "s_add_u32 m0, m0, s82\n" \
  "ds_read_b128 a[64:67], v242 offset:32768\n" \
  "v_mul_f32 v195, s86, v195\n" \
  "v_mfma_f32_16x16x32_bf16 a[136:139], a[16:19], a[32:35], a[136:139]\n" \
  "buffer_load_dwordx4 v235, s[32:35], s97 offen lds\n" \
  "v_mfma_f32_16x16x32_bf16 a[136:139], a[20:23], a[36:39], a[136:139]\n" \
  "ds_read_b128 a[68:71], v243 offset:32768\n" \
  "v_exp_f32 v194, v194\n" \
  "v_add_f32 v193, 1.0, v193\n" \
  "v_mfma_f32_16x16x32_bf16 a[140:143], a[28:31], a[36:39], a[140:143]\n" \
  "ds_read_b128 a[72:75], v242 offset:34816\n" \
  "v_rcp_f32 v192, v192\n" \
  "v_mfma_f32_16x16x32_bf16 a[140:143], a[24:27], a[32:35], a[140:143]\n" \
  "s_add_u32 m0, m0, s82\n" \
  "ds_read_b128 a[76:79], v243 offset:34816\n" \
  "v_mul_f32 v200, s86, v200\n" \
  "v_mfma_f32_16x16x32_bf16 a[144:147], a[0:3], a[40:43], a[144:147]\n" \
  "buffer_load_dwordx4 v235, s[32:35], s98 offen lds\n" \
  "v_mfma_f32_16x16x32_bf16 a[144:147], a[4:7], a[44:47], a[144:147]\n" \
  "ds_read_b128 a[80:83], v242 offset:36864\n" \
  "v_exp_f32 v195, v195\n" \
  "v_add_f32 v194, 1.0, v194\n" \
  "v_mfma_f32_16x16x32_bf16 a[148:151], a[12:15], a[44:47], a[148:151]\n" \
  "s_add_u32 m0, m0, s82\n" \
  "v_rcp_f32 v193, v193\n" \
  "v_mfma_f32_16x16x32_bf16 a[148:151], a[8:11], a[40:43], a[148:151]\n" \
  "buffer_load_dwordx4 v235, s[32:35], s99 offen lds\n" \
  "v_mul_f32 v192, v192, v196\n" \
  "v_mfma_f32_16x16x32_bf16 a[152:155], a[16:19], a[40:43], a[152:155]\n" \
  "ds_read_b128 a[84:87], v243 offset:36864\n" \
  "v_mul_f32 v201, s86, v201\n" \
  "v_mfma_f32_16x16x32_bf16 a[152:155], a[20:23], a[44:47], a[152:155]\n" \
  "ds_read_b128 a[88:91], v242 offset:38912\n" \
  "v_exp_f32 v200, v200\n" \
  "v_add_f32 v195, 1.0, v195\n" \
  "v_mfma_f32_16x16x32_bf16 a[156:159], a[28:31], a[44:47], a[156:159]\n" \
  "s_add_u32 m0, s92, 0\n" \
  "v_rcp_f32 v194, v194\n" \
  "v_mfma_f32_16x16x32_bf16 a[156:159], a[24:27], a[40:43], a[156:159]\n" \
  "buffer_load_dwordx4 v234, s[28:31], 0 offen sc1 lds\n" \
  "v_mul_f32 v193, v193, v197\n" \
  "v_mfma_f32_16x16x32_bf16 a[160:163], a[0:3], a[48:51], a[160:163]\n" \
  "ds_read_b128 a[92:95], v243 offset:38912\n" \
  "v_mul_f32 v202, s86, v202\n" \
  "v_mfma_f32_16x16x32_bf16 a[160:163], a[4:7], a[52:55], a[160:163]\n" \
  "ds_read_b128 a[104:107], v246 offset:34816\n" \
  "v_exp_f32 v201, v201\n" \
  "v_add_f32 v200, 1.0, v200\n" \
  "v_mfma_f32_16x16x32_bf16 a[164:167], a[12:15], a[52:55], a[164:167]\n" \
  "buffer_load_dwordx4 v234, s[28:31], 0 offen offset:1024 sc1 lds\n" \
  "v_mfma_f32_16x16x32_bf16 a[164:167], a[8:11], a[48:51], a[164:167]\n" \
  "ds_read_b128 a[108:111], v247 offset:34816\n" \
  "v_rcp_f32 v195, v195\n" \
  "v_mul_f32 v194, v194, v198\n" \
  "v_mfma_f32_16x16x32_bf16 a[168:171], a[16:19], a[48:51], a[168:171]\n" \
  "ds_read_b128 a[112:115], v246 offset:36864\n" \
  "v_mul_f32 v203, s86, v203\n" \
  "v_mfma_f32_16x16x32_bf16 a[168:171], a[20:23], a[52:55], a[168:171]\n" \
  "s_add_u32 m0, m0, 2048\n" \
  "v_exp_f32 v202, v202\n" \
  "v_mfma_f32_16x16x32_bf16 a[172:175], a[28:31], a[52:55], a[172:175]\n" \
  "buffer_load_dwordx4 v234, s[28:31], s85 offen sc1 lds\n" \
  "v_add_f32 v201, 1.0, v201\n" \
  "v_mfma_f32_16x16x32_bf16 a[172:175], a[24:27], a[48:51], a[172:175]\n" \
  "ds_read_b128 a[116:119], v247 offset:36864\n" \
  "v_rcp_f32 v200, v200\n" \
  "v_mfma_f32_16x16x32_bf16 a[176:179], a[0:3], a[56:59], a[176:179]\n" \
  "ds_read_b128 a[120:123], v246 offset:38912\n" \
  "v_mul_f32 v195, v195, v199\n" \
  "v_mfma_f32_16x16x32_bf16 a[176:179], a[4:7], a[60:63], a[176:179]\n" \
  "buffer_load_dwordx4 v234, s[28:31], s85 offen offset:1024 sc1 lds\n" \
  "v_mfma_f32_16x16x32_bf16 a[180:183], a[12:15], a[60:63], a[180:183]\n" \
  "ds_read_b128 a[124:127], v247 offset:38912\n" \
  "v_exp_f32 v203, v203\n" \
  "v_add_f32 v202, 1.0, v202\n" \
  "v_mfma_f32_16x16x32_bf16 a[180:183], a[8:11], a[56:59], a[180:183]\n" \
  "v_rcp_f32 v201, v201\n" \
  "v_mfma_f32_16x16x32_bf16 a[184:187], a[16:19], a[56:59], a[184:187]\n" \
  "s_add_u32 s28, s28, 2048\n" \
  "s_addc_u32 s29, s29, 0\n" \
  "v_mul_f32 v200, v200, v204\n" \
  "v_add_f32 v203, 1.0, v203\n" \
  "v_mfma_f32_16x16x32_bf16 a[184:187], a[20:23], a[60:63], a[184:187]\n" \
  "v_mfma_f32_16x16x32_bf16 a[188:191], a[28:31], a[60:63], a[188:191]\n" \
  "s_add_u32 s32, s32, 229376\n" \
  "s_addc_u32 s33, s33, 0\n" \
  "v_rcp_f32 v202, v202\n" \
  "v_mfma_f32_16x16x32_bf16 a[188:191], a[24:27], a[56:59], a[188:191]\n" \
  "v_mul_f32 v201, v201, v205\n" \
  "v_rcp_f32 v203, v203\n" \
  "s_waitcnt lgkmcnt(0)\n" \
  "s_barrier\n" \
  "v_mfma_f32_16x16x32_bf16 a[128:131], a[64:67], a[96:99], a[128:131]\n" \
  "s_add_u32 m0, s93, 98304\n" \
  "ds_read_b128 a[32:35], v244 offset:32768\n" \
  "v_mul_f32 v202, v202, v206\n" \
  "v_mfma_f32_16x16x32_bf16 a[128:131], a[68:71], a[100:103], a[128:131]\n" \
  "buffer_load_dwordx4 v235, s[32:35], s96 offen lds\n" \
  "v_mul_f32 v203, v203, v207\n" \
  "v_mfma_f32_16x16x32_bf16 a[132:135], a[76:79], a[100:103], a[132:135]\n" \
  "ds_read_b128 a[36:39], v245 offset:32768\n" \
  "v_cvt_pk_bf16_f32 v196, v192, v193\n" \
  "v_cvt_pk_bf16_f32 v197, v194, v195\n" \
  "v_cvt_pk_bf16_f32 v198, v200, v201\n" \
  "v_mfma_f32_16x16x32_bf16 a[132:135], a[72:75], a[96:99], a[132:135]\n" \
  "s_add_u32 m0, m0, s82\n" \
  "ds_read_b128 a[0:3], v240 offset:32768\n" \
  "v_cvt_pk_bf16_f32 v199, v202, v203\n" \
  "v_mfma_f32_16x16x32_bf16 a[136:139], a[80:83], a[96:99], a[136:139]\n" \
  "buffer_load_dwordx4 v235, s[32:35], s97 offen lds\n" \
  "s_mov_b32 s69, 0\n" \
  "v_mfma_f32_16x16x32_bf16 a[136:139], a[84:87], a[100:103], a[136:139]\n" \
  "ds_read_b128 a[4:7], v241 offset:32768\n" \
  "v_permlane16_swap_b32 v196, v198\n" \
  "v_permlane16_swap_b32 v197, v199\n" \
  "v_mfma_f32_16x16x32_bf16 a[140:143], a[92:95], a[100:103], a[140:143]\n" \
  "ds_read_b128 a[8:11], v240 offset:34816\n" \
  "buffer_store_dwordx4 v[196:199], v236, s[24:27], s69 offen sc1\n" \
  "v_mfma_f32_16x16x32_bf16 a[140:143], a[88:91], a[96:99], a[140:143]\n" \
  "s_add_u32 m0, m0, s82\n" \
  "ds_read_b128 a[12:15], v241 offset:34816\n" \
  "v_permlane32_swap_b32 v208, v212\n" \
  "v_mfma_f32_16x16x32_bf16 a[144:147], a[64:67], a[104:107], a[144:147]\n" \
  "buffer_load_dwordx4 v235, s[32:35], s98 offen lds\n" \
  "v_permlane32_swap_b32 v209, v213\n" \
  "v_mfma_f32_16x16x32_bf16 a[144:147], a[68:71], a[108:111], a[144:147]\n" \
  "ds_read_b128 a[16:19], v240 offset:36864\n" \
  "v_permlane32_swap_b32 v210, v214\n" \
  "v_permlane32_swap_b32 v211, v215\n" \
  "v_mfma_f32_16x16x32_bf16 a[148:151], a[76:79], a[108:111], a[148:151]\n" \
  "s_add_u32 m0, m0, s82\n" \
  "v_permlane32_swap_b32 v216, v220\n" \
  "v_permlane32_swap_b32 v217, v221\n" \
  "v_mfma_f32_16x16x32_bf16 a[148:151], a[72:75], a[104:107], a[148:151]\n" \
  "buffer_load_dwordx4 v235, s[32:35], s99 offen lds\n" \
  "v_permlane32_swap_b32 v218, v222\n" \
  "v_mfma_f32_16x16x32_bf16 a[152:155], a[80:83], a[104:107], a[152:155]\n" \
  "ds_read_b128 a[20:23], v241 offset:36864\n" \
  "v_permlane32_swap_b32 v219, v223\n" \
  "v_mul_f32 v208, s86, v208\n" \
  "v_mfma_f32_16x16x32_bf16 a[152:155], a[84:87], a[108:111], a[152:155]\n" \
  "ds_read_b128 a[24:27], v240 offset:38912\n" \
  "v_mul_f32 v209, s86, v209\n" \
  "v_mfma_f32_16x16x32_bf16 a[156:159], a[92:95], a[108:111], a[156:159]\n" \
  "s_add_u32 m0, s92, 98304\n" \
  "v_exp_f32 v208, v208\n" \
  "v_mul_f32 v210, s86, v210\n" \
  "v_mfma_f32_16x16x32_bf16 a[156:159], a[88:91], a[104:107], a[156:159]\n" \
  "buffer_load_dwordx4 v234, s[28:31], 0 offen sc1 lds\n" \
  "v_mfma_f32_16x16x32_bf16 a[160:163], a[64:67], a[112:115], a[160:163]\n" \
  "ds_read_b128 a[28:31], v241 offset:38912\n" \
  "v_exp_f32 v209, v209\n" \
  "v_add_f32 v208, 1.0, v208\n" \
  "v_mfma_f32_16x16x32_bf16 a[160:163], a[68:71], a[116:119], a[160:163]\n" \
  "ds_read_b128 a[40:43], v244 offset:34816\n" \
  "v_mul_f32 v211, s86, v211\n" \
  "v_mfma_f32_16x16x32_bf16 a[164:167], a[76:79], a[116:119], a[164:167]\n" \
  "buffer_load_dwordx4 v234, s[28:31], 0 offen offset:1024 sc1 lds\n" \
  "v_mfma_f32_16x16x32_bf16 a[164:167], a[72:75], a[112:115], a[164:167]\n" \
  "ds_read_b128 a[44:47], v245 offset:34816\n" \
  "v_exp_f32 v210, v210\n" \
  "v_add_f32 v209, 1.0, v209\n" \
  "v_mfma_f32_16x16x32_bf16 a[168:171], a[80:83], a[112:115], a[168:171]\n" \
  "ds_read_b128 a[48:51], v244 offset:36864\n" \
  "v_rcp_f32 v208, v208\n" \
  "v_mfma_f32_16x16x32_bf16 a[168:171], a[84:87], a[116:119], a[168:171]\n" \
  "s_add_u32 m0, m0, 2048\n" \
  "v_mul_f32 v216, s86, v216\n" \
  "v_mfma_f32_16x16x32_bf16 a[172:175], a[92:95], a[116:119], a[172:175]\n" \
  "buffer_load_dwordx4 v234, s[28:31], s85 offen sc1 lds\n" \
  "v_mfma_f32_16x16x32_bf16 a[172:175], a[88:91], a[112:115], a[172:175]\n" \
  "ds_read_b128 a[52:55], v245 offset:36864\n" \
  "v_exp_f32 v211, v211\n" \
  "v_add_f32 v210, 1.0, v210\n" \
  "v_mfma_f32_16x16x32_bf16 a[176:179], a[64:67], a[120:123], a[176:179]\n" \
  "ds_read_b128 a[56:59], v244 offset:38912\n" \
  "v_rcp_f32 v209, v209\n" \
  "v_mfma_f32_16x16x32_bf16 a[176:179], a[68:71], a[124:127], a[176:179]\n" \
  "buffer_load_dwordx4 v234, s[28:31], s85 offen offset:1024 sc1 lds\n" \
  "v_mul_f32 v208, v208, v212\n" \
  "v_mfma_f32_16x16x32_bf16 a[180:183], a[76:79], a[124:127], a[180:183]\n" \
  "ds_read_b128 a[60:63], v245 offset:38912\n" \
  "v_mul_f32 v217, s86, v217\n" \
  "v_mfma_f32_16x16x32_bf16 a[180:183], a[72:75], a[120:123], a[180:183]\n" \
  "v_exp_f32 v216, v216\n" \
  "v_add_f32 v211, 1.0, v211\n" \
  "v_mfma_f32_16x16x32_bf16 a[184:187], a[80:83], a[120:123], a[184:187]\n" \
  "s_add_u32 s28, s28, 2048\n" \
  "s_addc_u32 s29, s29, 0\n" \
  "v_rcp_f32 v210, v210\n" \
  "v_mfma_f32_16x16x32_bf16 a[184:187], a[84:87], a[124:127], a[184:187]\n" \
  "v_mul_f32 v209, v209, v213\n" \
  "v_mfma_f32_16x16x32_bf16 a[188:191], a[92:95], a[124:127], a[188:191]\n" \
  "s_add_u32 s32, s32, 229376\n" \
  "s_addc_u32 s33, s33, 0\n" \
  "v_mul_f32 v218, s86, v218\n" \
  "v_mfma_f32_16x16x32_bf16 a[188:191], a[88:91], a[120:123], a[188:191]\n" \
  "v_exp_f32 v217, v217\n" \
  "v_add_f32 v216, 1.0, v216\n" \
  "s_waitcnt lgkmcnt(0)\n" \
  "s_waitcnt vmcnt(17)\n" \
  "s_barrier\n" \
  "v_mfma_f32_16x16x32_bf16 a[128:131], a[0:3], a[32:35], a[128:131]\n" \
  "s_add_u32 m0, s93, 32768\n" \
  "ds_read_b128 a[96:99], v246 offset:0\n" \
  "v_mfma_f32_16x16x32_bf16 a[128:131], a[4:7], a[36:39], a[128:131]\n" \
  "buffer_load_dwordx4 v235, s[32:35], s96 offen lds\n" \
  "v_mfma_f32_16x16x32_bf16 a[132:135], a[12:15], a[36:39], a[132:135]\n" \
  "ds_read_b128 a[100:103], v247 offset:0\n" \
  "v_rcp_f32 v211, v211\n" \
  "v_mul_f32 v210, v210, v214\n" \
  "v_mfma_f32_16x16x32_bf16 a[132:135], a[8:11], a[32:35], a[132:135]\n" \
  "s_add_u32 m0, m0, s82\n" \
  "ds_read_b128 a[64:67], v242 offset:0\n" \
  "v_mul_f32 v219, s86, v219\n" \
  "v_mfma_f32_16x16x32_bf16 a[136:139], a[16:19], a[32:35], a[136:139]\n" \
  "buffer_load_dwordx4 v235, s[32:35], s97 offen lds\n" \
  "v_mfma_f32_16x16x32_bf16 a[136:139], a[20:23], a[36:39], a[136:139]\n" \
  "ds_read_b128 a[68:71], v243 offset:0\n" \
  "v_exp_f32 v218, v218\n" \
  "v_add_f32 v217, 1.0, v217\n" \
  "v_mfma_f32_16x16x32_bf16 a[140:143], a[28:31], a[36:39], a[140:143]\n" \
  "ds_read_b128 a[72:75], v242 offset:2048\n" \
  "v_rcp_f32 v216, v216\n" \
  "v_mfma_f32_16x16x32_bf16 a[140:143], a[24:27], a[32:35], a[140:143]\n" \
  "s_add_u32 m0, m0, s82\n" \
  "ds_read_b128 a[76:79], v243 offset:2048\n" \
  "v_mul_f32 v211, v211, v215\n" \
  "v_mfma_f32_16x16x32_bf16 a[144:147], a[0:3], a[40:43], a[144:147]\n" \
  "buffer_load_dwordx4 v235, s[32:35], s98 offen lds\n" \
  "v_mfma_f32_16x16x32_bf16 a[144:147], a[4:7], a[44:47], a[144:147]\n" \
  "ds_read_b128 a[80:83], v242 offset:4096\n" \
  "v_exp_f32 v219, v219\n" \
  "v_add_f32 v218, 1.0, v218\n" \
  "v_mfma_f32_16x16x32_bf16 a[148:151], a[12:15], a[44:47], a[148:151]\n" \
  "s_add_u32 m0, m0, s82\n" \
  "v_rcp_f32 v217, v217\n" \
  "v_mfma_f32_16x16x32_bf16 a[148:151], a[8:11], a[40:43], a[148:151]\n" \
  "buffer_load_dwordx4 v235, s[32:35], s99 offen lds\n" \
  "v_mul_f32 v216, v216, v220\n" \
  "v_mfma_f32_16x16x32_bf16 a[152:155], a[16:19], a[40:43], a[152:155]\n" \
  "ds_read_b128 a[84:87], v243 offset:4096\n" \
  "v_add_f32 v219, 1.0, v219\n" \
  "v_mfma_f32_16x16x32_bf16 a[152:155], a[20:23], a[44:47], a[152:155]\n" \
  "ds_read_b128 a[88:91], v242 offset:6144\n" \
  "v_rcp_f32 v218, v218\n" \
  "v_mul_f32 v217, v217, v221\n" \
  "v_mfma_f32_16x16x32_bf16 a[156:159], a[28:31], a[44:47], a[156:159]\n" \
  "s_add_u32 m0, s92, 32768\n" \
  "v_rcp_f32 v219, v219\n" \
  "v_mfma_f32_16x16x32_bf16 a[156:159], a[24:27], a[40:43], a[156:159]\n" \
  "buffer_load_dwordx4 v234, s[28:31], 0 offen sc1 lds\n" \
  "v_mul_f32 v218, v218, v222\n" \
  "v_mfma_f32_16x16x32_bf16 a[160:163], a[0:3], a[48:51], a[160:163]\n" \
  "ds_read_b128 a[92:95], v243 offset:6144\n" \
  "v_mul_f32 v219, v219, v223\n" \
  "v_cvt_pk_bf16_f32 v212, v208, v209\n" \
  "v_mfma_f32_16x16x32_bf16 a[160:163], a[4:7], a[52:55], a[160:163]\n" \
  "ds_read_b128 a[104:107], v246 offset:2048\n" \
  "v_cvt_pk_bf16_f32 v213, v210, v211\n" \
  "v_cvt_pk_bf16_f32 v214, v216, v217\n" \
  "v_mfma_f32_16x16x32_bf16 a[164:167], a[12:15], a[52:55], a[164:167]\n" \
  "buffer_load_dwordx4 v234, s[28:31], 0 offen offset:1024 sc1 lds\n" \
  "v_cvt_pk_bf16_f32 v215, v218, v219\n" \
  "v_mfma_f32_16x16x32_bf16 a[164:167], a[8:11], a[48:51], a[164:167]\n" \
  "ds_read_b128 a[108:111], v247 offset:2048\n" \
  "s_mov_b32 s69, 458752\n" \
  "v_permlane16_swap_b32 v212, v214\n" \
  "v_mfma_f32_16x16x32_bf16 a[168:171], a[16:19], a[48:51], a[168:171]\n" \
  "ds_read_b128 a[112:115], v246 offset:4096\n" \
  "v_permlane16_swap_b32 v213, v215\n" \
  "v_mfma_f32_16x16x32_bf16 a[168:171], a[20:23], a[52:55], a[168:171]\n" \
  "s_add_u32 m0, m0, 2048\n" \
  "buffer_store_dwordx4 v[212:215], v236, s[24:27], s69 offen sc1\n" \
  "v_mfma_f32_16x16x32_bf16 a[172:175], a[28:31], a[52:55], a[172:175]\n" \
  "buffer_load_dwordx4 v234, s[28:31], s85 offen sc1 lds\n" \
  "v_mfma_f32_16x16x32_bf16 a[172:175], a[24:27], a[48:51], a[172:175]\n" \
  "ds_read_b128 a[116:119], v247 offset:4096\n" \
  "v_mfma_f32_16x16x32_bf16 a[176:179], a[0:3], a[56:59], a[176:179]\n" \
  "ds_read_b128 a[120:123], v246 offset:6144\n" \
  "v_mfma_f32_16x16x32_bf16 a[176:179], a[4:7], a[60:63], a[176:179]\n" \
  "buffer_load_dwordx4 v234, s[28:31], s85 offen offset:1024 sc1 lds\n" \
  "v_mfma_f32_16x16x32_bf16 a[180:183], a[12:15], a[60:63], a[180:183]\n" \
  "ds_read_b128 a[124:127], v247 offset:6144\n" \
  "v_mfma_f32_16x16x32_bf16 a[180:183], a[8:11], a[56:59], a[180:183]\n" \
  "v_mfma_f32_16x16x32_bf16 a[184:187], a[16:19], a[56:59], a[184:187]\n" \
  "s_add_u32 s28, s28, 2048\n" \
  "s_addc_u32 s29, s29, 0\n" \
  "v_mfma_f32_16x16x32_bf16 a[184:187], a[20:23], a[60:63], a[184:187]\n" \
  "v_mfma_f32_16x16x32_bf16 a[188:191], a[28:31], a[60:63], a[188:191]\n" \
  "s_add_u32 s32, s32, 229376\n" \
  "s_addc_u32 s33, s33, 0\n" \
  "v_mfma_f32_16x16x32_bf16 a[188:191], a[24:27], a[56:59], a[188:191]\n" \
  "s_waitcnt lgkmcnt(0)\n" \
  "s_waitcnt vmcnt(18)\n" \
  "s_barrier\n" \
  "v_mfma_f32_16x16x32_bf16 a[128:131], a[64:67], a[96:99], a[128:131]\n" \
  "s_add_u32 m0, s93, 65536\n" \
  "ds_read_b64_tr_b16 a[32:33], v226 offset:0\n" \
  "v_mfma_f32_16x16x32_bf16 a[128:131], a[68:71], a[100:103], a[128:131]\n" \
  "buffer_load_dwordx4 v235, s[32:35], s96 offen lds\n" \
  "v_mfma_f32_16x16x32_bf16 a[132:135], a[76:79], a[100:103], a[132:135]\n" \
  "ds_read_b64_tr_b16 a[34:35], v226 offset:1024\n" \
  "ds_read_b64_tr_b16 a[36:37], v226 offset:8192\n" \
  "v_mfma_f32_16x16x32_bf16 a[132:135], a[72:75], a[96:99], a[132:135]\n" \
  "s_add_u32 m0, m0, s82\n" \
  "ds_read_b64_tr_b16 a[38:39], v226 offset:9216\n" \
  "v_mfma_f32_16x16x32_bf16 a[136:139], a[80:83], a[96:99], a[136:139]\n" \
  "buffer_load_dwordx4 v235, s[32:35], s97 offen lds\n" \
  "v_mfma_f32_16x16x32_bf16 a[136:139], a[84:87], a[100:103], a[136:139]\n" \
  "ds_read_b128 a[0:3], v224 offset:0\n" \
  "ds_read_b128 a[4:7], v224 offset:1024\n" \
  "v_mfma_f32_16x16x32_bf16 a[140:143], a[92:95], a[100:103], a[140:143]\n" \
  "ds_read_b128 a[8:11], v224 offset:2048\n" \
  "ds_read_b128 a[12:15], v224 offset:3072\n" \
  "v_mfma_f32_16x16x32_bf16 a[140:143], a[88:91], a[96:99], a[140:143]\n" \
  "s_add_u32 m0, m0, s82\n" \
  "ds_read_b128 a[16:19], v224 offset:4096\n" \
  "v_mfma_f32_16x16x32_bf16 a[144:147], a[64:67], a[104:107], a[144:147]\n" \
  "buffer_load_dwordx4 v235, s[32:35], s98 offen lds\n" \
  "v_mfma_f32_16x16x32_bf16 a[144:147], a[68:71], a[108:111], a[144:147]\n" \
  "ds_read_b128 a[20:23], v224 offset:5120\n" \
  "ds_read_b128 a[24:27], v224 offset:6144\n" \
  "v_mfma_f32_16x16x32_bf16 a[148:151], a[76:79], a[108:111], a[148:151]\n" \
  "s_add_u32 m0, m0, s82\n" \
  "ds_read_b128 a[28:31], v224 offset:7168\n" \
  "v_mfma_f32_16x16x32_bf16 a[148:151], a[72:75], a[104:107], a[148:151]\n" \
  "buffer_load_dwordx4 v235, s[32:35], s99 offen lds\n" \
  "v_mfma_f32_16x16x32_bf16 a[152:155], a[80:83], a[104:107], a[152:155]\n" \
  "ds_read_b64_tr_b16 a[40:41], v227 offset:0\n" \
  "ds_read_b64_tr_b16 a[42:43], v227 offset:1024\n" \
  "v_mfma_f32_16x16x32_bf16 a[152:155], a[84:87], a[108:111], a[152:155]\n" \
  "ds_read_b64_tr_b16 a[44:45], v227 offset:8192\n" \
  "v_mfma_f32_16x16x32_bf16 a[156:159], a[92:95], a[108:111], a[156:159]\n" \
  "s_add_u32 m0, s92, 65536\n" \
  "ds_read_b64_tr_b16 a[46:47], v227 offset:9216\n" \
  "v_mfma_f32_16x16x32_bf16 a[156:159], a[88:91], a[104:107], a[156:159]\n" \
  "buffer_load_dwordx4 v234, s[28:31], 0 offen sc1 lds\n" \
  "v_mfma_f32_16x16x32_bf16 a[160:163], a[64:67], a[112:115], a[160:163]\n" \
  "ds_read_b64_tr_b16 a[48:49], v228 offset:0\n" \
  "v_mfma_f32_16x16x32_bf16 a[160:163], a[68:71], a[116:119], a[160:163]\n" \
  "ds_read_b64_tr_b16 a[50:51], v228 offset:1024\n" \
  "v_mfma_f32_16x16x32_bf16 a[164:167], a[76:79], a[116:119], a[164:167]\n" \
  "buffer_load_dwordx4 v234, s[28:31], 0 offen offset:1024 sc1 lds\n" \
  "v_mfma_f32_16x16x32_bf16 a[164:167], a[72:75], a[112:115], a[164:167]\n" \
  "ds_read_b64_tr_b16 a[52:53], v228 offset:8192\n" \
  "v_mfma_f32_16x16x32_bf16 a[168:171], a[80:83], a[112:115], a[168:171]\n" \
  "ds_read_b64_tr_b16 a[54:55], v228 offset:9216\n" \
  "v_mfma_f32_16x16x32_bf16 a[168:171], a[84:87], a[116:119], a[168:171]\n" \
  "s_add_u32 m0, m0, 2048\n" \
  "ds_read_b64_tr_b16 a[56:57], v229 offset:0\n" \
  "v_mfma_f32_16x16x32_bf16 a[172:175], a[92:95], a[116:119], a[172:175]\n" \
  "buffer_load_dwordx4 v234, s[28:31], s85 offen sc1 lds\n" \
  "v_mfma_f32_16x16x32_bf16 a[172:175], a[88:91], a[112:115], a[172:175]\n" \
  "ds_read_b64_tr_b16 a[58:59], v229 offset:1024\n" \
  "v_mfma_f32_16x16x32_bf16 a[176:179], a[64:67], a[120:123], a[176:179]\n" \
  "ds_read_b64_tr_b16 a[60:61], v229 offset:8192\n" \
  "v_mfma_f32_16x16x32_bf16 a[176:179], a[68:71], a[124:127], a[176:179]\n" \
  "buffer_load_dwordx4 v234, s[28:31], s85 offen offset:1024 sc1 lds\n" \
  "v_mfma_f32_16x16x32_bf16 a[180:183], a[76:79], a[124:127], a[180:183]\n" \
  "ds_read_b64_tr_b16 a[62:63], v229 offset:9216\n" \
  "v_mfma_f32_16x16x32_bf16 a[180:183], a[72:75], a[120:123], a[180:183]\n" \
  "v_mfma_f32_16x16x32_bf16 a[184:187], a[80:83], a[120:123], a[184:187]\n" \
  "s_add_u32 s28, s28, 2048\n" \
  "s_addc_u32 s29, s29, 0\n" \
  "v_mfma_f32_16x16x32_bf16 a[184:187], a[84:87], a[124:127], a[184:187]\n" \
  "v_mfma_f32_16x16x32_bf16 a[188:191], a[92:95], a[124:127], a[188:191]\n" \
  "s_add_u32 s32, s32, 229376\n" \
  "s_addc_u32 s33, s33, 0\n" \
  "v_mfma_f32_16x16x32_bf16 a[188:191], a[88:91], a[120:123], a[188:191]\n" \
  "s_waitcnt lgkmcnt(0)\n" \
  "s_waitcnt vmcnt(17)\n" \
  "s_barrier\n" \
  "v_mfma_f32_16x16x32_bf16 v[0:3], a[32:35], a[0:3], 0\n" \
  "s_add_u32 m0, s93, 0\n" \
  "ds_read_b64_tr_b16 a[96:97], v230 offset:32768\n" \
  "v_accvgpr_read_b32 v128, a224\n" \
  "v_accvgpr_read_b32 v129, a225\n" \
  "v_mfma_f32_16x16x32_bf16 v[0:3], a[36:39], a[4:7], v[0:3]\n" \
  "buffer_load_dwordx4 v235, s[32:35], s96 offen lds\n" \
  "v_accvgpr_read_b32 v140, a236\n" \
  "v_mfma_f32_16x16x32_bf16 v[4:7], a[36:39], a[12:15], 0\n" \
  "ds_read_b64_tr_b16 a[98:99], v230 offset:33792\n" \
  "ds_read_b64_tr_b16 a[100:101], v230 offset:40960\n" \
  "v_accvgpr_read_b32 v141, a237\n" \
  "v_accvgpr_read_b32 v64, a128\n" \
  "v_mfma_f32_16x16x32_bf16 v[4:7], a[32:35], a[8:11], v[4:7]\n" \
  "s_add_u32 m0, m0, s82\n" \
  "ds_read_b64_tr_b16 a[102:103], v230 offset:41984\n" \
  "v_accvgpr_read_b32 v80, a144\n" \
  "v_accvgpr_read_b32 v96, a160\n" \
  "v_mfma_f32_16x16x32_bf16 v[8:11], a[32:35], a[16:19], 0\n" \
  "buffer_load_dwordx4 v235, s[32:35], s97 offen lds\n" \
  "v_accvgpr_read_b32 v112, a176\n" \
  "v_mfma_f32_16x16x32_bf16 v[8:11], a[36:39], a[20:23], v[8:11]\n" \
  "ds_read_b128 a[64:67], v225 offset:32768\n" \
  "ds_read_b128 a[68:71], v225 offset:33792\n" \
  "v_mov_b32 v204, v64\n" \
  "v_mov_b32 v205, v80\n" \
  "v_mfma_f32_16x16x32_bf16 v[12:15], a[36:39], a[28:31], 0\n" \
  "ds_read_b128 a[72:75], v225 offset:34816\n" \
  "ds_read_b128 a[76:79], v225 offset:35840\n" \
  "v_mov_b32 v206, v96\n" \
  "v_mov_b32 v207, v112\n" \
  "v_mfma_f32_16x16x32_bf16 v[12:15], a[32:35], a[24:27], v[12:15]\n" \
  "s_add_u32 m0, m0, s82\n" \
  "ds_read_b128 a[80:83], v225 offset:36864\n" \
  "v_accvgpr_read_b32 v65, a129\n" \
  "v_accvgpr_read_b32 v81, a145\n" \
  "v_mfma_f32_16x16x32_bf16 v[16:19], a[40:43], a[0:3], 0\n" \
  "buffer_load_dwordx4 v235, s[32:35], s98 offen lds\n" \
  "v_accvgpr_read_b32 v97, a161\n" \
  "v_mfma_f32_16x16x32_bf16 v[16:19], a[44:47], a[4:7], v[16:19]\n" \
  "ds_read_b128 a[84:87], v225 offset:37888\n" \
  "ds_read_b128 a[88:91], v225 offset:38912\n" \
  "v_accvgpr_read_b32 v113, a177\n" \
  "v_fma_f32 v212, -v129, v205, v65\n" \
  "v_mfma_f32_16x16x32_bf16 v[20:23], a[44:47], a[12:15], 0\n" \
  "s_add_u32 m0, m0, s82\n" \
  "ds_read_b128 a[92:95], v225 offset:39936\n" \
  "v_fma_f32 v213, v129, v204, v81\n" \
  "v_fma_f32 v214, -v141, v207, v97\n" \
  "v_mfma_f32_16x16x32_bf16 v[20:23], a[40:43], a[8:11], v[20:23]\n" \
  "buffer_load_dwordx4 v235, s[32:35], s99 offen lds\n" \
  "v_fma_f32 v215, v141, v206, v113\n" \
  "v_mfma_f32_16x16x32_bf16 v[24:27], a[40:43], a[16:19], 0\n" \
  "ds_read_b64_tr_b16 a[104:105], v231 offset:32768\n" \
  "ds_read_b64_tr_b16 a[106:107], v231 offset:33792\n" \
  "v_fma_f32 v204, v128, v204, v212\n" \
  "v_fma_f32 v205, v128, v205, v213\n" \
  "v_mfma_f32_16x16x32_bf16 v[24:27], a[44:47], a[20:23], v[24:27]\n" \
  "ds_read_b64_tr_b16 a[108:109], v231 offset:40960\n" \
  "v_fma_f32 v206, v140, v206, v214\n" \
  "v_mfma_f32_16x16x32_bf16 v[28:31], a[44:47], a[28:31], 0\n" \
  "s_add_u32 m0, s92, 0\n" \
  "ds_read_b64_tr_b16 a[110:111], v231 offset:41984\n" \
  "v_fma_f32 v207, v140, v207, v215\n" \
  "v_accvgpr_read_b32 v66, a130\n" \
  "v_mfma_f32_16x16x32_bf16 v[28:31], a[40:43], a[24:27], v[28:31]\n" \
  "buffer_load_dwordx4 v234, s[28:31], 0 offen sc1 lds\n" \
  "v_accvgpr_read_b32 v82, a146\n" \
  "v_mfma_f32_16x16x32_bf16 v[32:35], a[48:51], a[0:3], 0\n" \
  "ds_read_b64_tr_b16 a[112:113], v232 offset:32768\n" \
  "v_accvgpr_read_b32 v98, a162\n" \
  "v_accvgpr_read_b32 v114, a178\n" \
  "v_mfma_f32_16x16x32_bf16 v[32:35], a[52:55], a[4:7], v[32:35]\n" \
  "ds_read_b64_tr_b16 a[114:115], v232 offset:33792\n" \
  "v_fma_f32 v212, -v129, v205, v66\n" \
  "v_mfma_f32_16x16x32_bf16 v[36:39], a[52:55], a[12:15], 0\n" \
  "buffer_load_dwordx4 v234, s[28:31], 0 offen offset:1024 sc1 lds\n" \
  "v_fma_f32 v213, v129, v204, v82\n" \
  "v_mfma_f32_16x16x32_bf16 v[36:39], a[48:51], a[8:11], v[36:39]\n" \
  "ds_read_b64_tr_b16 a[116:117], v232 offset:40960\n" \
  "v_fma_f32 v214, -v141, v207, v98\n" \
  "v_fma_f32 v215, v141, v206, v114\n" \
  "v_mfma_f32_16x16x32_bf16 v[40:43], a[48:51], a[16:19], 0\n" \
  "ds_read_b64_tr_b16 a[118:119], v232 offset:41984\n" \
  "v_fma_f32 v204, v128, v204, v212\n" \
  "v_fma_f32 v205, v128, v205, v213\n" \
  "v_mfma_f32_16x16x32_bf16 v[40:43], a[52:55], a[20:23], v[40:43]\n" \
  "s_add_u32 m0, m0, 2048\n" \
  "ds_read_b64_tr_b16 a[120:121], v233 offset:32768\n" \
  "v_fma_f32 v206, v140, v206, v214\n" \
  "v_mfma_f32_16x16x32_bf16 v[44:47], a[52:55], a[28:31], 0\n" \
  "buffer_load_dwordx4 v234, s[28:31], s85 offen sc1 lds\n" \
  "v_fma_f32 v207, v140, v207, v215\n" \
  "v_mfma_f32_16x16x32_bf16 v[44:47], a[48:51], a[24:27], v[44:47]\n" \
  "ds_read_b64_tr_b16 a[122:123], v233 offset:33792\n" \
  "v_accvgpr_read_b32 v67, a131\n" \
  "v_accvgpr_read_b32 v83, a147\n" \
  "v_mfma_f32_16x16x32_bf16 v[48:51], a[56:59], a[0:3], 0\n" \
  "ds_read_b64_tr_b16 a[124:125], v233 offset:40960\n" \
  "v_accvgpr_read_b32 v99, a163\n" \
  "v_accvgpr_read_b32 v115, a179\n" \
  "v_mfma_f32_16x16x32_bf16 v[48:51], a[60:63], a[4:7], v[48:51]\n" \
  "buffer_load_dwordx4 v234, s[28:31], s85 offen offset:1024 sc1 lds\n" \
  "v_fma_f32 v212, -v129, v205, v67\n" \
  "v_mfma_f32_16x16x32_bf16 v[52:55], a[60:63], a[12:15], 0\n" \
  "ds_read_b64_tr_b16 a[126:127], v233 offset:41984\n" \
  "v_fma_f32 v213, v129, v204, v83\n" \
  "v_fma_f32 v214, -v141, v207, v99\n" \
  "v_mfma_f32_16x16x32_bf16 v[52:55], a[56:59], a[8:11], v[52:55]\n" \
  "v_fma_f32 v215, v141, v206, v115\n" \
  "v_mfma_f32_16x16x32_bf16 v[56:59], a[56:59], a[16:19], 0\n" \
  "s_add_u32 s28, s28, 2048\n" \
  "s_addc_u32 s29, s29, 0\n" \
  "v_fma_f32 v204, v128, v204, v212\n" \
  "v_fma_f32 v205, v128, v205, v213\n" \
  "v_mfma_f32_16x16x32_bf16 v[56:59], a[60:63], a[20:23], v[56:59]\n" \
  "v_fma_f32 v206, v140, v206, v214\n" \
  "v_mfma_f32_16x16x32_bf16 v[60:63], a[60:63], a[28:31], 0\n" \
  "s_add_u32 s32, s32, 229376\n" \
  "s_addc_u32 s33, s33, 0\n" \
  "v_fma_f32 v207, v140, v207, v215\n" \
  "v_accvgpr_read_b32 v68, a132\n" \
  "v_mfma_f32_16x16x32_bf16 v[60:63], a[56:59], a[24:27], v[60:63]\n" \
  "v_accvgpr_read_b32 v84, a148\n" \
  "s_waitcnt lgkmcnt(0)\n" \
  "s_waitcnt vmcnt(16)\n" \
  "s_barrier\n" \
  "v_mfma_f32_16x16x32_bf16 v[0:3], a[96:99], a[64:67], v[0:3]\n" \
  "s_add_u32 m0, s93, 98304\n" \
  "ds_read_b64_tr_b16 a[32:33], v226 offset:32768\n" \
  "v_accvgpr_read_b32 v100, a164\n" \
  "v_accvgpr_read_b32 v116, a180\n" \
  "v_mfma_f32_16x16x32_bf16 v[0:3], a[100:103], a[68:71], v[0:3]\n" \
  "buffer_load_dwordx4 v235, s[32:35], s96 offen lds\n" \
  "v_fma_f32 v212, -v129, v205, v68\n" \
  "v_mfma_f32_16x16x32_bf16 v[4:7], a[100:103], a[76:79], v[4:7]\n" \
  "ds_read_b64_tr_b16 a[34:35], v226 offset:33792\n" \
  "ds_read_b64_tr_b16 a[36:37], v226 offset:40960\n" \
  "buffer_store_dwordx2 v[254:255], v237, s[52:55], s0 offen sc1\n" \
  "v_mfma_f32_16x16x32_bf16 v[4:7], a[96:99], a[72:75], v[4:7]\n" \
  "s_add_u32 m0, m0, s82\n" \
  "ds_read_b64_tr_b16 a[38:39], v226 offset:41984\n" \
  "v_fma_f32 v213, v129, v204, v84\n" \
  "v_fma_f32 v214, -v141, v207, v100\n" \
  "v_mfma_f32_16x16x32_bf16 v[8:11], a[96:99], a[80:83], v[8:11]\n" \
  "buffer_load_dwordx4 v235, s[32:35], s97 offen lds\n" \
  "v_fma_f32 v215, v141, v206, v116\n" \
  "v_mfma_f32_16x16x32_bf16 v[8:11], a[100:103], a[84:87], v[8:11]\n" \
  "ds_read_b128 a[0:3], v224 offset:32768\n" \
  "ds_read_b128 a[4:7], v224 offset:33792\n" \
  "v_fma_f32 v204, v128, v204, v212\n" \
  "v_fma_f32 v205, v128, v205, v213\n" \
  "v_mfma_f32_16x16x32_bf16 v[12:15], a[100:103], a[92:95], v[12:15]\n" \
  "ds_read_b128 a[8:11], v224 offset:34816\n" \
  "ds_read_b128 a[12:15], v224 offset:35840\n" \
  "v_fma_f32 v206, v140, v206, v214\n" \
  "v_fma_f32 v207, v140, v207, v215\n" \
  "v_mfma_f32_16x16x32_bf16 v[12:15], a[96:99], a[88:91], v[12:15]\n" \
  "s_add_u32 m0, m0, s82\n" \
  "ds_read_b128 a[16:19], v224 offset:36864\n" \
  "v_accvgpr_read_b32 v69, a133\n" \
  "v_accvgpr_read_b32 v85, a149\n" \
  "v_mfma_f32_16x16x32_bf16 v[16:19], a[104:107], a[64:67], v[16:19]\n" \
  "buffer_load_dwordx4 v235, s[32:35], s98 offen lds\n" \
  "v_accvgpr_read_b32 v101, a165\n" \
  "v_mfma_f32_16x16x32_bf16 v[16:19], a[108:111], a[68:71], v[16:19]\n" \
  "ds_read_b128 a[20:23], v224 offset:37888\n" \
  "ds_read_b128 a[24:27], v224 offset:38912\n" \
  "v_accvgpr_read_b32 v117, a181\n" \
  "v_fma_f32 v212, -v129, v205, v69\n" \
  "v_mfma_f32_16x16x32_bf16 v[20:23], a[108:111], a[76:79], v[20:23]\n" \
  "s_add_u32 m0, m0, s82\n" \
  "ds_read_b128 a[28:31], v224 offset:39936\n" \
  "v_fma_f32 v213, v129, v204, v85\n" \
  "v_fma_f32 v214, -v141, v207, v101\n" \
  "v_mfma_f32_16x16x32_bf16 v[20:23], a[104:107], a[72:75], v[20:23]\n" \
  "buffer_load_dwordx4 v235, s[32:35], s99 offen lds\n" \
  "v_fma_f32 v215, v141, v206, v117\n" \
  "v_mfma_f32_16x16x32_bf16 v[24:27], a[104:107], a[80:83], v[24:27]\n" \
  "ds_read_b64_tr_b16 a[40:41], v227 offset:32768\n" \
  "ds_read_b64_tr_b16 a[42:43], v227 offset:33792\n" \
  "v_fma_f32 v204, v128, v204, v212\n" \
  "v_fma_f32 v205, v128, v205, v213\n" \
  "v_mfma_f32_16x16x32_bf16 v[24:27], a[108:111], a[84:87], v[24:27]\n" \
  "ds_read_b64_tr_b16 a[44:45], v227 offset:40960\n" \
  "v_fma_f32 v206, v140, v206, v214\n" \
  "v_mfma_f32_16x16x32_bf16 v[28:31], a[108:111], a[92:95], v[28:31]\n" \
  "s_add_u32 m0, s92, 98304\n" \
  "ds_read_b64_tr_b16 a[46:47], v227 offset:41984\n" \
  "v_fma_f32 v207, v140, v207, v215\n" \
  "v_accvgpr_read_b32 v70, a134\n" \
  "v_mfma_f32_16x16x32_bf16 v[28:31], a[104:107], a[88:91], v[28:31]\n" \
  "buffer_load_dwordx4 v234, s[28:31], 0 offen sc1 lds\n" \
  "v_accvgpr_read_b32 v86, a150\n" \
  "v_mfma_f32_16x16x32_bf16 v[32:35], a[112:115], a[64:67], v[32:35]\n" \
  "ds_read_b64_tr_b16 a[48:49], v228 offset:32768\n" \
  "v_accvgpr_read_b32 v102, a166\n" \
  "v_accvgpr_read_b32 v118, a182\n" \
  "v_mfma_f32_16x16x32_bf16 v[32:35], a[116:119], a[68:71], v[32:35]\n" \
  "ds_read_b64_tr_b16 a[50:51], v228 offset:33792\n" \
  "v_fma_f32 v212, -v129, v205, v70\n" \
  "v_mfma_f32_16x16x32_bf16 v[36:39], a[116:119], a[76:79], v[36:39]\n" \
  "buffer_load_dwordx4 v234, s[28:31], 0 offen offset:1024 sc1 lds\n" \
  "v_fma_f32 v213, v129, v204, v86\n" \
  "v_mfma_f32_16x16x32_bf16 v[36:39], a[112:115], a[72:75], v[36:39]\n" \
  "ds_read_b64_tr_b16 a[52:53], v228 offset:40960\n" \
  "v_fma_f32 v214, -v141, v207, v102\n" \
  "v_fma_f32 v215, v141, v206, v118\n" \
  "v_mfma_f32_16x16x32_bf16 v[40:43], a[112:115], a[80:83], v[40:43]\n" \
  "ds_read_b64_tr_b16 a[54:55], v228 offset:41984\n" \
  "v_fma_f32 v204, v128, v204, v212\n" \
  "v_fma_f32 v205, v128, v205, v213\n" \
  "v_mfma_f32_16x16x32_bf16 v[40:43], a[116:119], a[84:87], v[40:43]\n" \
  "s_add_u32 m0, m0, 2048\n" \
  "ds_read_b64_tr_b16 a[56:57], v229 offset:32768\n" \
  "v_fma_f32 v206, v140, v206, v214\n" \
  "v_mfma_f32_16x16x32_bf16 v[44:47], a[116:119], a[92:95], v[44:47]\n" \
  "buffer_load_dwordx4 v234, s[28:31], s85 offen sc1 lds\n" \
  "v_fma_f32 v207, v140, v207, v215\n" \
  "v_mfma_f32_16x16x32_bf16 v[44:47], a[112:115], a[88:91], v[44:47]\n" \
  "ds_read_b64_tr_b16 a[58:59], v229 offset:33792\n" \
  "v_accvgpr_read_b32 v71, a135\n" \
  "v_accvgpr_read_b32 v87, a151\n" \
  "v_mfma_f32_16x16x32_bf16 v[48:51], a[120:123], a[64:67], v[48:51]\n" \
  "ds_read_b64_tr_b16 a[60:61], v229 offset:40960\n" \
  "v_accvgpr_read_b32 v103, a167\n" \
  "v_accvgpr_read_b32 v119, a183\n" \
  "v_mfma_f32_16x16x32_bf16 v[48:51], a[124:127], a[68:71], v[48:51]\n" \
  "buffer_load_dwordx4 v234, s[28:31], s85 offen offset:1024 sc1 lds\n" \
  "v_fma_f32 v212, -v129, v205, v71\n" \
  "v_mfma_f32_16x16x32_bf16 v[52:55], a[124:127], a[76:79], v[52:55]\n" \
  "ds_read_b64_tr_b16 a[62:63], v229 offset:41984\n" \
  "v_fma_f32 v213, v129, v204, v87\n" \
  "v_fma_f32 v214, -v141, v207, v103\n" \
  "v_mfma_f32_16x16x32_bf16 v[52:55], a[120:123], a[72:75], v[52:55]\n" \
  "v_fma_f32 v215, v141, v206, v119\n" \
  "v_mfma_f32_16x16x32_bf16 v[56:59], a[120:123], a[80:83], v[56:59]\n" \
  "s_add_u32 s28, s28, 2048\n" \
  "s_addc_u32 s29, s29, 0\n" \
  "v_fma_f32 v204, v128, v204, v212\n" \
  "v_fma_f32 v205, v128, v205, v213\n" \
  "v_mfma_f32_16x16x32_bf16 v[56:59], a[124:127], a[84:87], v[56:59]\n" \
  "v_fma_f32 v206, v140, v206, v214\n" \
  "v_mfma_f32_16x16x32_bf16 v[60:63], a[124:127], a[92:95], v[60:63]\n" \
  "s_add_u32 s32, s32, 229376\n" \
  "s_addc_u32 s33, s33, 0\n" \
  "v_fma_f32 v207, v140, v207, v215\n" \
  "v_accvgpr_read_b32 v72, a136\n" \
  "v_mfma_f32_16x16x32_bf16 v[60:63], a[120:123], a[88:91], v[60:63]\n" \
  "v_accvgpr_read_b32 v88, a152\n" \
  "s_waitcnt lgkmcnt(0)\n" \
  "s_waitcnt vmcnt(17)\n" \
  "s_barrier\n" \
  "v_mfma_f32_16x16x32_bf16 v[0:3], a[32:35], a[0:3], v[0:3]\n" \
  "s_add_u32 m0, s93, 32768\n" \
  "ds_read_b64_tr_b16 a[96:97], v230 offset:0\n" \
  "v_accvgpr_read_b32 v104, a168\n" \
  "v_accvgpr_read_b32 v120, a184\n" \
  "v_mfma_f32_16x16x32_bf16 v[0:3], a[36:39], a[4:7], v[0:3]\n" \
  "buffer_load_dwordx4 v235, s[32:35], s96 offen lds\n" \
  "v_fma_f32 v212, -v129, v205, v72\n" \
  "v_mfma_f32_16x16x32_bf16 v[4:7], a[36:39], a[12:15], v[4:7]\n" \
  "ds_read_b64_tr_b16 a[98:99], v230 offset:1024\n" \
  "ds_read_b64_tr_b16 a[100:101], v230 offset:8192\n" \
  "v_fma_f32 v213, v129, v204, v88\n" \
  "v_fma_f32 v214, -v141, v207, v104\n" \
  "v_mfma_f32_16x16x32_bf16 v[4:7], a[32:35], a[8:11], v[4:7]\n" \
  "s_add_u32 m0, m0, s82\n" \
  "ds_read_b64_tr_b16 a[102:103], v230 offset:9216\n" \
  "v_fma_f32 v215, v141, v206, v120\n" \
  "v_mfma_f32_16x16x32_bf16 v[8:11], a[32:35], a[16:19], v[8:11]\n" \
  "buffer_load_dwordx4 v235, s[32:35], s97 offen lds\n" \
  "v_fma_f32 v204, v128, v204, v212\n" \
  "v_mfma_f32_16x16x32_bf16 v[8:11], a[36:39], a[20:23], v[8:11]\n" \
  "ds_read_b128 a[64:67], v225 offset:0\n" \
  "ds_read_b128 a[68:71], v225 offset:1024\n" \
  "v_fma_f32 v205, v128, v205, v213\n" \
  "v_fma_f32 v206, v140, v206, v214\n" \
  "v_mfma_f32_16x16x32_bf16 v[12:15], a[36:39], a[28:31], v[12:15]\n" \
  "ds_read_b128 a[72:75], v225 offset:2048\n" \
  "ds_read_b128 a[76:79], v225 offset:3072\n" \
  "v_fma_f32 v207, v140, v207, v215\n" \
  "v_accvgpr_read_b32 v73, a137\n" \
  "v_mfma_f32_16x16x32_bf16 v[12:15], a[32:35], a[24:27], v[12:15]\n" \
  "s_add_u32 m0, m0, s82\n" \
  "ds_read_b128 a[80:83], v225 offset:4096\n" \
  "v_accvgpr_read_b32 v89, a153\n" \
  "v_mfma_f32_16x16x32_bf16 v[16:19], a[40:43], a[0:3], v[16:19]\n" \
  "buffer_load_dwordx4 v235, s[32:35], s98 offen lds\n" \
  "v_accvgpr_read_b32 v105, a169\n" \
  "v_mfma_f32_16x16x32_bf16 v[16:19], a[44:47], a[4:7], v[16:19]\n" \
  "ds_read_b128 a[84:87], v225 offset:5120\n" \
  "ds_read_b128 a[88:91], v225 offset:6144\n" \
  "v_accvgpr_read_b32 v121, a185\n" \
  "v_fma_f32 v212, -v129, v205, v73\n" \
  "v_mfma_f32_16x16x32_bf16 v[20:23], a[44:47], a[12:15], v[20:23]\n" \
  "s_add_u32 m0, m0, s82\n" \
  "ds_read_b128 a[92:95], v225 offset:7168\n" \
  "v_fma_f32 v213, v129, v204, v89\n" \
  "v_fma_f32 v214, -v141, v207, v105\n" \
  "v_mfma_f32_16x16x32_bf16 v[20:23], a[40:43], a[8:11], v[20:23]\n" \
  "buffer_load_dwordx4 v235, s[32:35], s99 offen lds\n" \
  "v_fma_f32 v215, v141, v206, v121\n" \
  "v_mfma_f32_16x16x32_bf16 v[24:27], a[40:43], a[16:19], v[24:27]\n" \
  "ds_read_b64_tr_b16 a[104:105], v231 offset:0\n" \
  "ds_read_b64_tr_b16 a[106:107], v231 offset:1024\n" \
  "v_fma_f32 v204, v128, v204, v212\n" \
  "v_fma_f32 v205, v128, v205, v213\n" \
  "v_mfma_f32_16x16x32_bf16 v[24:27], a[44:47], a[20:23], v[24:27]\n" \
  "ds_read_b64_tr_b16 a[108:109], v231 offset:8192\n" \
  "v_fma_f32 v206, v140, v206, v214\n" \
  "v_mfma_f32_16x16x32_bf16 v[28:31], a[44:47], a[28:31], v[28:31]\n" \
  "s_add_u32 m0, s92, 32768\n" \
  "ds_read_b64_tr_b16 a[110:111], v231 offset:9216\n" \
  "v_fma_f32 v207, v140, v207, v215\n" \
  "v_accvgpr_read_b32 v74, a138\n" \
  "v_mfma_f32_16x16x32_bf16 v[28:31], a[40:43], a[24:27], v[28:31]\n" \
  "buffer_load_dwordx4 v234, s[28:31], 0 offen sc1 lds\n" \
  "v_accvgpr_read_b32 v90, a154\n" \
  "v_mfma_f32_16x16x32_bf16 v[32:35], a[48:51], a[0:3], v[32:35]\n" \
  "ds_read_b64_tr_b16 a[112:113], v232 offset:0\n" \
  "v_accvgpr_read_b32 v106, a170\n" \
  "v_accvgpr_read_b32 v122, a186\n" \
  "v_mfma_f32_16x16x32_bf16 v[32:35], a[52:55], a[4:7], v[32:35]\n" \
  "ds_read_b64_tr_b16 a[114:115], v232 offset:1024\n" \
  "v_fma_f32 v212, -v129, v205, v74\n" \
  "v_mfma_f32_16x16x32_bf16 v[36:39], a[52:55], a[12:15], v[36:39]\n" \
  "buffer_load_dwordx4 v234, s[28:31], 0 offen offset:1024 sc1 lds\n" \
  "v_fma_f32 v213, v129, v204, v90\n" \
  "v_mfma_f32_16x16x32_bf16 v[36:39], a[48:51], a[8:11], v[36:39]\n" \
  "ds_read_b64_tr_b16 a[116:117], v232 offset:8192\n" \
  "v_fma_f32 v214, -v141, v207, v106\n" \
  "v_fma_f32 v215, v141, v206, v122\n" \
  "v_mfma_f32_16x16x32_bf16 v[40:43], a[48:51], a[16:19], v[40:43]\n" \
  "ds_read_b64_tr_b16 a[118:119], v232 offset:9216\n" \
  "v_fma_f32 v204, v128, v204, v212\n" \
  "v_fma_f32 v205, v128, v205, v213\n" \
  "v_mfma_f32_16x16x32_bf16 v[40:43], a[52:55], a[20:23], v[40:43]\n" \
  "s_add_u32 m0, m0, 2048\n" \
  "ds_read_b64_tr_b16 a[120:121], v233 offset:0\n" \
  "v_fma_f32 v206, v140, v206, v214\n" \
  "v_mfma_f32_16x16x32_bf16 v[44:47], a[52:55], a[28:31], v[44:47]\n" \
  "buffer_load_dwordx4 v234, s[28:31], s85 offen sc1 lds\n" \
  "v_fma_f32 v207, v140, v207, v215\n" \
  "v_mfma_f32_16x16x32_bf16 v[44:47], a[48:51], a[24:27], v[44:47]\n" \
  "ds_read_b64_tr_b16 a[122:123], v233 offset:1024\n" \
  "v_accvgpr_read_b32 v75, a139\n" \
  "v_accvgpr_read_b32 v91, a155\n" \
  "v_mfma_f32_16x16x32_bf16 v[48:51], a[56:59], a[0:3], v[48:51]\n" \
  "ds_read_b64_tr_b16 a[124:125], v233 offset:8192\n" \
  "v_accvgpr_read_b32 v107, a171\n" \
  "v_accvgpr_read_b32 v123, a187\n" \
  "v_mfma_f32_16x16x32_bf16 v[48:51], a[60:63], a[4:7], v[48:51]\n" \
  "buffer_load_dwordx4 v234, s[28:31], s85 offen offset:1024 sc1 lds\n" \
  "v_fma_f32 v212, -v129, v205, v75\n" \
  "v_mfma_f32_16x16x32_bf16 v[52:55], a[60:63], a[12:15], v[52:55]\n" \
  "ds_read_b64_tr_b16 a[126:127], v233 offset:9216\n" \
  "v_fma_f32 v213, v129, v204, v91\n" \
  "v_fma_f32 v214, -v141, v207, v107\n" \
  "v_mfma_f32_16x16x32_bf16 v[52:55], a[56:59], a[8:11], v[52:55]\n" \
  "v_fma_f32 v215, v141, v206, v123\n" \
  "v_mfma_f32_16x16x32_bf16 v[56:59], a[56:59], a[16:19], v[56:59]\n" \
  "s_add_u32 s28, s28, 2048\n" \
  "s_addc_u32 s29, s29, 0\n" \
  "v_fma_f32 v204, v128, v204, v212\n" \
  "v_fma_f32 v205, v128, v205, v213\n" \
  "v_mfma_f32_16x16x32_bf16 v[56:59], a[60:63], a[20:23], v[56:59]\n" \
  "v_fma_f32 v206, v140, v206, v214\n" \
  "v_mfma_f32_16x16x32_bf16 v[60:63], a[60:63], a[28:31], v[60:63]\n" \
  "s_add_u32 s32, s32, 229376\n" \
  "s_addc_u32 s33, s33, 0\n" \
  "v_fma_f32 v207, v140, v207, v215\n" \
  "v_accvgpr_read_b32 v76, a140\n" \
  "v_mfma_f32_16x16x32_bf16 v[60:63], a[56:59], a[24:27], v[60:63]\n" \
  "v_accvgpr_read_b32 v92, a156\n" \
  "s_waitcnt lgkmcnt(0)\n" \
  "s_waitcnt vmcnt(17)\n" \
  "s_barrier\n" \
  "v_mfma_f32_16x16x32_bf16 v[0:3], a[96:99], a[64:67], v[0:3]\n" \
  "s_add_u32 m0, s93, 65536\n" \
  "ds_read_b64_tr_b16 a[32:33], v226 offset:0\n" \
  "v_accvgpr_read_b32 v108, a172\n" \
  "v_accvgpr_read_b32 v124, a188\n" \
  "v_mfma_f32_16x16x32_bf16 v[0:3], a[100:103], a[68:71], v[0:3]\n" \
  "buffer_load_dwordx4 v235, s[32:35], s96 offen lds\n" \
  "v_fma_f32 v212, -v129, v205, v76\n" \
  "v_mfma_f32_16x16x32_bf16 v[4:7], a[100:103], a[76:79], v[4:7]\n" \
  "ds_read_b64_tr_b16 a[34:35], v226 offset:1024\n" \
  "ds_read_b64_tr_b16 a[36:37], v226 offset:8192\n" \
  "v_fma_f32 v213, v129, v204, v92\n" \
  "v_fma_f32 v214, -v141, v207, v108\n" \
  "v_mfma_f32_16x16x32_bf16 v[4:7], a[96:99], a[72:75], v[4:7]\n" \
  "s_add_u32 m0, m0, s82\n" \
  "ds_read_b64_tr_b16 a[38:39], v226 offset:9216\n" \
  "v_fma_f32 v215, v141, v206, v124\n" \
  "v_mfma_f32_16x16x32_bf16 v[8:11], a[96:99], a[80:83], v[8:11]\n" \
  "buffer_load_dwordx4 v235, s[32:35], s97 offen lds\n" \
  "v_fma_f32 v204, v128, v204, v212\n" \
  "v_mfma_f32_16x16x32_bf16 v[8:11], a[100:103], a[84:87], v[8:11]\n" \
  "ds_read_b128 a[0:3], v224 offset:0\n" \
  "ds_read_b128 a[4:7], v224 offset:1024\n" \
  "v_fma_f32 v205, v128, v205, v213\n" \
  "v_fma_f32 v206, v140, v206, v214\n" \
  "v_mfma_f32_16x16x32_bf16 v[12:15], a[100:103], a[92:95], v[12:15]\n" \
  "ds_read_b128 a[8:11], v224 offset:2048\n" \
  "ds_read_b128 a[12:15], v224 offset:3072\n" \
  "v_fma_f32 v207, v140, v207, v215\n" \
  "v_accvgpr_read_b32 v77, a141\n" \
  "v_mfma_f32_16x16x32_bf16 v[12:15], a[96:99], a[88:91], v[12:15]\n" \
  "s_add_u32 m0, m0, s82\n" \
  "ds_read_b128 a[16:19], v224 offset:4096\n" \
  "v_accvgpr_read_b32 v93, a157\n" \
  "v_mfma_f32_16x16x32_bf16 v[16:19], a[104:107], a[64:67], v[16:19]\n" \
  "buffer_load_dwordx4 v235, s[32:35], s98 offen lds\n" \
  "v_accvgpr_read_b32 v109, a173\n" \
  "v_mfma_f32_16x16x32_bf16 v[16:19], a[108:111], a[68:71], v[16:19]\n" \
  "ds_read_b128 a[20:23], v224 offset:5120\n" \
  "ds_read_b128 a[24:27], v224 offset:6144\n" \
  "v_accvgpr_read_b32 v125, a189\n" \
  "v_fma_f32 v212, -v129, v205, v77\n" \
  "v_mfma_f32_16x16x32_bf16 v[20:23], a[108:111], a[76:79], v[20:23]\n" \
  "s_add_u32 m0, m0, s82\n" \
  "ds_read_b128 a[28:31], v224 offset:7168\n" \
  "v_fma_f32 v213, v129, v204, v93\n" \
  "v_fma_f32 v214, -v141, v207, v109\n" \
  "v_mfma_f32_16x16x32_bf16 v[20:23], a[104:107], a[72:75], v[20:23]\n" \
  "buffer_load_dwordx4 v235, s[32:35], s99 offen lds\n" \
  "v_fma_f32 v215, v141, v206, v125\n" \
  "v_mfma_f32_16x16x32_bf16 v[24:27], a[104:107], a[80:83], v[24:27]\n" \
  "ds_read_b64_tr_b16 a[40:41], v227 offset:0\n" \
  "ds_read_b64_tr_b16 a[42:43], v227 offset:1024\n" \
  "v_fma_f32 v204, v128, v204, v212\n" \
  "v_fma_f32 v205, v128, v205, v213\n" \
  "v_mfma_f32_16x16x32_bf16 v[24:27], a[108:111], a[84:87], v[24:27]\n" \
  "ds_read_b64_tr_b16 a[44:45], v227 offset:8192\n" \
  "v_fma_f32 v206, v140, v206, v214\n" \
  "v_mfma_f32_16x16x32_bf16 v[28:31], a[108:111], a[92:95], v[28:31]\n" \
  "s_add_u32 m0, s92, 65536\n" \
  "ds_read_b64_tr_b16 a[46:47], v227 offset:9216\n" \
  "v_fma_f32 v207, v140, v207, v215\n" \
  "v_accvgpr_read_b32 v78, a142\n" \
  "v_mfma_f32_16x16x32_bf16 v[28:31], a[104:107], a[88:91], v[28:31]\n" \
  "buffer_load_dwordx4 v234, s[28:31], 0 offen sc1 lds\n" \
  "v_accvgpr_read_b32 v94, a158\n" \
  "v_mfma_f32_16x16x32_bf16 v[32:35], a[112:115], a[64:67], v[32:35]\n" \
  "ds_read_b64_tr_b16 a[48:49], v228 offset:0\n" \
  "v_accvgpr_read_b32 v110, a174\n" \
  "v_accvgpr_read_b32 v126, a190\n" \
  "v_mfma_f32_16x16x32_bf16 v[32:35], a[116:119], a[68:71], v[32:35]\n" \
  "ds_read_b64_tr_b16 a[50:51], v228 offset:1024\n" \
  "v_fma_f32 v212, -v129, v205, v78\n" \
  "v_mfma_f32_16x16x32_bf16 v[36:39], a[116:119], a[76:79], v[36:39]\n" \
  "buffer_load_dwordx4 v234, s[28:31], 0 offen offset:1024 sc1 lds\n" \
  "v_fma_f32 v213, v129, v204, v94\n" \
  "v_mfma_f32_16x16x32_bf16 v[36:39], a[112:115], a[72:75], v[36:39]\n" \
  "ds_read_b64_tr_b16 a[52:53], v228 offset:8192\n" \
  "v_fma_f32 v214, -v141, v207, v110\n" \
  "v_fma_f32 v215, v141, v206, v126\n" \
  "v_mfma_f32_16x16x32_bf16 v[40:43], a[112:115], a[80:83], v[40:43]\n" \
  "ds_read_b64_tr_b16 a[54:55], v228 offset:9216\n" \
  "v_fma_f32 v204, v128, v204, v212\n" \
  "v_fma_f32 v205, v128, v205, v213\n" \
  "v_mfma_f32_16x16x32_bf16 v[40:43], a[116:119], a[84:87], v[40:43]\n" \
  "s_add_u32 m0, m0, 2048\n" \
  "ds_read_b64_tr_b16 a[56:57], v229 offset:0\n" \
  "v_fma_f32 v206, v140, v206, v214\n" \
  "v_mfma_f32_16x16x32_bf16 v[44:47], a[116:119], a[92:95], v[44:47]\n" \
  "buffer_load_dwordx4 v234, s[28:31], s85 offen sc1 lds\n" \
  "v_fma_f32 v207, v140, v207, v215\n" \
  "v_mfma_f32_16x16x32_bf16 v[44:47], a[112:115], a[88:91], v[44:47]\n" \
  "ds_read_b64_tr_b16 a[58:59], v229 offset:1024\n" \
  "v_accvgpr_read_b32 v79, a143\n" \
  "v_accvgpr_read_b32 v95, a159\n" \
  "v_mfma_f32_16x16x32_bf16 v[48:51], a[120:123], a[64:67], v[48:51]\n" \
  "ds_read_b64_tr_b16 a[60:61], v229 offset:8192\n" \
  "v_accvgpr_read_b32 v111, a175\n" \
  "v_accvgpr_read_b32 v127, a191\n" \
  "v_mfma_f32_16x16x32_bf16 v[48:51], a[124:127], a[68:71], v[48:51]\n" \
  "buffer_load_dwordx4 v234, s[28:31], s85 offen offset:1024 sc1 lds\n" \
  "v_fma_f32 v212, -v129, v205, v79\n" \
  "v_mfma_f32_16x16x32_bf16 v[52:55], a[124:127], a[76:79], v[52:55]\n" \
  "ds_read_b64_tr_b16 a[62:63], v229 offset:9216\n" \
  "v_fma_f32 v213, v129, v204, v95\n" \
  "v_fma_f32 v214, -v141, v207, v111\n" \
  "v_mfma_f32_16x16x32_bf16 v[52:55], a[120:123], a[72:75], v[52:55]\n" \
  "v_fma_f32 v215, v141, v206, v127\n" \
  "v_mfma_f32_16x16x32_bf16 v[56:59], a[120:123], a[80:83], v[56:59]\n" \
  "s_add_u32 s28, s28, 2048\n" \
  "s_addc_u32 s29, s29, 0\n" \
  "v_fma_f32 v204, v128, v204, v212\n" \
  "v_fma_f32 v205, v128, v205, v213\n" \
  "v_mfma_f32_16x16x32_bf16 v[56:59], a[124:127], a[84:87], v[56:59]\n" \
  "v_fma_f32 v206, v140, v206, v214\n" \
  "v_mfma_f32_16x16x32_bf16 v[60:63], a[124:127], a[92:95], v[60:63]\n" \
  "s_add_u32 s32, s32, 229376\n" \
  "s_addc_u32 s33, s33, 0\n" \
  "v_fma_f32 v207, v140, v207, v215\n" \
  "v_accvgpr_read_b32 v220, a214\n" \
  "v_mfma_f32_16x16x32_bf16 v[60:63], a[120:123], a[88:91], v[60:63]\n" \
  "v_accvgpr_read_b32 v221, a215\n" \
  "s_waitcnt lgkmcnt(0)\n" \
  "s_waitcnt vmcnt(16)\n" \
  "s_barrier\n" \
  "v_mfma_f32_16x16x32_bf16 v[0:3], a[32:35], a[0:3], v[0:3]\n" \
  "s_add_u32 m0, s93, 0\n" \
  "ds_read_b64_tr_b16 a[96:97], v230 offset:32768\n" \
  "ds_write_b64 v220, v[204:205] offset:0\n" \
  "ds_write_b64 v220, v[206:207] offset:1280\n" \
  "v_mfma_f32_16x16x32_bf16 v[0:3], a[36:39], a[4:7], v[0:3]\n" \
  "buffer_load_dwordx4 v235, s[32:35], s96 offen lds\n" \
  "v_accvgpr_read_b32 v130, a226\n" \
  "v_mfma_f32_16x16x32_bf16 v[4:7], a[36:39], a[12:15], v[4:7]\n" \
  "ds_read_b64_tr_b16 a[98:99], v230 offset:33792\n" \
  "ds_read_b64_tr_b16 a[100:101], v230 offset:40960\n" \
  "v_accvgpr_read_b32 v131, a227\n" \
  "v_accvgpr_read_b32 v132, a228\n" \
  "v_mfma_f32_16x16x32_bf16 v[4:7], a[32:35], a[8:11], v[4:7]\n" \
  "s_add_u32 m0, m0, s82\n" \
  "ds_read_b64_tr_b16 a[102:103], v230 offset:41984\n" \
  "v_accvgpr_read_b32 v133, a229\n" \
  "v_mfma_f32_16x16x32_bf16 v[8:11], a[32:35], a[16:19], v[8:11]\n" \
  "buffer_load_dwordx4 v235, s[32:35], s97 offen lds\n" \
  "v_accvgpr_read_b32 v134, a230\n" \
  "v_mfma_f32_16x16x32_bf16 v[8:11], a[36:39], a[20:23], v[8:11]\n" \
  "ds_read_b128 a[64:67], v225 offset:32768\n" \
  "ds_read_b128 a[68:71], v225 offset:33792\n" \
  "v_accvgpr_read_b32 v135, a231\n" \
  "v_accvgpr_read_b32 v136, a232\n" \
  "v_mfma_f32_16x16x32_bf16 v[12:15], a[36:39], a[28:31], v[12:15]\n" \
  "ds_read_b128 a[72:75], v225 offset:34816\n" \
  "ds_read_b128 a[76:79], v225 offset:35840\n" \
  "v_accvgpr_read_b32 v137, a233\n" \
  "v_accvgpr_read_b32 v138, a234\n" \
  "v_mfma_f32_16x16x32_bf16 v[12:15], a[32:35], a[24:27], v[12:15]\n" \
  "s_add_u32 m0, m0, s82\n" \
  "ds_read_b128 a[80:83], v225 offset:36864\n" \
  "v_accvgpr_read_b32 v139, a235\n" \
  "v_mfma_f32_16x16x32_bf16 v[16:19], a[40:43], a[0:3], v[16:19]\n" \
  "buffer_load_dwordx4 v235, s[32:35], s98 offen lds\n" \
  "v_accvgpr_read_b32 v142, a238\n" \
  "v_mfma_f32_16x16x32_bf16 v[16:19], a[44:47], a[4:7], v[16:19]\n" \
  "ds_read_b128 a[84:87], v225 offset:37888\n" \
  "ds_read_b128 a[88:91], v225 offset:38912\n" \
  "v_accvgpr_read_b32 v143, a239\n" \
  "v_accvgpr_read_b32 v144, a240\n" \
  "v_mfma_f32_16x16x32_bf16 v[20:23], a[44:47], a[12:15], v[20:23]\n" \
  "s_add_u32 m0, m0, s82\n" \
  "ds_read_b128 a[92:95], v225 offset:39936\n" \
  "v_accvgpr_read_b32 v145, a241\n" \
  "v_accvgpr_read_b32 v146, a242\n" \
  "v_mfma_f32_16x16x32_bf16 v[20:23], a[40:43], a[8:11], v[20:23]\n" \
  "buffer_load_dwordx4 v235, s[32:35], s99 offen lds\n" \
  "v_accvgpr_read_b32 v147, a243\n" \
  "v_mfma_f32_16x16x32_bf16 v[24:27], a[40:43], a[16:19], v[24:27]\n" \
  "ds_read_b64_tr_b16 a[104:105], v231 offset:32768\n" \
  "ds_read_b64_tr_b16 a[106:107], v231 offset:33792\n" \
  "v_accvgpr_read_b32 v148, a244\n" \
  "v_accvgpr_read_b32 v149, a245\n" \
  "v_mfma_f32_16x16x32_bf16 v[24:27], a[44:47], a[20:23], v[24:27]\n" \
  "ds_read_b64_tr_b16 a[108:109], v231 offset:40960\n" \
  "v_accvgpr_read_b32 v150, a246\n" \
  "v_mfma_f32_16x16x32_bf16 v[28:31], a[44:47], a[28:31], v[28:31]\n" \
  "s_add_u32 m0, s92, 0\n" \
  "ds_read_b64_tr_b16 a[110:111], v231 offset:41984\n" \
  "v_accvgpr_read_b32 v151, a247\n" \
  "v_accvgpr_read_b32 v222, a216\n" \
  "v_mfma_f32_16x16x32_bf16 v[28:31], a[40:43], a[24:27], v[28:31]\n" \
  "buffer_load_dwordx4 v234, s[28:31], 0 offen sc1 lds\n" \
  "v_accvgpr_read_b32 v223, a220\n" \
  "v_mfma_f32_16x16x32_bf16 v[32:35], a[48:51], a[0:3], v[32:35]\n" \
  "ds_read_b64_tr_b16 a[112:113], v232 offset:32768\n" \
  "v_xor_b32 v152, 0x80000000, v129\n" \
  "v_xor_b32 v153, 0x80000000, v141\n" \
  "v_mfma_f32_16x16x32_bf16 v[32:35], a[52:55], a[4:7], v[32:35]\n" \
  "ds_read_b64_tr_b16 a[114:115], v232 offset:33792\n" \
  "v_mov_b32 v208, 0\n" \
  "v_mfma_f32_16x16x32_bf16 v[36:39], a[52:55], a[12:15], v[36:39]\n" \
  "buffer_load_dwordx4 v234, s[28:31], 0 offen offset:1024 sc1 lds\n" \
  "v_mov_b32 v210, 0\n" \
  "v_mfma_f32_16x16x32_bf16 v[36:39], a[48:51], a[8:11], v[36:39]\n" \
  "ds_read_b64_tr_b16 a[116:117], v232 offset:40960\n" \
  "v_mov_b32 v209, 0\n" \
  "v_mov_b32 v211, 0\n" \
  "v_mfma_f32_16x16x32_bf16 v[40:43], a[48:51], a[16:19], v[40:43]\n" \
  "ds_read_b64_tr_b16 a[118:119], v232 offset:41984\n" \
  "v_mfma_f32_16x16x32_bf16 v[40:43], a[52:55], a[20:23], v[40:43]\n" \
  "s_add_u32 m0, m0, 2048\n" \
  "ds_read_b64_tr_b16 a[120:121], v233 offset:32768\n" \
  "v_mfma_f32_16x16x32_bf16 v[44:47], a[52:55], a[28:31], v[44:47]\n" \
  "buffer_load_dwordx4 v234, s[28:31], s85 offen sc1 lds\n" \
  "v_mfma_f32_16x16x32_bf16 v[44:47], a[48:51], a[24:27], v[44:47]\n" \
  "ds_read_b64_tr_b16 a[122:123], v233 offset:33792\n" \
  "v_mfma_f32_16x16x32_bf16 v[48:51], a[56:59], a[0:3], v[48:51]\n" \
  "ds_read_b64_tr_b16 a[124:125], v233 offset:40960\n" \
  "v_mfma_f32_16x16x32_bf16 v[48:51], a[60:63], a[4:7], v[48:51]\n" \
  "buffer_load_dwordx4 v234, s[28:31], s85 offen offset:1024 sc1 lds\n" \
  "v_mfma_f32_16x16x32_bf16 v[52:55], a[60:63], a[12:15], v[52:55]\n" \
  "ds_read_b64_tr_b16 a[126:127], v233 offset:41984\n" \
  "v_mfma_f32_16x16x32_bf16 v[52:55], a[56:59], a[8:11], v[52:55]\n" \
  "v_mfma_f32_16x16x32_bf16 v[56:59], a[56:59], a[16:19], v[56:59]\n" \
  "s_add_u32 s28, s28, 2048\n" \
  "s_addc_u32 s29, s29, 0\n" \
  "v_mfma_f32_16x16x32_bf16 v[56:59], a[60:63], a[20:23], v[56:59]\n" \
  "v_mfma_f32_16x16x32_bf16 v[60:63], a[60:63], a[28:31], v[60:63]\n" \
  "s_add_u32 s32, s32, 229376\n" \
  "s_addc_u32 s33, s33, 0\n" \
  "v_mfma_f32_16x16x32_bf16 v[60:63], a[56:59], a[24:27], v[60:63]\n" \
  "s_waitcnt lgkmcnt(0)\n" \
  "s_waitcnt vmcnt(16)\n" \
  "s_barrier\n" \
  "v_mfma_f32_16x16x32_bf16 v[0:3], a[96:99], a[64:67], v[0:3]\n" \
  "s_add_u32 m0, s93, 98304\n" \
  "ds_read_b64_tr_b16 a[32:33], v226 offset:32768\n" \
  "ds_read_b128 v[172:175], v221 offset:0\n" \
  "ds_read_b128 v[188:191], v221 offset:1280\n" \
  "v_mfma_f32_16x16x32_bf16 v[0:3], a[100:103], a[68:71], v[0:3]\n" \
  "buffer_load_dwordx4 v235, s[32:35], s96 offen lds\n" \
  "ds_read_b128 v[176:179], v221 offset:16\n" \
  "v_mfma_f32_16x16x32_bf16 v[4:7], a[100:103], a[76:79], v[4:7]\n" \
  "ds_read_b64_tr_b16 a[34:35], v226 offset:33792\n" \
  "ds_read_b64_tr_b16 a[36:37], v226 offset:40960\n" \
  "ds_read_b128 v[192:195], v221 offset:1296\n" \
  "ds_read_b128 v[180:183], v221 offset:32\n" \
  "v_mfma_f32_16x16x32_bf16 v[4:7], a[96:99], a[72:75], v[4:7]\n" \
  "s_add_u32 m0, m0, s82\n" \
  "ds_read_b64_tr_b16 a[38:39], v226 offset:41984\n" \
  "ds_read_b128 v[196:199], v221 offset:1312\n" \
  "ds_read_b128 v[184:187], v221 offset:48\n" \
  "v_mfma_f32_16x16x32_bf16 v[8:11], a[96:99], a[80:83], v[8:11]\n" \
  "buffer_load_dwordx4 v235, s[32:35], s97 offen lds\n" \
  "ds_read_b128 v[200:203], v221 offset:1328\n" \
  "v_mfma_f32_16x16x32_bf16 v[8:11], a[100:103], a[84:87], v[8:11]\n" \
  "ds_read_b128 a[0:3], v224 offset:32768\n" \
  "ds_read_b128 a[4:7], v224 offset:33792\n" \
  "v_mul_f32 v216, v133, v80\n" \
  "v_mul_f32 v217, v133, v64\n" \
  "v_mfma_f32_16x16x32_bf16 v[12:15], a[100:103], a[92:95], v[12:15]\n" \
  "ds_read_b128 a[8:11], v224 offset:34816\n" \
  "ds_read_b128 a[12:15], v224 offset:35840\n" \
  "v_fma_f32 v64, v132, v64, -v216\n" \
  "v_fma_f32 v80, v132, v80, v217\n" \
  "v_mfma_f32_16x16x32_bf16 v[12:15], a[96:99], a[88:91], v[12:15]\n" \
  "s_add_u32 m0, m0, s82\n" \
  "ds_read_b128 a[16:19], v224 offset:36864\n" \
  "v_mul_f32 v218, v145, v112\n" \
  "v_mul_f32 v219, v145, v96\n" \
  "v_mfma_f32_16x16x32_bf16 v[16:19], a[104:107], a[64:67], v[16:19]\n" \
  "buffer_load_dwordx4 v235, s[32:35], s98 offen lds\n" \
  "v_fma_f32 v96, v144, v96, -v218\n" \
  "v_mfma_f32_16x16x32_bf16 v[16:19], a[108:111], a[68:71], v[16:19]\n" \
  "ds_read_b128 a[20:23], v224 offset:37888\n" \
  "ds_read_b128 a[24:27], v224 offset:38912\n" \
  "v_fma_f32 v112, v144, v112, v219\n" \
  "v_mul_f32 v216, v133, v81\n" \
  "v_mfma_f32_16x16x32_bf16 v[20:23], a[108:111], a[76:79], v[20:23]\n" \
  "s_add_u32 m0, m0, s82\n" \
  "ds_read_b128 a[28:31], v224 offset:39936\n" \
  "v_mul_f32 v217, v133, v65\n" \
  "v_fma_f32 v65, v132, v65, -v216\n" \
  "v_mfma_f32_16x16x32_bf16 v[20:23], a[104:107], a[72:75], v[20:23]\n" \
  "buffer_load_dwordx4 v235, s[32:35], s99 offen lds\n" \
  "v_fma_f32 v81, v132, v81, v217\n" \
  "v_mfma_f32_16x16x32_bf16 v[24:27], a[104:107], a[80:83], v[24:27]\n" \
  "ds_read_b64_tr_b16 a[40:41], v227 offset:32768\n" \
  "ds_read_b64_tr_b16 a[42:43], v227 offset:33792\n" \
  "v_mul_f32 v218, v145, v113\n" \
  "v_mul_f32 v219, v145, v97\n" \
  "v_mfma_f32_16x16x32_bf16 v[24:27], a[108:111], a[84:87], v[24:27]\n" \
  "ds_read_b64_tr_b16 a[44:45], v227 offset:40960\n" \
  "v_fma_f32 v97, v144, v97, -v218\n" \
  "v_mfma_f32_16x16x32_bf16 v[28:31], a[108:111], a[92:95], v[28:31]\n" \
  "s_add_u32 m0, s92, 98304\n" \
  "ds_read_b64_tr_b16 a[46:47], v227 offset:41984\n" \
  "v_fma_f32 v113, v144, v113, v219\n" \
  "v_mul_f32 v216, v133, v82\n" \
  "v_mfma_f32_16x16x32_bf16 v[28:31], a[104:107], a[88:91], v[28:31]\n" \
  "buffer_load_dwordx4 v234, s[28:31], 0 offen sc1 lds\n" \
  "v_mul_f32 v217, v133, v66\n" \
  "v_mfma_f32_16x16x32_bf16 v[32:35], a[112:115], a[64:67], v[32:35]\n" \
  "ds_read_b64_tr_b16 a[48:49], v228 offset:32768\n" \
  "v_fma_f32 v66, v132, v66, -v216\n" \
  "v_fma_f32 v82, v132, v82, v217\n" \
  "v_mfma_f32_16x16x32_bf16 v[32:35], a[116:119], a[68:71], v[32:35]\n" \
  "ds_read_b64_tr_b16 a[50:51], v228 offset:33792\n" \
  "v_mul_f32 v218, v145, v114\n" \
  "v_mfma_f32_16x16x32_bf16 v[36:39], a[116:119], a[76:79], v[36:39]\n" \
  "buffer_load_dwordx4 v234, s[28:31], 0 offen offset:1024 sc1 lds\n" \
  "v_mul_f32 v219, v145, v98\n" \
  "v_mfma_f32_16x16x32_bf16 v[36:39], a[112:115], a[72:75], v[36:39]\n" \
  "ds_read_b64_tr_b16 a[52:53], v228 offset:40960\n" \
  "v_fma_f32 v98, v144, v98, -v218\n" \
  "v_fma_f32 v114, v144, v114, v219\n" \
  "v_mfma_f32_16x16x32_bf16 v[40:43], a[112:115], a[80:83], v[40:43]\n" \
  "ds_read_b64_tr_b16 a[54:55], v228 offset:41984\n" \
  "v_cmp_eq_u32 vcc, 1, v222\n" \
  "s_waitcnt lgkmcnt(15)\n" \
  "v_fma_f32 v212, -v131, v173, v174\n" \
  "v_mfma_f32_16x16x32_bf16 v[40:43], a[116:119], a[84:87], v[40:43]\n" \
  "s_add_u32 m0, m0, 2048\n" \
  "ds_read_b64_tr_b16 a[56:57], v229 offset:32768\n" \
  "v_fma_f32 v213, v131, v172, v175\n" \
  "v_mfma_f32_16x16x32_bf16 v[44:47], a[116:119], a[92:95], v[44:47]\n" \
  "buffer_load_dwordx4 v234, s[28:31], s85 offen sc1 lds\n" \
  "v_fma_f32 v214, -v143, v189, v190\n" \
  "v_mfma_f32_16x16x32_bf16 v[44:47], a[112:115], a[88:91], v[44:47]\n" \
  "ds_read_b64_tr_b16 a[58:59], v229 offset:33792\n" \
  "v_fma_f32 v215, v143, v188, v191\n" \
  "v_cndmask_b32 v208, v208, v172, vcc\n" \
  "v_mfma_f32_16x16x32_bf16 v[48:51], a[120:123], a[64:67], v[48:51]\n" \
  "ds_read_b64_tr_b16 a[60:61], v229 offset:40960\n" \
  "v_cndmask_b32 v209, v209, v173, vcc\n" \
  "v_cndmask_b32 v210, v210, v188, vcc\n" \
  "v_mfma_f32_16x16x32_bf16 v[48:51], a[124:127], a[68:71], v[48:51]\n" \
  "buffer_load_dwordx4 v234, s[28:31], s85 offen offset:1024 sc1 lds\n" \
  "v_cndmask_b32 v211, v211, v189, vcc\n" \
  "v_mfma_f32_16x16x32_bf16 v[52:55], a[124:127], a[76:79], v[52:55]\n" \
  "ds_read_b64_tr_b16 a[62:63], v229 offset:41984\n" \
  "v_fma_f32 v204, v130, v172, v212\n" \
  "v_fma_f32 v205, v130, v173, v213\n" \
  "v_mfma_f32_16x16x32_bf16 v[52:55], a[120:123], a[72:75], v[52:55]\n" \
  "v_fma_f32 v206, v142, v188, v214\n" \
  "v_mfma_f32_16x16x32_bf16 v[56:59], a[120:123], a[80:83], v[56:59]\n" \
  "s_add_u32 s28, s28, 2048\n" \
  "s_addc_u32 s29, s29, 0\n" \
  "v_fma_f32 v207, v142, v189, v215\n" \
  "v_cmp_eq_u32 vcc, 2, v222\n" \
  "v_mfma_f32_16x16x32_bf16 v[56:59], a[124:127], a[84:87], v[56:59]\n" \
  "v_fma_f32 v212, -v131, v205, v176\n" \
  "v_mfma_f32_16x16x32_bf16 v[60:63], a[124:127], a[92:95], v[60:63]\n" \
  "s_add_u32 s32, s32, 229376\n" \
  "s_addc_u32 s33, s33, 0\n" \
  "v_fma_f32 v213, v131, v204, v177\n" \
  "v_fma_f32 v214, -v143, v207, v192\n" \
  "v_mfma_f32_16x16x32_bf16 v[60:63], a[120:123], a[88:91], v[60:63]\n" \
  "v_fma_f32 v215, v143, v206, v193\n" \
  "s_waitcnt lgkmcnt(0)\n" \
  "s_waitcnt vmcnt(16)\n" \
  "s_barrier\n" \
  "v_mfma_f32_16x16x32_bf16 v[0:3], a[32:35], a[0:3], v[0:3]\n" \
  "s_add_u32 m0, s93, 32768\n" \
  "ds_read_b64_tr_b16 a[96:97], v230 offset:0\n" \
  "v_cndmask_b32 v208, v208, v204, vcc\n" \
  "v_cndmask_b32 v209, v209, v205, vcc\n" \
  "v_mfma_f32_16x16x32_bf16 v[0:3], a[36:39], a[4:7], v[0:3]\n" \
  "buffer_load_dwordx4 v235, s[32:35], s96 offen lds\n" \
  "v_cndmask_b32 v210, v210, v206, vcc\n" \
  "v_mfma_f32_16x16x32_bf16 v[4:7], a[36:39], a[12:15], v[4:7]\n" \
  "ds_read_b64_tr_b16 a[98:99], v230 offset:1024\n" \
  "ds_read_b64_tr_b16 a[100:101], v230 offset:8192\n" \
  "v_cndmask_b32 v211, v211, v207, vcc\n" \
  "v_fma_f32 v204, v130, v204, v212\n" \
  "v_mfma_f32_16x16x32_bf16 v[4:7], a[32:35], a[8:11], v[4:7]\n" \
  "s_add_u32 m0, m0, s82\n" \
  "ds_read_b64_tr_b16 a[102:103], v230 offset:9216\n" \
  "v_fma_f32 v205, v130, v205, v213\n" \
  "v_mfma_f32_16x16x32_bf16 v[8:11], a[32:35], a[16:19], v[8:11]\n" \
  "buffer_load_dwordx4 v235, s[32:35], s97 offen lds\n" \
  "v_fma_f32 v206, v142, v206, v214\n" \
  "v_mfma_f32_16x16x32_bf16 v[8:11], a[36:39], a[20:23], v[8:11]\n" \
  "ds_read_b128 a[64:67], v225 offset:0\n" \
  "ds_read_b128 a[68:71], v225 offset:1024\n" \
  "v_fma_f32 v207, v142, v207, v215\n" \
  "v_cmp_eq_u32 vcc, 3, v222\n" \
  "v_mfma_f32_16x16x32_bf16 v[12:15], a[36:39], a[28:31], v[12:15]\n" \
  "ds_read_b128 a[72:75], v225 offset:2048\n" \
  "ds_read_b128 a[76:79], v225 offset:3072\n" \
  "v_fma_f32 v212, -v131, v205, v178\n" \
  "v_fma_f32 v213, v131, v204, v179\n" \
  "v_mfma_f32_16x16x32_bf16 v[12:15], a[32:35], a[24:27], v[12:15]\n" \
  "s_add_u32 m0, m0, s82\n" \
  "ds_read_b128 a[80:83], v225 offset:4096\n" \
  "v_fma_f32 v214, -v143, v207, v194\n" \
  "v_mfma_f32_16x16x32_bf16 v[16:19], a[40:43], a[0:3], v[16:19]\n" \
  "buffer_load_dwordx4 v235, s[32:35], s98 offen lds\n" \
  "v_fma_f32 v215, v143, v206, v195\n" \
  "v_mfma_f32_16x16x32_bf16 v[16:19], a[44:47], a[4:7], v[16:19]\n" \
  "ds_read_b128 a[84:87], v225 offset:5120\n" \
  "ds_read_b128 a[88:91], v225 offset:6144\n" \
  "v_cndmask_b32 v208, v208, v204, vcc\n" \
  "v_cndmask_b32 v209, v209, v205, vcc\n" \
  "v_mfma_f32_16x16x32_bf16 v[20:23], a[44:47], a[12:15], v[20:23]\n" \
  "s_add_u32 m0, m0, s82\n" \
  "ds_read_b128 a[92:95], v225 offset:7168\n" \
  "v_cndmask_b32 v210, v210, v206, vcc\n" \
  "v_cndmask_b32 v211, v211, v207, vcc\n" \
  "v_mfma_f32_16x16x32_bf16 v[20:23], a[40:43], a[8:11], v[20:23]\n" \
  "buffer_load_dwordx4 v235, s[32:35], s99 offen lds\n" \
  "v_fma_f32 v204, v130, v204, v212\n" \
  "v_mfma_f32_16x16x32_bf16 v[24:27], a[40:43], a[16:19], v[24:27]\n" \
  "ds_read_b64_tr_b16 a[104:105], v231 offset:0\n" \
  "ds_read_b64_tr_b16 a[106:107], v231 offset:1024\n" \
  "v_fma_f32 v205, v130, v205, v213\n" \
  "v_fma_f32 v206, v142, v206, v214\n" \
  "v_mfma_f32_16x16x32_bf16 v[24:27], a[44:47], a[20:23], v[24:27]\n" \
  "ds_read_b64_tr_b16 a[108:109], v231 offset:8192\n" \
  "v_fma_f32 v207, v142, v207, v215\n" \
  "v_mfma_f32_16x16x32_bf16 v[28:31], a[44:47], a[28:31], v[28:31]\n" \
  "s_add_u32 m0, s92, 32768\n" \
  "ds_read_b64_tr_b16 a[110:111], v231 offset:9216\n" \
  "v_cmp_eq_u32 vcc, 4, v222\n" \
  "v_fma_f32 v212, -v131, v205, v180\n" \
  "v_mfma_f32_16x16x32_bf16 v[28:31], a[40:43], a[24:27], v[28:31]\n" \
  "buffer_load_dwordx4 v234, s[28:31], 0 offen sc1 lds\n" \
  "v_fma_f32 v213, v131, v204, v181\n" \
  "v_mfma_f32_16x16x32_bf16 v[32:35], a[48:51], a[0:3], v[32:35]\n" \
  "ds_read_b64_tr_b16 a[112:113], v232 offset:0\n" \
  "v_fma_f32 v214, -v143, v207, v196\n" \
  "v_fma_f32 v215, v143, v206, v197\n" \
  "v_mfma_f32_16x16x32_bf16 v[32:35], a[52:55], a[4:7], v[32:35]\n" \
  "ds_read_b64_tr_b16 a[114:115], v232 offset:1024\n" \
  "v_cndmask_b32 v208, v208, v204, vcc\n" \
  "v_mfma_f32_16x16x32_bf16 v[36:39], a[52:55], a[12:15], v[36:39]\n" \
  "buffer_load_dwordx4 v234, s[28:31], 0 offen offset:1024 sc1 lds\n" \
  "v_cndmask_b32 v209, v209, v205, vcc\n" \
  "v_mfma_f32_16x16x32_bf16 v[36:39], a[48:51], a[8:11], v[36:39]\n" \
  "ds_read_b64_tr_b16 a[116:117], v232 offset:8192\n" \
  "v_cndmask_b32 v210, v210, v206, vcc\n" \
  "v_cndmask_b32 v211, v211, v207, vcc\n" \
  "v_mfma_f32_16x16x32_bf16 v[40:43], a[48:51], a[16:19], v[40:43]\n" \
  "ds_read_b64_tr_b16 a[118:119], v232 offset:9216\n" \
  "v_fma_f32 v204, v130, v204, v212\n" \
  "v_fma_f32 v205, v130, v205, v213\n" \
  "v_mfma_f32_16x16x32_bf16 v[40:43], a[52:55], a[20:23], v[40:43]\n" \
  "s_add_u32 m0, m0, 2048\n" \
  "ds_read_b64_tr_b16 a[120:121], v233 offset:0\n" \
  "v_fma_f32 v206, v142, v206, v214\n" \
  "v_mfma_f32_16x16x32_bf16 v[44:47], a[52:55], a[28:31], v[44:47]\n" \
  "buffer_load_dwordx4 v234, s[28:31], s85 offen sc1 lds\n" \
  "v_fma_f32 v207, v142, v207, v215\n" \
  "v_mfma_f32_16x16x32_bf16 v[44:47], a[48:51], a[24:27], v[44:47]\n" \
  "ds_read_b64_tr_b16 a[122:123], v233 offset:1024\n" \
  "v_cmp_eq_u32 vcc, 5, v222\n" \
  "v_fma_f32 v212, -v131, v205, v182\n" \
  "v_mfma_f32_16x16x32_bf16 v[48:51], a[56:59], a[0:3], v[48:51]\n" \
  "ds_read_b64_tr_b16 a[124:125], v233 offset:8192\n" \
  "v_fma_f32 v213, v131, v204, v183\n" \
  "v_fma_f32 v214, -v143, v207, v198\n" \
  "v_mfma_f32_16x16x32_bf16 v[48:51], a[60:63], a[4:7], v[48:51]\n" \
  "buffer_load_dwordx4 v234, s[28:31], s85 offen offset:1024 sc1 lds\n" \
  "v_fma_f32 v215, v143, v206, v199\n" \
  "v_mfma_f32_16x16x32_bf16 v[52:55], a[60:63], a[12:15], v[52:55]\n" \
  "ds_read_b64_tr_b16 a[126:127], v233 offset:9216\n" \
  "v_cndmask_b32 v208, v208, v204, vcc\n" \
  "v_cndmask_b32 v209, v209, v205, vcc\n" \
  "v_mfma_f32_16x16x32_bf16 v[52:55], a[56:59], a[8:11], v[52:55]\n" \
  "v_cndmask_b32 v210, v210, v206, vcc\n" \
  "v_mfma_f32_16x16x32_bf16 v[56:59], a[56:59], a[16:19], v[56:59]\n" \
  "s_add_u32 s28, s28, 2048\n" \
  "s_addc_u32 s29, s29, 0\n" \
  "v_cndmask_b32 v211, v211, v207, vcc\n" \
  "v_fma_f32 v204, v130, v204, v212\n" \
  "v_mfma_f32_16x16x32_bf16 v[56:59], a[60:63], a[20:23], v[56:59]\n" \
  "v_fma_f32 v205, v130, v205, v213\n" \
  "v_mfma_f32_16x16x32_bf16 v[60:63], a[60:63], a[28:31], v[60:63]\n" \
  "s_add_u32 s32, s32, 229376\n" \
  "s_addc_u32 s33, s33, 0\n" \
  "v_fma_f32 v206, v142, v206, v214\n" \
  "v_fma_f32 v207, v142, v207, v215\n" \
  "v_mfma_f32_16x16x32_bf16 v[60:63], a[56:59], a[24:27], v[60:63]\n" \
  "v_cmp_eq_u32 vcc, 6, v222\n" \
  "s_waitcnt lgkmcnt(0)\n" \
  "s_waitcnt vmcnt(16)\n" \
  "s_barrier\n" \
  "v_mfma_f32_16x16x32_bf16 v[0:3], a[96:99], a[64:67], v[0:3]\n" \
  "s_add_u32 m0, s93, 65536\n" \
  "ds_read_b64_tr_b16 a[32:33], v226 offset:0\n" \
  "v_fma_f32 v212, -v131, v205, v184\n" \
  "v_fma_f32 v213, v131, v204, v185\n" \
  "v_mfma_f32_16x16x32_bf16 v[0:3], a[100:103], a[68:71], v[0:3]\n" \
  "buffer_load_dwordx4 v235, s[32:35], s96 offen lds\n" \
  "v_fma_f32 v214, -v143, v207, v200\n" \
  "v_mfma_f32_16x16x32_bf16 v[4:7], a[100:103], a[76:79], v[4:7]\n" \
  "ds_read_b64_tr_b16 a[34:35], v226 offset:1024\n" \
  "ds_read_b64_tr_b16 a[36:37], v226 offset:8192\n" \
  "v_fma_f32 v215, v143, v206, v201\n" \
  "v_cndmask_b32 v208, v208, v204, vcc\n" \
  "v_mfma_f32_16x16x32_bf16 v[4:7], a[96:99], a[72:75], v[4:7]\n" \
  "s_add_u32 m0, m0, s82\n" \
  "ds_read_b64_tr_b16 a[38:39], v226 offset:9216\n" \
  "v_cndmask_b32 v209, v209, v205, vcc\n" \
  "v_mfma_f32_16x16x32_bf16 v[8:11], a[96:99], a[80:83], v[8:11]\n" \
  "buffer_load_dwordx4 v235, s[32:35], s97 offen lds\n" \
  "v_cndmask_b32 v210, v210, v206, vcc\n" \
  "v_mfma_f32_16x16x32_bf16 v[8:11], a[100:103], a[84:87], v[8:11]\n" \
  "ds_read_b128 a[0:3], v224 offset:0\n" \
  "ds_read_b128 a[4:7], v224 offset:1024\n" \
  "v_cndmask_b32 v211, v211, v207, vcc\n" \
  "v_fma_f32 v204, v130, v204, v212\n" \
  "v_mfma_f32_16x16x32_bf16 v[12:15], a[100:103], a[92:95], v[12:15]\n" \
  "ds_read_b128 a[8:11], v224 offset:2048\n" \
  "ds_read_b128 a[12:15], v224 offset:3072\n" \
  "v_fma_f32 v205, v130, v205, v213\n" \
  "v_fma_f32 v206, v142, v206, v214\n" \
  "v_mfma_f32_16x16x32_bf16 v[12:15], a[96:99], a[88:91], v[12:15]\n" \
  "s_add_u32 m0, m0, s82\n" \
  "ds_read_b128 a[16:19], v224 offset:4096\n" \
  "v_fma_f32 v207, v142, v207, v215\n" \
  "v_mfma_f32_16x16x32_bf16 v[16:19], a[104:107], a[64:67], v[16:19]\n" \
  "buffer_load_dwordx4 v235, s[32:35], s98 offen lds\n" \
  "v_cmp_eq_u32 vcc, 7, v222\n" \
  "v_mfma_f32_16x16x32_bf16 v[16:19], a[108:111], a[68:71], v[16:19]\n" \
  "ds_read_b128 a[20:23], v224 offset:5120\n" \
  "ds_read_b128 a[24:27], v224 offset:6144\n" \
  "v_fma_f32 v212, -v131, v205, v186\n" \
  "v_fma_f32 v213, v131, v204, v187\n" \
  "v_mfma_f32_16x16x32_bf16 v[20:23], a[108:111], a[76:79], v[20:23]\n" \
  "s_add_u32 m0, m0, s82\n" \
  "ds_read_b128 a[28:31], v224 offset:7168\n" \
  "v_fma_f32 v214, -v143, v207, v202\n" \
  "v_fma_f32 v215, v143, v206, v203\n" \
  "v_mfma_f32_16x16x32_bf16 v[20:23], a[104:107], a[72:75], v[20:23]\n" \
  "buffer_load_dwordx4 v235, s[32:35], s99 offen lds\n" \
  "v_cndmask_b32 v208, v208, v204, vcc\n" \
  "v_mfma_f32_16x16x32_bf16 v[24:27], a[104:107], a[80:83], v[24:27]\n" \
  "ds_read_b64_tr_b16 a[40:41], v227 offset:0\n" \
  "ds_read_b64_tr_b16 a[42:43], v227 offset:1024\n" \
  "v_cndmask_b32 v209, v209, v205, vcc\n" \
  "v_cndmask_b32 v210, v210, v206, vcc\n" \
  "v_mfma_f32_16x16x32_bf16 v[24:27], a[108:111], a[84:87], v[24:27]\n" \
  "ds_read_b64_tr_b16 a[44:45], v227 offset:8192\n" \
  "v_cndmask_b32 v211, v211, v207, vcc\n" \
  "v_mfma_f32_16x16x32_bf16 v[28:31], a[108:111], a[92:95], v[28:31]\n" \
  "s_add_u32 m0, s92, 65536\n" \
  "ds_read_b64_tr_b16 a[46:47], v227 offset:9216\n" \
  "v_fma_f32 v204, v130, v204, v212\n" \
  "v_fma_f32 v205, v130, v205, v213\n" \
  "v_mfma_f32_16x16x32_bf16 v[28:31], a[104:107], a[88:91], v[28:31]\n" \
  "buffer_load_dwordx4 v234, s[28:31], 0 offen sc1 lds\n" \
  "v_fma_f32 v206, v142, v206, v214\n" \
  "v_mfma_f32_16x16x32_bf16 v[32:35], a[112:115], a[64:67], v[32:35]\n" \
  "ds_read_b64_tr_b16 a[48:49], v228 offset:0\n" \
  "v_fma_f32 v207, v142, v207, v215\n" \
  "v_accvgpr_read_b32 v220, a210\n" \
  "v_mfma_f32_16x16x32_bf16 v[32:35], a[116:119], a[68:71], v[32:35]\n" \
  "ds_read_b64_tr_b16 a[50:51], v228 offset:1024\n" \
  "v_mov_b32 v156, v204\n" \
  "v_mfma_f32_16x16x32_bf16 v[36:39], a[116:119], a[76:79], v[36:39]\n" \
  "buffer_load_dwordx4 v234, s[28:31], 0 offen offset:1024 sc1 lds\n" \
  "v_mov_b32 v157, v254\n" \
  "v_mfma_f32_16x16x32_bf16 v[36:39], a[112:115], a[72:75], v[36:39]\n" \
  "ds_read_b64_tr_b16 a[52:53], v228 offset:8192\n" \
  "v_mov_b32 v158, v205\n" \
  "v_mov_b32 v159, v254\n" \
  "v_mfma_f32_16x16x32_bf16 v[40:43], a[112:115], a[80:83], v[40:43]\n" \
  "ds_read_b64_tr_b16 a[54:55], v228 offset:9216\n" \
  "v_mov_b32 v164, v206\n" \
  "v_mov_b32 v165, v254\n" \
  "v_mfma_f32_16x16x32_bf16 v[40:43], a[116:119], a[84:87], v[40:43]\n" \
  "s_add_u32 m0, m0, 2048\n" \
  "ds_read_b64_tr_b16 a[56:57], v229 offset:0\n" \
  "v_mov_b32 v166, v207\n" \
  "v_mfma_f32_16x16x32_bf16 v[44:47], a[116:119], a[92:95], v[44:47]\n" \
  "buffer_load_dwordx4 v234, s[28:31], s85 offen sc1 lds\n" \
  "v_mov_b32 v167, v254\n" \
  "v_mfma_f32_16x16x32_bf16 v[44:47], a[112:115], a[88:91], v[44:47]\n" \
  "ds_read_b64_tr_b16 a[58:59], v229 offset:1024\n" \
  "buffer_store_dwordx4 v[156:159], v220, s[52:55], 0 offen sc1\n" \
  "v_mfma_f32_16x16x32_bf16 v[48:51], a[120:123], a[64:67], v[48:51]\n" \
  "ds_read_b64_tr_b16 a[60:61], v229 offset:8192\n" \
  "v_mfma_f32_16x16x32_bf16 v[48:51], a[124:127], a[68:71], v[48:51]\n" \
  "buffer_load_dwordx4 v234, s[28:31], s85 offen offset:1024 sc1 lds\n" \
  "v_mfma_f32_16x16x32_bf16 v[52:55], a[124:127], a[76:79], v[52:55]\n" \
  "ds_read_b64_tr_b16 a[62:63], v229 offset:9216\n" \
  "buffer_store_dwordx4 v[164:167], v220, s[52:55], s82 offen sc1\n" \
  "v_mfma_f32_16x16x32_bf16 v[52:55], a[120:123], a[72:75], v[52:55]\n" \
  "v_mul_f32 v216, v133, v83\n" \
  "v_mul_f32 v217, v133, v67\n" \
  "v_fma_f32 v67, v132, v67, -v216\n" \
  "v_mfma_f32_16x16x32_bf16 v[56:59], a[120:123], a[80:83], v[56:59]\n" \
  "s_add_u32 s28, s28, 2048\n" \
  "s_addc_u32 s29, s29, 0\n" \
  "v_fma_f32 v83, v132, v83, v217\n" \
  "v_mfma_f32_16x16x32_bf16 v[56:59], a[124:127], a[84:87], v[56:59]\n" \
  "v_mul_f32 v218, v145, v115\n" \
  "v_mul_f32 v219, v145, v99\n" \
  "v_mfma_f32_16x16x32_bf16 v[60:63], a[124:127], a[92:95], v[60:63]\n" \
  "s_add_u32 s32, s32, 229376\n" \
  "s_addc_u32 s33, s33, 0\n" \
  "v_fma_f32 v99, v144, v99, -v218\n" \
  "v_mfma_f32_16x16x32_bf16 v[60:63], a[120:123], a[88:91], v[60:63]\n" \
  "v_fma_f32 v115, v144, v115, v219\n" \
  "v_mul_f32 v216, v133, v84\n" \
  "s_waitcnt lgkmcnt(0)\n" \
  "s_waitcnt vmcnt(18)\n" \
  "s_barrier\n" \
  "v_mfma_f32_16x16x32_bf16 v[0:3], a[32:35], a[0:3], v[0:3]\n" \
  "s_add_u32 m0, s93, 0\n" \
  "ds_read_b64_tr_b16 a[96:97], v230 offset:32768\n" \
  "v_mul_f32 v217, v133, v68\n" \
  "v_mfma_f32_16x16x32_bf16 v[0:3], a[36:39], a[4:7], v[0:3]\n" \
  "buffer_load_dwordx4 v235, s[32:35], s96 offen lds\n" \
  "v_fma_f32 v68, v132, v68, -v216\n" \
  "v_mfma_f32_16x16x32_bf16 v[4:7], a[36:39], a[12:15], v[4:7]\n" \
  "ds_read_b64_tr_b16 a[98:99], v230 offset:33792\n" \
  "ds_read_b64_tr_b16 a[100:101], v230 offset:40960\n" \
  "v_fma_f32 v84, v132, v84, v217\n" \
  "v_mul_f32 v218, v145, v116\n" \
  "v_mfma_f32_16x16x32_bf16 v[4:7], a[32:35], a[8:11], v[4:7]\n" \
  "s_add_u32 m0, m0, s82\n" \
  "ds_read_b64_tr_b16 a[102:103], v230 offset:41984\n" \
  "v_mul_f32 v219, v145, v100\n" \
  "v_fma_f32 v100, v144, v100, -v218\n" \
  "v_mfma_f32_16x16x32_bf16 v[8:11], a[32:35], a[16:19], v[8:11]\n" \
  "buffer_load_dwordx4 v235, s[32:35], s97 offen lds\n" \
  "v_fma_f32 v116, v144, v116, v219\n" \
  "v_mfma_f32_16x16x32_bf16 v[8:11], a[36:39], a[20:23], v[8:11]\n" \
  "ds_read_b128 a[64:67], v225 offset:32768\n" \
  "ds_read_b128 a[68:71], v225 offset:33792\n" \
  "v_mul_f32 v216, v133, v85\n" \
  "v_mul_f32 v217, v133, v69\n" \
  "v_mfma_f32_16x16x32_bf16 v[12:15], a[36:39], a[28:31], v[12:15]\n" \
  "ds_read_b128 a[72:75], v225 offset:34816\n" \
  "ds_read_b128 a[76:79], v225 offset:35840\n" \
  "v_fma_f32 v69, v132, v69, -v216\n" \
  "v_mfma_f32_16x16x32_bf16 v[12:15], a[32:35], a[24:27], v[12:15]\n" \
  "s_add_u32 m0, m0, s82\n" \
  "ds_read_b128 a[80:83], v225 offset:36864\n" \
  "v_fma_f32 v85, v132, v85, v217\n" \
  "v_mul_f32 v218, v145, v117\n" \
  "v_mfma_f32_16x16x32_bf16 v[16:19], a[40:43], a[0:3], v[16:19]\n" \
  "buffer_load_dwordx4 v235, s[32:35], s98 offen lds\n" \
  "v_mul_f32 v219, v145, v101\n" \
  "v_mfma_f32_16x16x32_bf16 v[16:19], a[44:47], a[4:7], v[16:19]\n" \
  "ds_read_b128 a[84:87], v225 offset:37888\n" \
  "ds_read_b128 a[88:91], v225 offset:38912\n" \
  "v_fma_f32 v101, v144, v101, -v218\n" \
  "v_fma_f32 v117, v144, v117, v219\n" \
  "v_mfma_f32_16x16x32_bf16 v[20:23], a[44:47], a[12:15], v[20:23]\n" \
  "s_add_u32 m0, m0, s82\n" \
  "ds_read_b128 a[92:95], v225 offset:39936\n" \
  "v_mul_f32 v216, v133, v86\n" \
  "v_mfma_f32_16x16x32_bf16 v[20:23], a[40:43], a[8:11], v[20:23]\n" \
  "buffer_load_dwordx4 v235, s[32:35], s99 offen lds\n" \
  "v_mul_f32 v217, v133, v70\n" \
  "v_mfma_f32_16x16x32_bf16 v[24:27], a[40:43], a[16:19], v[24:27]\n" \
  "ds_read_b64_tr_b16 a[104:105], v231 offset:32768\n" \
  "ds_read_b64_tr_b16 a[106:107], v231 offset:33792\n" \
  "v_fma_f32 v70, v132, v70, -v216\n" \
  "v_fma_f32 v86, v132, v86, v217\n" \
  "v_mfma_f32_16x16x32_bf16 v[24:27], a[44:47], a[20:23], v[24:27]\n" \
  "ds_read_b64_tr_b16 a[108:109], v231 offset:40960\n" \
  "v_mul_f32 v218, v145, v118\n" \
  "v_mul_f32 v219, v145, v102\n" \
  "v_mfma_f32_16x16x32_bf16 v[28:31], a[44:47], a[28:31], v[28:31]\n" \
  "s_add_u32 m0, s92, 0\n" \
  "ds_read_b64_tr_b16 a[110:111], v231 offset:41984\n" \
  "v_fma_f32 v102, v144, v102, -v218\n" \
  "v_mfma_f32_16x16x32_bf16 v[28:31], a[40:43], a[24:27], v[28:31]\n" \
  "buffer_load_dwordx4 v234, s[28:31], 0 offen sc1 lds\n" \
  "v_fma_f32 v118, v144, v118, v219\n" \
  "v_mfma_f32_16x16x32_bf16 v[32:35], a[48:51], a[0:3], v[32:35]\n" \
  "ds_read_b64_tr_b16 a[112:113], v232 offset:32768\n" \
  "v_mul_f32 v216, v133, v87\n" \
  "v_mul_f32 v217, v133, v71\n" \
  "v_mfma_f32_16x16x32_bf16 v[32:35], a[52:55], a[4:7], v[32:35]\n" \
  "ds_read_b64_tr_b16 a[114:115], v232 offset:33792\n" \
  "v_fma_f32 v71, v132, v71, -v216\n" \
  "v_fma_f32 v87, v132, v87, v217\n" \
  "v_mfma_f32_16x16x32_bf16 v[36:39], a[52:55], a[12:15], v[36:39]\n" \
  "buffer_load_dwordx4 v234, s[28:31], 0 offen offset:1024 sc1 lds\n" \
  "v_mul_f32 v218, v145, v119\n" \
  "v_mfma_f32_16x16x32_bf16 v[36:39], a[48:51], a[8:11], v[36:39]\n" \
  "ds_read_b64_tr_b16 a[116:117], v232 offset:40960\n" \
  "v_mul_f32 v219, v145, v103\n" \
  "v_fma_f32 v103, v144, v103, -v218\n" \
  "v_mfma_f32_16x16x32_bf16 v[40:43], a[48:51], a[16:19], v[40:43]\n" \
  "ds_read_b64_tr_b16 a[118:119], v232 offset:41984\n" \
  "v_fma_f32 v119, v144, v119, v219\n" \
  "v_mfma_f32_16x16x32_bf16 v[40:43], a[52:55], a[20:23], v[40:43]\n" \
  "s_add_u32 m0, m0, 2048\n" \
  "ds_read_b64_tr_b16 a[120:121], v233 offset:32768\n" \
  "v_mul_f32 v216, v133, v88\n" \
  "v_mul_f32 v217, v133, v72\n" \
  "v_mfma_f32_16x16x32_bf16 v[44:47], a[52:55], a[28:31], v[44:47]\n" \
  "buffer_load_dwordx4 v234, s[28:31], s85 offen sc1 lds\n" \
  "v_fma_f32 v72, v132, v72, -v216\n" \
  "v_mfma_f32_16x16x32_bf16 v[44:47], a[48:51], a[24:27], v[44:47]\n" \
  "ds_read_b64_tr_b16 a[122:123], v233 offset:33792\n" \
  "v_fma_f32 v88, v132, v88, v217\n" \
  "v_mul_f32 v218, v145, v120\n" \
  "v_mfma_f32_16x16x32_bf16 v[48:51], a[56:59], a[0:3], v[48:51]\n" \
  "ds_read_b64_tr_b16 a[124:125], v233 offset:40960\n" \
  "v_mul_f32 v219, v145, v104\n" \
  "v_mfma_f32_16x16x32_bf16 v[48:51], a[60:63], a[4:7], v[48:51]\n" \
  "buffer_load_dwordx4 v234, s[28:31], s85 offen offset:1024 sc1 lds\n" \
  "v_fma_f32 v104, v144, v104, -v218\n" \
  "v_mfma_f32_16x16x32_bf16 v[52:55], a[60:63], a[12:15], v[52:55]\n" \
  "ds_read_b64_tr_b16 a[126:127], v233 offset:41984\n" \
  "v_fma_f32 v120, v144, v120, v219\n" \
  "v_mul_f32 v216, v133, v89\n" \
  "v_mfma_f32_16x16x32_bf16 v[52:55], a[56:59], a[8:11], v[52:55]\n" \
  "v_mul_f32 v217, v133, v73\n" \
  "v_fma_f32 v73, v132, v73, -v216\n" \
  "v_mfma_f32_16x16x32_bf16 v[56:59], a[56:59], a[16:19], v[56:59]\n" \
  "s_add_u32 s28, s28, 2048\n" \
  "s_addc_u32 s29, s29, 0\n" \
  "v_fma_f32 v89, v132, v89, v217\n" \
  "v_mfma_f32_16x16x32_bf16 v[56:59], a[60:63], a[20:23], v[56:59]\n" \
  "v_mul_f32 v218, v145, v121\n" \
  "v_mul_f32 v219, v145, v105\n" \
  "v_mfma_f32_16x16x32_bf16 v[60:63], a[60:63], a[28:31], v[60:63]\n" \
  "s_add_u32 s32, s32, 229376\n" \
  "s_addc_u32 s33, s33, 0\n" \
  "v_fma_f32 v105, v144, v105, -v218\n" \
  "v_mfma_f32_16x16x32_bf16 v[60:63], a[56:59], a[24:27], v[60:63]\n" \
  "v_fma_f32 v121, v144, v121, v219\n" \
  "v_mul_f32 v216, v133, v90\n" \
  "s_waitcnt lgkmcnt(0)\n" \
  "s_waitcnt vmcnt(18)\n" \
  "s_barrier\n" \
  "v_mfma_f32_16x16x32_bf16 v[0:3], a[96:99], a[64:67], v[0:3]\n" \
  "s_add_u32 m0, s93, 98304\n" \
  "ds_read_b64_tr_b16 a[32:33], v226 offset:32768\n" \
  "v_mul_f32 v217, v133, v74\n" \
  "v_mfma_f32_16x16x32_bf16 v[0:3], a[100:103], a[68:71], v[0:3]\n" \
  "buffer_load_dwordx4 v235, s[32:35], s96 offen lds\n" \
  "v_fma_f32 v74, v132, v74, -v216\n" \
  "v_mfma_f32_16x16x32_bf16 v[4:7], a[100:103], a[76:79], v[4:7]\n" \
  "ds_read_b64_tr_b16 a[34:35], v226 offset:33792\n" \
  "ds_read_b64_tr_b16 a[36:37], v226 offset:40960\n" \
  "v_fma_f32 v90, v132, v90, v217\n" \
  "v_mul_f32 v218, v145, v122\n" \
  "v_mfma_f32_16x16x32_bf16 v[4:7], a[96:99], a[72:75], v[4:7]\n" \
  "s_add_u32 m0, m0, s82\n" \
  "ds_read_b64_tr_b16 a[38:39], v226 offset:41984\n" \
  "v_mul_f32 v219, v145, v106\n" \
  "v_fma_f32 v106, v144, v106, -v218\n" \
  "v_mfma_f32_16x16x32_bf16 v[8:11], a[96:99], a[80:83], v[8:11]\n" \
  "buffer_load_dwordx4 v235, s[32:35], s97 offen lds\n" \
  "v_fma_f32 v122, v144, v122, v219\n" \
  "v_mfma_f32_16x16x32_bf16 v[8:11], a[100:103], a[84:87], v[8:11]\n" \
  "ds_read_b128 a[0:3], v224 offset:32768\n" \
  "ds_read_b128 a[4:7], v224 offset:33792\n" \
  "v_mul_f32 v216, v133, v91\n" \
  "v_mul_f32 v217, v133, v75\n" \
  "v_mfma_f32_16x16x32_bf16 v[12:15], a[100:103], a[92:95], v[12:15]\n" \
  "ds_read_b128 a[8:11], v224 offset:34816\n" \
  "ds_read_b128 a[12:15], v224 offset:35840\n" \
  "v_fma_f32 v75, v132, v75, -v216\n" \
  "v_mfma_f32_16x16x32_bf16 v[12:15], a[96:99], a[88:91], v[12:15]\n" \
  "s_add_u32 m0, m0, s82\n" \
  "ds_read_b128 a[16:19], v224 offset:36864\n" \
  "v_fma_f32 v91, v132, v91, v217\n" \
  "v_mul_f32 v218, v145, v123\n" \
  "v_mfma_f32_16x16x32_bf16 v[16:19], a[104:107], a[64:67], v[16:19]\n" \
  "buffer_load_dwordx4 v235, s[32:35], s98 offen lds\n" \
  "v_mul_f32 v219, v145, v107\n" \
  "v_mfma_f32_16x16x32_bf16 v[16:19], a[108:111], a[68:71], v[16:19]\n" \
  "ds_read_b128 a[20:23], v224 offset:37888\n" \
  "ds_read_b128 a[24:27], v224 offset:38912\n" \
  "v_fma_f32 v107, v144, v107, -v218\n" \
  "v_fma_f32 v123, v144, v123, v219\n" \
  "v_mfma_f32_16x16x32_bf16 v[20:23], a[108:111], a[76:79], v[20:23]\n" \
  "s_add_u32 m0, m0, s82\n" \
  "ds_read_b128 a[28:31], v224 offset:39936\n" \
  "v_accvgpr_read_b32 v221, a209\n" \
  "v_mfma_f32_16x16x32_bf16 v[20:23], a[104:107], a[72:75], v[20:23]\n" \
  "buffer_load_dwordx4 v235, s[32:35], s99 offen lds\n" \
  "v_mfma_f32_16x16x32_bf16 v[24:27], a[104:107], a[80:83], v[24:27]\n" \
  "ds_read_b64_tr_b16 a[40:41], v227 offset:32768\n" \
  "ds_read_b64_tr_b16 a[42:43], v227 offset:33792\n" \
  "v_mfma_f32_16x16x32_bf16 v[24:27], a[108:111], a[84:87], v[24:27]\n" \
  "ds_read_b64_tr_b16 a[44:45], v227 offset:40960\n" \
  "buffer_load_dwordx4 v[156:159], v221, s[52:55], 0 offen sc1\n" \
  "v_mfma_f32_16x16x32_bf16 v[28:31], a[108:111], a[92:95], v[28:31]\n" \
  "s_add_u32 m0, s92, 98304\n" \
  "ds_read_b64_tr_b16 a[46:47], v227 offset:41984\n" \
  "v_mfma_f32_16x16x32_bf16 v[28:31], a[104:107], a[88:91], v[28:31]\n" \
  "buffer_load_dwordx4 v234, s[28:31], 0 offen sc1 lds\n" \
  "v_mfma_f32_16x16x32_bf16 v[32:35], a[112:115], a[64:67], v[32:35]\n" \
  "ds_read_b64_tr_b16 a[48:49], v228 offset:32768\n" \
  "buffer_load_dwordx4 v[160:163], v221, s[52:55], 0 offen offset:256 sc1\n" \
  "v_mfma_f32_16x16x32_bf16 v[32:35], a[116:119], a[68:71], v[32:35]\n" \
  "ds_read_b64_tr_b16 a[50:51], v228 offset:33792\n" \
  "buffer_load_dwordx4 v[164:167], v221, s[52:55], s82 offen sc1\n" \
  "v_mfma_f32_16x16x32_bf16 v[36:39], a[116:119], a[76:79], v[36:39]\n" \
  "buffer_load_dwordx4 v234, s[28:31], 0 offen offset:1024 sc1 lds\n" \
  "v_mfma_f32_16x16x32_bf16 v[36:39], a[112:115], a[72:75], v[36:39]\n" \
  "ds_read_b64_tr_b16 a[52:53], v228 offset:40960\n" \
  "buffer_load_dwordx4 v[168:171], v221, s[52:55], s82 offen offset:256 sc1\n" \
  "v_mfma_f32_16x16x32_bf16 v[40:43], a[112:115], a[80:83], v[40:43]\n" \
  "ds_read_b64_tr_b16 a[54:55], v228 offset:41984\n" \
  "v_mul_f32 v216, v133, v92\n" \
  "v_mul_f32 v217, v133, v76\n" \
  "v_mfma_f32_16x16x32_bf16 v[40:43], a[116:119], a[84:87], v[40:43]\n" \
  "s_add_u32 m0, m0, 2048\n" \
  "ds_read_b64_tr_b16 a[56:57], v229 offset:32768\n" \
  "v_fma_f32 v76, v132, v76, -v216\n" \
  "v_fma_f32 v92, v132, v92, v217\n" \
  "v_mfma_f32_16x16x32_bf16 v[44:47], a[116:119], a[92:95], v[44:47]\n" \
  "buffer_load_dwordx4 v234, s[28:31], s85 offen sc1 lds\n" \
  "v_mul_f32 v218, v145, v124\n" \
  "v_mfma_f32_16x16x32_bf16 v[44:47], a[112:115], a[88:91], v[44:47]\n" \
  "ds_read_b64_tr_b16 a[58:59], v229 offset:33792\n" \
  "v_mul_f32 v219, v145, v108\n" \
  "v_fma_f32 v108, v144, v108, -v218\n" \
  "v_mfma_f32_16x16x32_bf16 v[48:51], a[120:123], a[64:67], v[48:51]\n" \
  "ds_read_b64_tr_b16 a[60:61], v229 offset:40960\n" \
  "v_fma_f32 v124, v144, v124, v219\n" \
  "v_mfma_f32_16x16x32_bf16 v[48:51], a[124:127], a[68:71], v[48:51]\n" \
  "buffer_load_dwordx4 v234, s[28:31], s85 offen offset:1024 sc1 lds\n" \
  "v_mul_f32 v216, v133, v93\n" \
  "v_mfma_f32_16x16x32_bf16 v[52:55], a[124:127], a[76:79], v[52:55]\n" \
  "ds_read_b64_tr_b16 a[62:63], v229 offset:41984\n" \
  "v_mul_f32 v217, v133, v77\n" \
  "v_fma_f32 v77, v132, v77, -v216\n" \
  "v_mfma_f32_16x16x32_bf16 v[52:55], a[120:123], a[72:75], v[52:55]\n" \
  "v_fma_f32 v93, v132, v93, v217\n" \
  "v_mul_f32 v218, v145, v125\n" \
  "v_mfma_f32_16x16x32_bf16 v[56:59], a[120:123], a[80:83], v[56:59]\n" \
  "s_add_u32 s28, s28, 2048\n" \
  "s_addc_u32 s29, s29, 0\n" \
  "v_mul_f32 v219, v145, v109\n" \
  "v_mfma_f32_16x16x32_bf16 v[56:59], a[124:127], a[84:87], v[56:59]\n" \
  "v_fma_f32 v109, v144, v109, -v218\n" \
  "v_fma_f32 v125, v144, v125, v219\n" \
  "v_mfma_f32_16x16x32_bf16 v[60:63], a[124:127], a[92:95], v[60:63]\n" \
  "s_add_u32 s32, s32, 229376\n" \
  "s_addc_u32 s33, s33, 0\n" \
  "v_mul_f32 v216, v133, v94\n" \
  "v_mfma_f32_16x16x32_bf16 v[60:63], a[120:123], a[88:91], v[60:63]\n" \
  "v_mul_f32 v217, v133, v78\n" \
  "v_fma_f32 v78, v132, v78, -v216\n" \
  "s_waitcnt lgkmcnt(0)\n" \
  "s_waitcnt vmcnt(21)\n" \
  "s_barrier\n" \
  "v_mfma_f32_16x16x32_bf16 v[0:3], a[32:35], a[0:3], v[0:3]\n" \
  "s_add_u32 m0, s93, 32768\n" \
  "ds_read_b64_tr_b16 a[96:97], v230 offset:0\n" \
  "v_fma_f32 v94, v132, v94, v217\n" \
  "v_mfma_f32_16x16x32_bf16 v[0:3], a[36:39], a[4:7], v[0:3]\n" \
  "buffer_load_dwordx4 v235, s[32:35], s96 offen lds\n" \
  "v_mul_f32 v218, v145, v126\n" \
  "v_mfma_f32_16x16x32_bf16 v[4:7], a[36:39], a[12:15], v[4:7]\n" \
  "ds_read_b64_tr_b16 a[98:99], v230 offset:1024\n" \
  "ds_read_b64_tr_b16 a[100:101], v230 offset:8192\n" \
  "v_mul_f32 v219, v145, v110\n" \
  "v_fma_f32 v110, v144, v110, -v218\n" \
  "v_mfma_f32_16x16x32_bf16 v[4:7], a[32:35], a[8:11], v[4:7]\n" \
  "s_add_u32 m0, m0, s82\n" \
  "ds_read_b64_tr_b16 a[102:103], v230 offset:9216\n" \
  "v_fma_f32 v126, v144, v126, v219\n" \
  "v_mul_f32 v216, v133, v95\n" \
  "v_mfma_f32_16x16x32_bf16 v[8:11], a[32:35], a[16:19], v[8:11]\n" \
  "buffer_load_dwordx4 v235, s[32:35], s97 offen lds\n" \
  "v_mul_f32 v217, v133, v79\n" \
  "v_mfma_f32_16x16x32_bf16 v[8:11], a[36:39], a[20:23], v[8:11]\n" \
  "ds_read_b128 a[64:67], v225 offset:0\n" \
  "ds_read_b128 a[68:71], v225 offset:1024\n" \
  "v_fma_f32 v79, v132, v79, -v216\n" \
  "v_fma_f32 v95, v132, v95, v217\n" \
  "v_mfma_f32_16x16x32_bf16 v[12:15], a[36:39], a[28:31], v[12:15]\n" \
  "ds_read_b128 a[72:75], v225 offset:2048\n" \
  "ds_read_b128 a[76:79], v225 offset:3072\n" \
  "v_mul_f32 v218, v145, v127\n" \
  "v_mfma_f32_16x16x32_bf16 v[12:15], a[32:35], a[24:27], v[12:15]\n" \
  "s_add_u32 m0, m0, s82\n" \
  "ds_read_b128 a[80:83], v225 offset:4096\n" \
  "v_mul_f32 v219, v145, v111\n" \
  "v_fma_f32 v111, v144, v111, -v218\n" \
  "v_mfma_f32_16x16x32_bf16 v[16:19], a[40:43], a[0:3], v[16:19]\n" \
  "buffer_load_dwordx4 v235, s[32:35], s98 offen lds\n" \
  "v_fma_f32 v127, v144, v127, v219\n" \
  "v_mfma_f32_16x16x32_bf16 v[16:19], a[44:47], a[4:7], v[16:19]\n" \
  "ds_read_b128 a[84:87], v225 offset:5120\n" \
  "ds_read_b128 a[88:91], v225 offset:6144\n" \
  "v_mfma_f32_16x16x32_bf16 v[20:23], a[44:47], a[12:15], v[20:23]\n" \
  "s_add_u32 m0, m0, s82\n" \
  "ds_read_b128 a[92:95], v225 offset:7168\n" \
  "v_mfma_f32_16x16x32_bf16 v[20:23], a[40:43], a[8:11], v[20:23]\n" \
  "buffer_load_dwordx4 v235, s[32:35], s99 offen lds\n" \
  "v_mfma_f32_16x16x32_bf16 v[24:27], a[40:43], a[16:19], v[24:27]\n" \
  "ds_read_b64_tr_b16 a[104:105], v231 offset:0\n" \
  "ds_read_b64_tr_b16 a[106:107], v231 offset:1024\n" \
  "v_mfma_f32_16x16x32_bf16 v[24:27], a[44:47], a[20:23], v[24:27]\n" \
  "ds_read_b64_tr_b16 a[108:109], v231 offset:8192\n" \
  "v_mfma_f32_16x16x32_bf16 v[28:31], a[44:47], a[28:31], v[28:31]\n" \
  "s_add_u32 m0, s92, 32768\n" \
  "ds_read_b64_tr_b16 a[110:111], v231 offset:9216\n" \
  "v_mfma_f32_16x16x32_bf16 v[28:31], a[40:43], a[24:27], v[28:31]\n" \
  "buffer_load_dwordx4 v234, s[28:31], 0 offen sc1 lds\n" \
  "v_mfma_f32_16x16x32_bf16 v[32:35], a[48:51], a[0:3], v[32:35]\n" \
  "ds_read_b64_tr_b16 a[112:113], v232 offset:0\n" \
  "v_mfma_f32_16x16x32_bf16 v[32:35], a[52:55], a[4:7], v[32:35]\n" \
  "ds_read_b64_tr_b16 a[114:115], v232 offset:1024\n" \
  "v_mfma_f32_16x16x32_bf16 v[36:39], a[52:55], a[12:15], v[36:39]\n" \
  "buffer_load_dwordx4 v234, s[28:31], 0 offen offset:1024 sc1 lds\n" \
  "v_mfma_f32_16x16x32_bf16 v[36:39], a[48:51], a[8:11], v[36:39]\n" \
  "ds_read_b64_tr_b16 a[116:117], v232 offset:8192\n" \
  "v_mfma_f32_16x16x32_bf16 v[40:43], a[48:51], a[16:19], v[40:43]\n" \
  "ds_read_b64_tr_b16 a[118:119], v232 offset:9216\n" \
  "v_mfma_f32_16x16x32_bf16 v[40:43], a[52:55], a[20:23], v[40:43]\n" \
  "s_add_u32 m0, m0, 2048\n" \
  "ds_read_b64_tr_b16 a[120:121], v233 offset:0\n" \
  "v_mfma_f32_16x16x32_bf16 v[44:47], a[52:55], a[28:31], v[44:47]\n" \
  "buffer_load_dwordx4 v234, s[28:31], s85 offen sc1 lds\n" \
  "v_mfma_f32_16x16x32_bf16 v[44:47], a[48:51], a[24:27], v[44:47]\n" \
  "ds_read_b64_tr_b16 a[122:123], v233 offset:1024\n" \
  "v_mfma_f32_16x16x32_bf16 v[48:51], a[56:59], a[0:3], v[48:51]\n" \
  "ds_read_b64_tr_b16 a[124:125], v233 offset:8192\n" \
  "v_mfma_f32_16x16x32_bf16 v[48:51], a[60:63], a[4:7], v[48:51]\n" \
  "buffer_load_dwordx4 v234, s[28:31], s85 offen offset:1024 sc1 lds\n" \
  "v_mfma_f32_16x16x32_bf16 v[52:55], a[60:63], a[12:15], v[52:55]\n" \
  "ds_read_b64_tr_b16 a[126:127], v233 offset:9216\n" \
  "v_mfma_f32_16x16x32_bf16 v[52:55], a[56:59], a[8:11], v[52:55]\n" \
  "v_mfma_f32_16x16x32_bf16 v[56:59], a[56:59], a[16:19], v[56:59]\n" \
  "s_add_u32 s28, s28, 2048\n" \
  "s_addc_u32 s29, s29, 0\n" \
  "v_mfma_f32_16x16x32_bf16 v[56:59], a[60:63], a[20:23], v[56:59]\n" \
  "v_mfma_f32_16x16x32_bf16 v[60:63], a[60:63], a[28:31], v[60:63]\n" \
  "s_add_u32 s32, s32, 229376\n" \
  "s_addc_u32 s33, s33, 0\n" \
  "v_mfma_f32_16x16x32_bf16 v[60:63], a[56:59], a[24:27], v[60:63]\n" \
  "v_accvgpr_read_b32 v253, a212\n" \
  "buffer_load_dwordx2 v[252:253], v253, s[52:55], s1 offen sc1\n" \
  "s_waitcnt lgkmcnt(0)\n" \
  "s_waitcnt vmcnt(21)\n" \
  "s_barrier\n" \
  "v_mfma_f32_16x16x32_bf16 v[0:3], a[96:99], a[64:67], v[0:3]\n" \
  "s_add_u32 m0, s93, 65536\n" \
  "ds_read_b64_tr_b16 a[32:33], v226 offset:0\n" \
  "v_mfma_f32_16x16x32_bf16 v[0:3], a[100:103], a[68:71], v[0:3]\n" \
  "buffer_load_dwordx4 v235, s[32:35], s96 offen lds\n" \
  "v_mfma_f32_16x16x32_bf16 v[4:7], a[100:103], a[76:79], v[4:7]\n" \
  "ds_read_b64_tr_b16 a[34:35], v226 offset:1024\n" \
  "ds_read_b64_tr_b16 a[36:37], v226 offset:8192\n" \
  "v_mfma_f32_16x16x32_bf16 v[4:7], a[96:99], a[72:75], v[4:7]\n" \
  "s_add_u32 m0, m0, s82\n" \
  "ds_read_b64_tr_b16 a[38:39], v226 offset:9216\n" \
  "v_mfma_f32_16x16x32_bf16 v[8:11], a[96:99], a[80:83], v[8:11]\n" \
  "buffer_load_dwordx4 v235, s[32:35], s97 offen lds\n" \
  "v_mfma_f32_16x16x32_bf16 v[8:11], a[100:103], a[84:87], v[8:11]\n" \
  "ds_read_b128 a[0:3], v224 offset:0\n" \
  "ds_read_b128 a[4:7], v224 offset:1024\n" \
  "v_mfma_f32_16x16x32_bf16 v[12:15], a[100:103], a[92:95], v[12:15]\n" \
  "ds_read_b128 a[8:11], v224 offset:2048\n" \
  "ds_read_b128 a[12:15], v224 offset:3072\n" \
  "v_mfma_f32_16x16x32_bf16 v[12:15], a[96:99], a[88:91], v[12:15]\n" \
  "s_add_u32 m0, m0, s82\n" \
  "ds_read_b128 a[16:19], v224 offset:4096\n" \
  "v_mfma_f32_16x16x32_bf16 v[16:19], a[104:107], a[64:67], v[16:19]\n" \
  "buffer_load_dwordx4 v235, s[32:35], s98 offen lds\n" \
  "v_mfma_f32_16x16x32_bf16 v[16:19], a[108:111], a[68:71], v[16:19]\n" \
  "ds_read_b128 a[20:23], v224 offset:5120\n" \
  "ds_read_b128 a[24:27], v224 offset:6144\n" \
  "v_mfma_f32_16x16x32_bf16 v[20:23], a[108:111], a[76:79], v[20:23]\n" \
  "s_add_u32 m0, m0, s82\n" \
  "ds_read_b128 a[28:31], v224 offset:7168\n" \
  "v_mfma_f32_16x16x32_bf16 v[20:23], a[104:107], a[72:75], v[20:23]\n" \
  "buffer_load_dwordx4 v235, s[32:35], s99 offen lds\n" \
  "v_mfma_f32_16x16x32_bf16 v[24:27], a[104:107], a[80:83], v[24:27]\n" \
  "ds_read_b64_tr_b16 a[40:41], v227 offset:0\n" \
  "ds_read_b64_tr_b16 a[42:43], v227 offset:1024\n" \
  "v_mfma_f32_16x16x32_bf16 v[24:27], a[108:111], a[84:87], v[24:27]\n" \
  "ds_read_b64_tr_b16 a[44:45], v227 offset:8192\n" \
  "v_mfma_f32_16x16x32_bf16 v[28:31], a[108:111], a[92:95], v[28:31]\n" \
  "s_add_u32 m0, s92, 65536\n" \
  "ds_read_b64_tr_b16 a[46:47], v227 offset:9216\n" \
  "v_mfma_f32_16x16x32_bf16 v[28:31], a[104:107], a[88:91], v[28:31]\n" \
  "buffer_load_dwordx4 v234, s[28:31], 0 offen sc1 lds\n" \
  "v_mfma_f32_16x16x32_bf16 v[32:35], a[112:115], a[64:67], v[32:35]\n" \
  "ds_read_b64_tr_b16 a[48:49], v228 offset:0\n" \
  "v_mfma_f32_16x16x32_bf16 v[32:35], a[116:119], a[68:71], v[32:35]\n" \
  "ds_read_b64_tr_b16 a[50:51], v228 offset:1024\n" \
  "v_mfma_f32_16x16x32_bf16 v[36:39], a[116:119], a[76:79], v[36:39]\n" \
  "buffer_load_dwordx4 v234, s[28:31], 0 offen offset:1024 sc1 lds\n" \
  "v_mfma_f32_16x16x32_bf16 v[36:39], a[112:115], a[72:75], v[36:39]\n" \
  "ds_read_b64_tr_b16 a[52:53], v228 offset:8192\n" \
  "v_mfma_f32_16x16x32_bf16 v[40:43], a[112:115], a[80:83], v[40:43]\n" \
  "ds_read_b64_tr_b16 a[54:55], v228 offset:9216\n" \
  "v_mfma_f32_16x16x32_bf16 v[40:43], a[116:119], a[84:87], v[40:43]\n" \
  "s_add_u32 m0, m0, 2048\n" \
  "ds_read_b64_tr_b16 a[56:57], v229 offset:0\n" \
  "v_mfma_f32_16x16x32_bf16 v[44:47], a[116:119], a[92:95], v[44:47]\n" \
  "buffer_load_dwordx4 v234, s[28:31], s85 offen sc1 lds\n" \
  "v_mfma_f32_16x16x32_bf16 v[44:47], a[112:115], a[88:91], v[44:47]\n" \
  "ds_read_b64_tr_b16 a[58:59], v229 offset:1024\n" \
  "v_mfma_f32_16x16x32_bf16 v[48:51], a[120:123], a[64:67], v[48:51]\n" \
  "ds_read_b64_tr_b16 a[60:61], v229 offset:8192\n" \
  "v_mfma_f32_16x16x32_bf16 v[48:51], a[124:127], a[68:71], v[48:51]\n" \
  "buffer_load_dwordx4 v234, s[28:31], s85 offen offset:1024 sc1 lds\n" \
  "v_mfma_f32_16x16x32_bf16 v[52:55], a[124:127], a[76:79], v[52:55]\n" \
  "ds_read_b64_tr_b16 a[62:63], v229 offset:9216\n" \
  "v_mfma_f32_16x16x32_bf16 v[52:55], a[120:123], a[72:75], v[52:55]\n" \
  "v_mfma_f32_16x16x32_bf16 v[56:59], a[120:123], a[80:83], v[56:59]\n" \
  "s_add_u32 s28, s28, 2048\n" \
  "s_addc_u32 s29, s29, 0\n" \
  "v_mfma_f32_16x16x32_bf16 v[56:59], a[124:127], a[84:87], v[56:59]\n" \
  "v_mfma_f32_16x16x32_bf16 v[60:63], a[124:127], a[92:95], v[60:63]\n" \
  "s_sub_u32 s32, s32, 3440640\n" \
  "s_subb_u32 s33, s33, 0\n" \
  "v_mfma_f32_16x16x32_bf16 v[60:63], a[120:123], a[88:91], v[60:63]\n" \
  "s_add_u32 s96, s95, 65536\n" \
  "s_add_u32 s97, s95, 98304\n" \
  "s_add_u32 s98, s95, 294912\n" \
  "s_add_u32 s99, s95, 327680\n" \
  "s_waitcnt lgkmcnt(0)\n" \
  "s_waitcnt vmcnt(17)\n" \
  "s_barrier\n" \
  "s_waitcnt vmcnt(8)\n" \
  "v_xor_b32 v252, v252, v254\n" \
  "v_xor_b32 v253, v253, v255\n" \
  "v_or_b32 v252, v252, v253\n" \
  "v_cmp_ne_u32 vcc, 0, v252\n" \
  "s_nop 4\n" \
  "s_cbranch_vccnz Lslow3_%=\n" \
  "Lres3_%=:\n" \
  "s_add_u32 s1, s1, 8192\n" \
  "v_mfma_f32_16x16x32_bf16 v[0:3], a[32:35], a[0:3], v[0:3]\n" \
  "s_add_u32 m0, s93, 0\n" \
  "ds_read_b64_tr_b16 a[96:97], v230 offset:32768\n" \
  "v_xor_b32 v157, v157, v254\n" \
  "v_xor_b32 v159, v159, v254\n" \
  "v_mfma_f32_16x16x32_bf16 v[0:3], a[36:39], a[4:7], v[0:3]\n" \
  "buffer_load_dwordx4 v235, s[32:35], s96 offen lds\n" \
  "v_xor_b32 v161, v161, v254\n" \
  "v_mfma_f32_16x16x32_bf16 v[4:7], a[36:39], a[12:15], v[4:7]\n" \
  "ds_read_b64_tr_b16 a[98:99], v230 offset:33792\n" \
  "ds_read_b64_tr_b16 a[100:101], v230 offset:40960\n" \
  "v_xor_b32 v163, v163, v254\n" \
  "v_xor_b32 v165, v165, v254\n" \
  "v_mfma_f32_16x16x32_bf16 v[4:7], a[32:35], a[8:11], v[4:7]\n" \
  "s_add_u32 m0, m0, s82\n" \
  "ds_read_b64_tr_b16 a[102:103], v230 offset:41984\n" \
  "v_xor_b32 v167, v167, v254\n" \
  "v_xor_b32 v169, v169, v254\n" \
  "v_mfma_f32_16x16x32_bf16 v[8:11], a[32:35], a[16:19], v[8:11]\n" \
  "buffer_load_dwordx4 v235, s[32:35], s97 offen lds\n" \
  "v_xor_b32 v171, v171, v254\n" \
  "v_mfma_f32_16x16x32_bf16 v[8:11], a[36:39], a[20:23], v[8:11]\n" \
  "ds_read_b128 a[64:67], v225 offset:32768\n" \
  "ds_read_b128 a[68:71], v225 offset:33792\n" \
  "v_or3_b32 v212, v157, v159, v165\n" \
  "v_or3_b32 v213, v161, v163, v169\n" \
  "v_mfma_f32_16x16x32_bf16 v[12:15], a[36:39], a[28:31], v[12:15]\n" \
  "ds_read_b128 a[72:75], v225 offset:34816\n" \
  "ds_read_b128 a[76:79], v225 offset:35840\n" \
  "v_or_b32 v212, v212, v167\n" \
  "v_or_b32 v213, v213, v171\n" \
  "v_mfma_f32_16x16x32_bf16 v[12:15], a[32:35], a[24:27], v[12:15]\n" \
  "s_add_u32 m0, m0, s82\n" \
  "ds_read_b128 a[80:83], v225 offset:36864\n" \
  "v_min_u32 v212, 1, v212\n" \
  "v_min_u32 v213, 1, v213\n" \
  "v_mfma_f32_16x16x32_bf16 v[16:19], a[40:43], a[0:3], v[16:19]\n" \
  "buffer_load_dwordx4 v235, s[32:35], s98 offen lds\n" \
  "v_lshl_or_b32 v212, v213, 1, v212\n" \
  "v_mfma_f32_16x16x32_bf16 v[16:19], a[44:47], a[4:7], v[16:19]\n" \
  "ds_read_b128 a[84:87], v225 offset:37888\n" \
  "ds_read_b128 a[88:91], v225 offset:38912\n" \
  "v_and_b32 v212, v212, v223\n" \
  "v_mfma_f32_16x16x32_bf16 v[20:23], a[44:47], a[12:15], v[20:23]\n" \
  "s_add_u32 m0, m0, s82\n" \
  "ds_read_b128 a[92:95], v225 offset:39936\n" \
  "v_mfma_f32_16x16x32_bf16 v[20:23], a[40:43], a[8:11], v[20:23]\n" \
  "buffer_load_dwordx4 v235, s[32:35], s99 offen lds\n" \
  "v_mfma_f32_16x16x32_bf16 v[24:27], a[40:43], a[16:19], v[24:27]\n" \
  "ds_read_b64_tr_b16 a[104:105], v231 offset:32768\n" \
  "ds_read_b64_tr_b16 a[106:107], v231 offset:33792\n" \
  "v_mfma_f32_16x16x32_bf16 v[24:27], a[44:47], a[20:23], v[24:27]\n" \
  "ds_read_b64_tr_b16 a[108:109], v231 offset:40960\n" \
  "v_cmp_ne_u32 vcc, 0, v212\n" \
  "s_nop 4\n" \
  "s_cbranch_vccnz Lslow0_%=\n" \
  "Lres0_%=:\n" \
  "v_mfma_f32_16x16x32_bf16 v[28:31], a[44:47], a[28:31], v[28:31]\n" \
  "s_add_u32 m0, s92, 0\n" \
  "ds_read_b64_tr_b16 a[110:111], v231 offset:41984\n" \
  "v_and_b32 v212, 1, v223\n" \
  "v_and_b32 v213, 2, v223\n" \
  "v_mfma_f32_16x16x32_bf16 v[28:31], a[40:43], a[24:27], v[28:31]\n" \
  "buffer_load_dwordx4 v234, s[28:31], 0 offen sc1 lds\n" \
  "v_cmp_ne_u32 vcc, 0, v212\n" \
  "v_mfma_f32_16x16x32_bf16 v[32:35], a[48:51], a[0:3], v[32:35]\n" \
  "ds_read_b64_tr_b16 a[112:113], v232 offset:32768\n" \
  "s_nop 1\n" \
  "v_cndmask_b32 v156, 0, v156, vcc\n" \
  "v_mfma_f32_16x16x32_bf16 v[32:35], a[52:55], a[4:7], v[32:35]\n" \
  "ds_read_b64_tr_b16 a[114:115], v232 offset:33792\n" \
  "v_cndmask_b32 v158, 0, v158, vcc\n" \
  "v_cndmask_b32 v164, 0, v164, vcc\n" \
  "v_mfma_f32_16x16x32_bf16 v[36:39], a[52:55], a[12:15], v[36:39]\n" \
  "buffer_load_dwordx4 v234, s[28:31], 0 offen offset:1024 sc1 lds\n" \
  "v_cndmask_b32 v166, 0, v166, vcc\n" \
  "v_mfma_f32_16x16x32_bf16 v[36:39], a[48:51], a[8:11], v[36:39]\n" \
  "ds_read_b64_tr_b16 a[116:117], v232 offset:40960\n" \
  "v_cmp_ne_u32 vcc, 0, v213\n" \
  "s_nop 1\n" \
  "v_mfma_f32_16x16x32_bf16 v[40:43], a[48:51], a[16:19], v[40:43]\n" \
  "ds_read_b64_tr_b16 a[118:119], v232 offset:41984\n" \
  "v_cndmask_b32 v160, 0, v160, vcc\n" \
  "v_mfma_f32_16x16x32_bf16 v[40:43], a[52:55], a[20:23], v[40:43]\n" \
  "s_add_u32 m0, m0, 2048\n" \
  "ds_read_b64_tr_b16 a[120:121], v233 offset:32768\n" \
  "v_cndmask_b32 v162, 0, v162, vcc\n" \
  "v_cndmask_b32 v168, 0, v168, vcc\n" \
  "v_mfma_f32_16x16x32_bf16 v[44:47], a[52:55], a[28:31], v[44:47]\n" \
  "buffer_load_dwordx4 v234, s[28:31], s85 offen sc1 lds\n" \
  "v_cndmask_b32 v170, 0, v170, vcc\n" \
  "v_mfma_f32_16x16x32_bf16 v[44:47], a[48:51], a[24:27], v[44:47]\n" \
  "ds_read_b64_tr_b16 a[122:123], v233 offset:33792\n" \
  "v_mul_f32 v212, v136, v156\n" \
  "v_mul_f32 v213, v136, v158\n" \
  "v_mfma_f32_16x16x32_bf16 v[48:51], a[56:59], a[0:3], v[48:51]\n" \
  "ds_read_b64_tr_b16 a[124:125], v233 offset:40960\n" \
  "v_fma_f32 v212, -v137, v158, v212\n" \
  "v_mfma_f32_16x16x32_bf16 v[48:51], a[60:63], a[4:7], v[48:51]\n" \
  "buffer_load_dwordx4 v234, s[28:31], s85 offen offset:1024 sc1 lds\n" \
  "v_fmac_f32 v213, v137, v156\n" \
  "v_mfma_f32_16x16x32_bf16 v[52:55], a[60:63], a[12:15], v[52:55]\n" \
  "ds_read_b64_tr_b16 a[126:127], v233 offset:41984\n" \
  "v_fmac_f32 v212, v134, v160\n" \
  "v_fmac_f32 v213, v134, v162\n" \
  "v_mfma_f32_16x16x32_bf16 v[52:55], a[56:59], a[8:11], v[52:55]\n" \
  "v_fma_f32 v204, -v135, v162, v212\n" \
  "v_fma_f32 v205, v135, v160, v213\n" \
  "v_mfma_f32_16x16x32_bf16 v[56:59], a[56:59], a[16:19], v[56:59]\n" \
  "s_add_u32 s28, s28, 2048\n" \
  "s_addc_u32 s29, s29, 0\n" \
  "v_mul_f32 v216, v148, v164\n" \
  "v_mfma_f32_16x16x32_bf16 v[56:59], a[60:63], a[20:23], v[56:59]\n" \
  "v_mul_f32 v217, v148, v166\n" \
  "v_fma_f32 v216, -v149, v166, v216\n" \
  "v_mfma_f32_16x16x32_bf16 v[60:63], a[60:63], a[28:31], v[60:63]\n" \
  "s_add_u32 s32, s32, 458752\n" \
  "s_addc_u32 s33, s33, 0\n" \
  "v_fmac_f32 v217, v149, v164\n" \
  "v_mfma_f32_16x16x32_bf16 v[60:63], a[56:59], a[24:27], v[60:63]\n" \
  "v_fmac_f32 v216, v146, v168\n" \
  "v_fmac_f32 v217, v146, v170\n" \
  "s_waitcnt lgkmcnt(0)\n" \
  "s_barrier\n" \
  "v_mfma_f32_16x16x32_bf16 v[0:3], a[96:99], a[64:67], v[0:3]\n" \
  "s_add_u32 m0, s93, 98304\n" \
  "ds_read_b64_tr_b16 a[32:33], v226 offset:32768\n" \
  "v_fma_f32 v206, -v147, v170, v216\n" \
  "v_mfma_f32_16x16x32_bf16 v[0:3], a[100:103], a[68:71], v[0:3]\n" \
  "buffer_load_dwordx4 v235, s[32:35], s96 offen lds\n" \
  "v_fma_f32 v207, v147, v168, v217\n" \
  "v_mfma_f32_16x16x32_bf16 v[4:7], a[100:103], a[76:79], v[4:7]\n" \
  "ds_read_b64_tr_b16 a[34:35], v226 offset:33792\n" \
  "ds_read_b64_tr_b16 a[36:37], v226 offset:40960\n" \
  "v_accvgpr_read_b32 v220, a214\n" \
  "v_accvgpr_read_b32 v221, a215\n" \
  "v_mfma_f32_16x16x32_bf16 v[4:7], a[96:99], a[72:75], v[4:7]\n" \
  "s_add_u32 m0, m0, s82\n" \
  "ds_read_b64_tr_b16 a[38:39], v226 offset:41984\n" \
  "ds_write_b64 v220, v[204:205] offset:5120\n" \
  "ds_write_b64 v220, v[206:207] offset:6400\n" \
  "v_mfma_f32_16x16x32_bf16 v[8:11], a[96:99], a[80:83], v[8:11]\n" \
  "buffer_load_dwordx4 v235, s[32:35], s97 offen lds\n" \
  "v_mfma_f32_16x16x32_bf16 v[8:11], a[100:103], a[84:87], v[8:11]\n" \
  "ds_read_b128 a[0:3], v224 offset:32768\n" \
  "ds_read_b128 a[4:7], v224 offset:33792\n" \
  "v_mfma_f32_16x16x32_bf16 v[12:15], a[100:103], a[92:95], v[12:15]\n" \
  "ds_read_b128 a[8:11], v224 offset:34816\n" \
  "ds_read_b128 a[12:15], v224 offset:35840\n" \
  "v_mfma_f32_16x16x32_bf16 v[12:15], a[96:99], a[88:91], v[12:15]\n" \
  "s_add_u32 m0, m0, s82\n" \
  "ds_read_b128 a[16:19], v224 offset:36864\n" \
  "v_mfma_f32_16x16x32_bf16 v[16:19], a[104:107], a[64:67], v[16:19]\n" \
  "buffer_load_dwordx4 v235, s[32:35], s98 offen lds\n" \
  "v_mfma_f32_16x16x32_bf16 v[16:19], a[108:111], a[68:71], v[16:19]\n" \
  "ds_read_b128 a[20:23], v224 offset:37888\n" \
  "ds_read_b128 a[24:27], v224 offset:38912\n" \
  "v_mfma_f32_16x16x32_bf16 v[20:23], a[108:111], a[76:79], v[20:23]\n" \
  "s_add_u32 m0, m0, s82\n" \
  "ds_read_b128 a[28:31], v224 offset:39936\n" \
  "v_mfma_f32_16x16x32_bf16 v[20:23], a[104:107], a[72:75], v[20:23]\n" \
  "buffer_load_dwordx4 v235, s[32:35], s99 offen lds\n" \
  "v_mfma_f32_16x16x32_bf16 v[24:27], a[104:107], a[80:83], v[24:27]\n" \
  "ds_read_b64_tr_b16 a[40:41], v227 offset:32768\n" \
  "ds_read_b64_tr_b16 a[42:43], v227 offset:33792\n" \
  "v_mfma_f32_16x16x32_bf16 v[24:27], a[108:111], a[84:87], v[24:27]\n" \
  "ds_read_b64_tr_b16 a[44:45], v227 offset:40960\n" \
  "v_mfma_f32_16x16x32_bf16 v[28:31], a[108:111], a[92:95], v[28:31]\n" \
  "s_add_u32 m0, s92, 98304\n" \
  "ds_read_b64_tr_b16 a[46:47], v227 offset:41984\n" \
  "v_mfma_f32_16x16x32_bf16 v[28:31], a[104:107], a[88:91], v[28:31]\n" \
  "buffer_load_dwordx4 v234, s[28:31], 0 offen sc1 lds\n" \
  "v_mfma_f32_16x16x32_bf16 v[32:35], a[112:115], a[64:67], v[32:35]\n" \
  "ds_read_b64_tr_b16 a[48:49], v228 offset:32768\n" \
  "v_mfma_f32_16x16x32_bf16 v[32:35], a[116:119], a[68:71], v[32:35]\n" \
  "ds_read_b64_tr_b16 a[50:51], v228 offset:33792\n" \
  "v_mfma_f32_16x16x32_bf16 v[36:39], a[116:119], a[76:79], v[36:39]\n" \
  "buffer_load_dwordx4 v234, s[28:31], 0 offen offset:1024 sc1 lds\n" \
  "v_mfma_f32_16x16x32_bf16 v[36:39], a[112:115], a[72:75], v[36:39]\n" \
  "ds_read_b64_tr_b16 a[52:53], v228 offset:40960\n" \
  "v_mfma_f32_16x16x32_bf16 v[40:43], a[112:115], a[80:83], v[40:43]\n" \
  "ds_read_b64_tr_b16 a[54:55], v228 offset:41984\n" \
  "v_mfma_f32_16x16x32_bf16 v[40:43], a[116:119], a[84:87], v[40:43]\n" \
  "s_add_u32 m0, m0, 2048\n" \
  "ds_read_b64_tr_b16 a[56:57], v229 offset:32768\n" \
  "v_mfma_f32_16x16x32_bf16 v[44:47], a[116:119], a[92:95], v[44:47]\n" \
  "buffer_load_dwordx4 v234, s[28:31], s85 offen sc1 lds\n" \
  "v_mfma_f32_16x16x32_bf16 v[44:47], a[112:115], a[88:91], v[44:47]\n" \
  "ds_read_b64_tr_b16 a[58:59], v229 offset:33792\n" \
  "v_mfma_f32_16x16x32_bf16 v[48:51], a[120:123], a[64:67], v[48:51]\n" \
  "ds_read_b64_tr_b16 a[60:61], v229 offset:40960\n" \
  "v_mfma_f32_16x16x32_bf16 v[48:51], a[124:127], a[68:71], v[48:51]\n" \
  "buffer_load_dwordx4 v234, s[28:31], s85 offen offset:1024 sc1 lds\n" \
  "v_mfma_f32_16x16x32_bf16 v[52:55], a[124:127], a[76:79], v[52:55]\n" \
  "ds_read_b64_tr_b16 a[62:63], v229 offset:41984\n" \
  "v_mfma_f32_16x16x32_bf16 v[52:55], a[120:123], a[72:75], v[52:55]\n" \
  "v_mfma_f32_16x16x32_bf16 v[56:59], a[120:123], a[80:83], v[56:59]\n" \
  "s_add_u32 s28, s28, 2048\n" \
  "s_addc_u32 s29, s29, 0\n" \
  "v_mfma_f32_16x16x32_bf16 v[56:59], a[124:127], a[84:87], v[56:59]\n" \
  "v_mfma_f32_16x16x32_bf16 v[60:63], a[124:127], a[92:95], v[60:63]\n" \
  "s_add_u32 s32, s32, 458752\n" \
  "s_addc_u32 s33, s33, 0\n" \
  "v_mfma_f32_16x16x32_bf16 v[60:63], a[120:123], a[88:91], v[60:63]\n" \
  "s_waitcnt lgkmcnt(0)\n" \
  "s_waitcnt vmcnt(16)\n" \
  "s_barrier\n" \
  "v_mfma_f32_16x16x32_bf16 v[0:3], a[32:35], a[0:3], v[0:3]\n" \
  "s_add_u32 m0, s93, 32768\n" \
  "ds_read_b64_tr_b16 a[96:97], v230 offset:0\n" \
  "ds_read_b128 v[172:175], v221 offset:5120\n" \
  "ds_read_b128 v[188:191], v221 offset:6400\n" \
  "v_mfma_f32_16x16x32_bf16 v[0:3], a[36:39], a[4:7], v[0:3]\n" \
  "buffer_load_dwordx4 v235, s[32:35], s96 offen lds\n" \
  "ds_read_b128 v[176:179], v221 offset:5136\n" \
  "v_mfma_f32_16x16x32_bf16 v[4:7], a[36:39], a[12:15], v[4:7]\n" \
  "ds_read_b64_tr_b16 a[98:99], v230 offset:1024\n" \
  "ds_read_b64_tr_b16 a[100:101], v230 offset:8192\n" \
  "ds_read_b128 v[192:195], v221 offset:6416\n" \
  "ds_read_b128 v[180:183], v221 offset:5152\n" \
  "v_mfma_f32_16x16x32_bf16 v[4:7], a[32:35], a[8:11], v[4:7]\n" \
  "s_add_u32 m0, m0, s82\n" \
  "ds_read_b64_tr_b16 a[102:103], v230 offset:9216\n" \
  "ds_read_b128 v[196:199], v221 offset:6432\n" \
  "ds_read_b128 v[184:187], v221 offset:5168\n" \
  "v_mfma_f32_16x16x32_bf16 v[8:11], a[32:35], a[16:19], v[8:11]\n" \
  "buffer_load_dwordx4 v235, s[32:35], s97 offen lds\n" \
  "ds_read_b128 v[200:203], v221 offset:6448\n" \
  "v_mfma_f32_16x16x32_bf16 v[8:11], a[36:39], a[20:23], v[8:11]\n" \
  "ds_read_b128 a[64:67], v225 offset:0\n" \
  "ds_read_b128 a[68:71], v225 offset:1024\n" \
  "v_accvgpr_read_b32 v220, a208\n" \
  "s_lshr_b32 s88, s92, 2\n" \
  "v_mfma_f32_16x16x32_bf16 v[12:15], a[36:39], a[28:31], v[12:15]\n" \
  "ds_read_b128 a[72:75], v225 offset:2048\n" \
  "ds_read_b128 a[76:79], v225 offset:3072\n" \
  "v_accvgpr_read_b32 v248, a203\n" \
  "v_accvgpr_read_b32 v249, a204\n" \
  "v_mfma_f32_16x16x32_bf16 v[12:15], a[32:35], a[24:27], v[12:15]\n" \
  "s_add_u32 m0, m0, s82\n" \
  "ds_read_b128 a[80:83], v225 offset:4096\n" \
  "s_waitcnt lgkmcnt(9)\n" \
  "v_add_f32 v172, v172, v180\n" \
  "s_waitcnt lgkmcnt(7)\n" \
  "v_add_f32 v188, v188, v196\n" \
  "v_mfma_f32_16x16x32_bf16 v[16:19], a[40:43], a[0:3], v[16:19]\n" \
  "buffer_load_dwordx4 v235, s[32:35], s98 offen lds\n" \
  "v_add_f32 v173, v173, v181\n" \
  "v_mfma_f32_16x16x32_bf16 v[16:19], a[44:47], a[4:7], v[16:19]\n" \
  "ds_read_b128 a[84:87], v225 offset:5120\n" \
  "ds_read_b128 a[88:91], v225 offset:6144\n" \
  "v_add_f32 v189, v189, v197\n" \
  "v_add_f32 v174, v174, v182\n" \
  "v_mfma_f32_16x16x32_bf16 v[20:23], a[44:47], a[12:15], v[20:23]\n" \
  "s_add_u32 m0, m0, s82\n" \
  "ds_read_b128 a[92:95], v225 offset:7168\n" \
  "v_add_f32 v190, v190, v198\n" \
  "v_add_f32 v175, v175, v183\n" \
  "v_mfma_f32_16x16x32_bf16 v[20:23], a[40:43], a[8:11], v[20:23]\n" \
  "buffer_load_dwordx4 v235, s[32:35], s99 offen lds\n" \
  "v_add_f32 v191, v191, v199\n" \
  "v_mfma_f32_16x16x32_bf16 v[24:27], a[40:43], a[16:19], v[24:27]\n" \
  "ds_read_b64_tr_b16 a[104:105], v231 offset:0\n" \
  "ds_read_b64_tr_b16 a[106:107], v231 offset:1024\n" \
  "s_waitcnt lgkmcnt(11)\n" \
  "v_add_f32 v176, v176, v184\n" \
  "s_waitcnt lgkmcnt(10)\n" \
  "v_add_f32 v192, v192, v200\n" \
  "v_mfma_f32_16x16x32_bf16 v[24:27], a[44:47], a[20:23], v[24:27]\n" \
  "ds_read_b64_tr_b16 a[108:109], v231 offset:8192\n" \
  "v_add_f32 v177, v177, v185\n" \
  "v_mfma_f32_16x16x32_bf16 v[28:31], a[44:47], a[28:31], v[28:31]\n" \
  "s_add_u32 m0, s92, 32768\n" \
  "ds_read_b64_tr_b16 a[110:111], v231 offset:9216\n" \
  "v_add_f32 v193, v193, v201\n" \
  "v_add_f32 v178, v178, v186\n" \
  "v_mfma_f32_16x16x32_bf16 v[28:31], a[40:43], a[24:27], v[28:31]\n" \
  "buffer_load_dwordx4 v234, s[28:31], 0 offen sc1 lds\n" \
  "v_add_f32 v194, v194, v202\n" \
  "v_mfma_f32_16x16x32_bf16 v[32:35], a[48:51], a[0:3], v[32:35]\n" \
  "ds_read_b64_tr_b16 a[112:113], v232 offset:0\n" \
  "v_add_f32 v179, v179, v187\n" \
  "v_add_f32 v195, v195, v203\n" \
  "v_mfma_f32_16x16x32_bf16 v[32:35], a[52:55], a[4:7], v[32:35]\n" \
  "ds_read_b64_tr_b16 a[114:115], v232 offset:1024\n" \
  "v_add_f32 v172, v172, v176\n" \
  "v_mfma_f32_16x16x32_bf16 v[36:39], a[52:55], a[12:15], v[36:39]\n" \
  "buffer_load_dwordx4 v234, s[28:31], 0 offen offset:1024 sc1 lds\n" \
  "v_add_f32 v188, v188, v192\n" \
  "v_mfma_f32_16x16x32_bf16 v[36:39], a[48:51], a[8:11], v[36:39]\n" \
  "ds_read_b64_tr_b16 a[116:117], v232 offset:8192\n" \
  "v_add_f32 v173, v173, v177\n" \
  "v_add_f32 v189, v189, v193\n" \
  "v_mfma_f32_16x16x32_bf16 v[40:43], a[48:51], a[16:19], v[40:43]\n" \
  "ds_read_b64_tr_b16 a[118:119], v232 offset:9216\n" \
  "v_add_f32 v174, v174, v178\n" \
  "v_add_f32 v190, v190, v194\n" \
  "v_mfma_f32_16x16x32_bf16 v[40:43], a[52:55], a[20:23], v[40:43]\n" \
  "s_add_u32 m0, m0, 2048\n" \
  "ds_read_b64_tr_b16 a[120:121], v233 offset:0\n" \
  "v_add_f32 v175, v175, v179\n" \
  "v_mfma_f32_16x16x32_bf16 v[44:47], a[52:55], a[28:31], v[44:47]\n" \
  "buffer_load_dwordx4 v234, s[28:31], s85 offen sc1 lds\n" \
  "v_add_f32 v191, v191, v195\n" \
  "v_mfma_f32_16x16x32_bf16 v[44:47], a[48:51], a[24:27], v[44:47]\n" \
  "ds_read_b64_tr_b16 a[122:123], v233 offset:1024\n" \
  "v_add_f32 v172, v172, v174\n" \
  "v_add_f32 v188, v188, v190\n" \
  "v_mfma_f32_16x16x32_bf16 v[48:51], a[56:59], a[0:3], v[48:51]\n" \
  "ds_read_b64_tr_b16 a[124:125], v233 offset:8192\n" \
  "v_add_f32 v173, v173, v175\n" \
  "v_add_f32 v189, v189, v191\n" \
  "v_mfma_f32_16x16x32_bf16 v[48:51], a[60:63], a[4:7], v[48:51]\n" \
  "buffer_load_dwordx4 v234, s[28:31], s85 offen offset:1024 sc1 lds\n" \
  "v_fma_f32 v212, -v139, v173, v208\n" \
  "v_mfma_f32_16x16x32_bf16 v[52:55], a[60:63], a[12:15], v[52:55]\n" \
  "ds_read_b64_tr_b16 a[126:127], v233 offset:9216\n" \
  "v_fma_f32 v213, v139, v172, v209\n" \
  "v_fma_f32 v208, v138, v172, v212\n" \
  "v_mfma_f32_16x16x32_bf16 v[52:55], a[56:59], a[8:11], v[52:55]\n" \
  "v_fma_f32 v209, v138, v173, v213\n" \
  "v_mfma_f32_16x16x32_bf16 v[56:59], a[56:59], a[16:19], v[56:59]\n" \
  "s_add_u32 s28, s28, 2048\n" \
  "s_addc_u32 s29, s29, 0\n" \
  "v_fma_f32 v216, -v151, v189, v210\n" \
  "v_fma_f32 v217, v151, v188, v211\n" \
  "v_mfma_f32_16x16x32_bf16 v[56:59], a[60:63], a[20:23], v[56:59]\n" \
  "v_fma_f32 v210, v150, v188, v216\n" \
  "v_mfma_f32_16x16x32_bf16 v[60:63], a[60:63], a[28:31], v[60:63]\n" \
  "s_add_u32 s32, s32, 458752\n" \
  "s_addc_u32 s33, s33, 0\n" \
  "v_fma_f32 v211, v150, v189, v217\n" \
  "v_mul_f32 v212, v133, v209\n" \
  "v_mfma_f32_16x16x32_bf16 v[60:63], a[56:59], a[24:27], v[60:63]\n" \
  "v_mul_f32 v213, v133, v208\n" \
  "s_waitcnt lgkmcnt(0)\n" \
  "s_waitcnt vmcnt(16)\n" \
  "s_barrier\n" \
  "v_mfma_f32_16x16x32_bf16 v[0:3], a[96:99], a[64:67], v[0:3]\n" \
  "s_add_u32 m0, s93, 65536\n" \
  "ds_read_b64_tr_b16 a[32:33], v226 offset:0\n" \
  "v_fma_f32 v204, v132, v208, -v212\n" \
  "v_fma_f32 v205, v132, v209, v213\n" \
  "v_mfma_f32_16x16x32_bf16 v[0:3], a[100:103], a[68:71], v[0:3]\n" \
  "buffer_load_dwordx4 v235, s[32:35], s96 offen lds\n" \
  "v_mul_f32 v216, v145, v211\n" \
  "v_mfma_f32_16x16x32_bf16 v[4:7], a[100:103], a[76:79], v[4:7]\n" \
  "ds_read_b64_tr_b16 a[34:35], v226 offset:1024\n" \
  "ds_read_b64_tr_b16 a[36:37], v226 offset:8192\n" \
  "v_mul_f32 v217, v145, v210\n" \
  "v_fma_f32 v206, v144, v210, -v216\n" \
  "v_mfma_f32_16x16x32_bf16 v[4:7], a[96:99], a[72:75], v[4:7]\n" \
  "s_add_u32 m0, m0, s82\n" \
  "ds_read_b64_tr_b16 a[38:39], v226 offset:9216\n" \
  "v_fma_f32 v207, v144, v211, v217\n" \
  "v_mfma_f32_16x16x32_bf16 v[8:11], a[96:99], a[80:83], v[8:11]\n" \
  "buffer_load_dwordx4 v235, s[32:35], s97 offen lds\n" \
  "v_fmac_f32 v64, v128, v204\n" \
  "v_mfma_f32_16x16x32_bf16 v[8:11], a[100:103], a[84:87], v[8:11]\n" \
  "ds_read_b128 a[0:3], v224 offset:0\n" \
  "ds_read_b128 a[4:7], v224 offset:1024\n" \
  "v_fmac_f32 v80, v128, v205\n" \
  "v_fmac_f32 v96, v140, v206\n" \
  "v_mfma_f32_16x16x32_bf16 v[12:15], a[100:103], a[92:95], v[12:15]\n" \
  "ds_read_b128 a[8:11], v224 offset:2048\n" \
  "ds_read_b128 a[12:15], v224 offset:3072\n" \
  "v_fmac_f32 v112, v140, v207\n" \
  "v_fmac_f32 v64, v152, v205\n" \
  "v_mfma_f32_16x16x32_bf16 v[12:15], a[96:99], a[88:91], v[12:15]\n" \
  "s_add_u32 m0, m0, s82\n" \
  "ds_read_b128 a[16:19], v224 offset:4096\n" \
  "v_fmac_f32 v80, v129, v204\n" \
  "v_mfma_f32_16x16x32_bf16 v[16:19], a[104:107], a[64:67], v[16:19]\n" \
  "buffer_load_dwordx4 v235, s[32:35], s98 offen lds\n" \
  "v_fmac_f32 v96, v153, v207\n" \
  "v_mfma_f32_16x16x32_bf16 v[16:19], a[108:111], a[68:71], v[16:19]\n" \
  "ds_read_b128 a[20:23], v224 offset:5120\n" \
  "ds_read_b128 a[24:27], v224 offset:6144\n" \
  "v_fmac_f32 v112, v141, v206\n" \
  "v_fmac_f32 v65, v128, v64\n" \
  "v_mfma_f32_16x16x32_bf16 v[20:23], a[108:111], a[76:79], v[20:23]\n" \
  "s_add_u32 m0, m0, s82\n" \
  "ds_read_b128 a[28:31], v224 offset:7168\n" \
  "v_fmac_f32 v81, v128, v80\n" \
  "v_fmac_f32 v97, v140, v96\n" \
  "v_mfma_f32_16x16x32_bf16 v[20:23], a[104:107], a[72:75], v[20:23]\n" \
  "buffer_load_dwordx4 v235, s[32:35], s99 offen lds\n" \
  "v_fmac_f32 v113, v140, v112\n" \
  "v_mfma_f32_16x16x32_bf16 v[24:27], a[104:107], a[80:83], v[24:27]\n" \
  "ds_read_b64_tr_b16 a[40:41], v227 offset:0\n" \
  "ds_read_b64_tr_b16 a[42:43], v227 offset:1024\n" \
  "v_fmac_f32 v65, v152, v80\n" \
  "v_fmac_f32 v81, v129, v64\n" \
  "v_mfma_f32_16x16x32_bf16 v[24:27], a[108:111], a[84:87], v[24:27]\n" \
  "ds_read_b64_tr_b16 a[44:45], v227 offset:8192\n" \
  "v_fmac_f32 v97, v153, v112\n" \
  "v_mfma_f32_16x16x32_bf16 v[28:31], a[108:111], a[92:95], v[28:31]\n" \
  "s_add_u32 m0, s92, 65536\n" \
  "ds_read_b64_tr_b16 a[46:47], v227 offset:9216\n" \
  "v_fmac_f32 v113, v141, v96\n" \
  "v_fmac_f32 v66, v128, v65\n" \
  "v_mfma_f32_16x16x32_bf16 v[28:31], a[104:107], a[88:91], v[28:31]\n" \
  "buffer_load_dwordx4 v234, s[28:31], 0 offen sc1 lds\n" \
  "v_fmac_f32 v82, v128, v81\n" \
  "v_mfma_f32_16x16x32_bf16 v[32:35], a[112:115], a[64:67], v[32:35]\n" \
  "ds_read_b64_tr_b16 a[48:49], v228 offset:0\n" \
  "v_fmac_f32 v98, v140, v97\n" \
  "v_fmac_f32 v114, v140, v113\n" \
  "v_mfma_f32_16x16x32_bf16 v[32:35], a[116:119], a[68:71], v[32:35]\n" \
  "ds_read_b64_tr_b16 a[50:51], v228 offset:1024\n" \
  "v_fmac_f32 v66, v152, v81\n" \
  "v_mfma_f32_16x16x32_bf16 v[36:39], a[116:119], a[76:79], v[36:39]\n" \
  "buffer_load_dwordx4 v234, s[28:31], 0 offen offset:1024 sc1 lds\n" \
  "v_fmac_f32 v82, v129, v65\n" \
  "v_mfma_f32_16x16x32_bf16 v[36:39], a[112:115], a[72:75], v[36:39]\n" \
  "ds_read_b64_tr_b16 a[52:53], v228 offset:8192\n" \
  "v_fmac_f32 v98, v153, v113\n" \
  "v_fmac_f32 v114, v141, v97\n" \
  "v_mfma_f32_16x16x32_bf16 v[40:43], a[112:115], a[80:83], v[40:43]\n" \
  "ds_read_b64_tr_b16 a[54:55], v228 offset:9216\n" \
  "v_fmac_f32 v67, v128, v66\n" \
  "v_fmac_f32 v83, v128, v82\n" \
  "v_mfma_f32_16x16x32_bf16 v[40:43], a[116:119], a[84:87], v[40:43]\n" \
  "s_add_u32 m0, m0, 2048\n" \
  "ds_read_b64_tr_b16 a[56:57], v229 offset:0\n" \
  "v_fmac_f32 v99, v140, v98\n" \
  "v_mfma_f32_16x16x32_bf16 v[44:47], a[116:119], a[92:95], v[44:47]\n" \
  "buffer_load_dwordx4 v234, s[28:31], s85 offen sc1 lds\n" \
  "v_fmac_f32 v115, v140, v114\n" \
  "v_mfma_f32_16x16x32_bf16 v[44:47], a[112:115], a[88:91], v[44:47]\n" \
  "ds_read_b64_tr_b16 a[58:59], v229 offset:1024\n" \
  "v_fmac_f32 v67, v152, v82\n" \
  "v_fmac_f32 v83, v129, v66\n" \
  "v_mfma_f32_16x16x32_bf16 v[48:51], a[120:123], a[64:67], v[48:51]\n" \
  "ds_read_b64_tr_b16 a[60:61], v229 offset:8192\n" \
  "v_fmac_f32 v99, v153, v114\n" \
  "v_fmac_f32 v115, v141, v98\n" \
  "v_mfma_f32_16x16x32_bf16 v[48:51], a[124:127], a[68:71], v[48:51]\n" \
  "buffer_load_dwordx4 v234, s[28:31], s85 offen offset:1024 sc1 lds\n" \
  "v_fmac_f32 v68, v128, v67\n" \
  "v_mfma_f32_16x16x32_bf16 v[52:55], a[124:127], a[76:79], v[52:55]\n" \
  "ds_read_b64_tr_b16 a[62:63], v229 offset:9216\n" \
  "v_fmac_f32 v84, v128, v83\n" \
  "v_fmac_f32 v100, v140, v99\n" \
  "v_mfma_f32_16x16x32_bf16 v[52:55], a[120:123], a[72:75], v[52:55]\n" \
  "v_fmac_f32 v116, v140, v115\n" \
  "v_mfma_f32_16x16x32_bf16 v[56:59], a[120:123], a[80:83], v[56:59]\n" \
  "s_add_u32 s28, s28, 2048\n" \
  "s_addc_u32 s29, s29, 0\n" \
  "v_fmac_f32 v68, v152, v83\n" \
  "v_fmac_f32 v84, v129, v67\n" \
  "v_mfma_f32_16x16x32_bf16 v[56:59], a[124:127], a[84:87], v[56:59]\n" \
  "v_fmac_f32 v100, v153, v115\n" \
  "v_mfma_f32_16x16x32_bf16 v[60:63], a[124:127], a[92:95], v[60:63]\n" \
  "s_add_u32 s32, s32, 458752\n" \
  "s_addc_u32 s33, s33, 0\n" \
  "v_fmac_f32 v116, v141, v99\n" \
  "v_fmac_f32 v69, v128, v68\n" \
  "v_mfma_f32_16x16x32_bf16 v[60:63], a[120:123], a[88:91], v[60:63]\n" \
  "v_fmac_f32 v85, v128, v84\n" \
  "s_waitcnt lgkmcnt(0)\n" \
  "s_waitcnt vmcnt(16)\n" \
  "s_barrier\n" \
  "v_mfma_f32_16x16x32_bf16 v[0:3], a[32:35], a[0:3], v[0:3]\n" \
  "s_add_u32 m0, s93, 0\n" \
  "ds_read_b64_tr_b16 a[96:97], v230 offset:32768\n" \
  "v_fmac_f32 v101, v140, v100\n" \
  "v_fmac_f32 v117, v140, v116\n" \
  "v_mfma_f32_16x16x32_bf16 v[0:3], a[36:39], a[4:7], v[0:3]\n" \
  "buffer_load_dwordx4 v235, s[32:35], s96 offen lds\n" \
  "v_fmac_f32 v69, v152, v84\n" \
  "v_mfma_f32_16x16x32_bf16 v[4:7], a[36:39], a[12:15], v[4:7]\n" \
  "ds_read_b64_tr_b16 a[98:99], v230 offset:33792\n" \
  "ds_read_b64_tr_b16 a[100:101], v230 offset:40960\n" \
  "v_fmac_f32 v85, v129, v68\n" \
  "v_fmac_f32 v101, v153, v116\n" \
  "v_mfma_f32_16x16x32_bf16 v[4:7], a[32:35], a[8:11], v[4:7]\n" \
  "s_add_u32 m0, m0, s82\n" \
  "ds_read_b64_tr_b16 a[102:103], v230 offset:41984\n" \
  "v_fmac_f32 v117, v141, v100\n" \
  "v_mfma_f32_16x16x32_bf16 v[8:11], a[32:35], a[16:19], v[8:11]\n" \
  "buffer_load_dwordx4 v235, s[32:35], s97 offen lds\n" \
  "v_fmac_f32 v70, v128, v69\n" \
  "v_mfma_f32_16x16x32_bf16 v[8:11], a[36:39], a[20:23], v[8:11]\n" \
  "ds_read_b128 a[64:67], v225 offset:32768\n" \
  "ds_read_b128 a[68:71], v225 offset:33792\n" \
  "v_fmac_f32 v86, v128, v85\n" \
  "v_fmac_f32 v102, v140, v101\n" \
  "v_mfma_f32_16x16x32_bf16 v[12:15], a[36:39], a[28:31], v[12:15]\n" \
  "ds_read_b128 a[72:75], v225 offset:34816\n" \
  "ds_read_b128 a[76:79], v225 offset:35840\n" \
  "v_fmac_f32 v118, v140, v117\n" \
  "v_fmac_f32 v70, v152, v85\n" \
  "v_mfma_f32_16x16x32_bf16 v[12:15], a[32:35], a[24:27], v[12:15]\n" \
  "s_add_u32 m0, m0, s82\n" \
  "ds_read_b128 a[80:83], v225 offset:36864\n" \
  "v_fmac_f32 v86, v129, v69\n" \
  "v_mfma_f32_16x16x32_bf16 v[16:19], a[40:43], a[0:3], v[16:19]\n" \
  "buffer_load_dwordx4 v235, s[32:35], s98 offen lds\n" \
  "v_fmac_f32 v102, v153, v117\n" \
  "v_mfma_f32_16x16x32_bf16 v[16:19], a[44:47], a[4:7], v[16:19]\n" \
  "ds_read_b128 a[84:87], v225 offset:37888\n" \
  "ds_read_b128 a[88:91], v225 offset:38912\n" \
  "v_fmac_f32 v118, v141, v101\n" \
  "v_fmac_f32 v71, v128, v70\n" \
  "v_mfma_f32_16x16x32_bf16 v[20:23], a[44:47], a[12:15], v[20:23]\n" \
  "s_add_u32 m0, m0, s82\n" \
  "ds_read_b128 a[92:95], v225 offset:39936\n" \
  "v_fmac_f32 v87, v128, v86\n" \
  "v_fmac_f32 v103, v140, v102\n" \
  "v_mfma_f32_16x16x32_bf16 v[20:23], a[40:43], a[8:11], v[20:23]\n" \
  "buffer_load_dwordx4 v235, s[32:35], s99 offen lds\n" \
  "v_fmac_f32 v119, v140, v118\n" \
  "v_mfma_f32_16x16x32_bf16 v[24:27], a[40:43], a[16:19], v[24:27]\n" \
  "ds_read_b64_tr_b16 a[104:105], v231 offset:32768\n" \
  "ds_read_b64_tr_b16 a[106:107], v231 offset:33792\n" \
  "v_fmac_f32 v71, v152, v86\n" \
  "v_fmac_f32 v87, v129, v70\n" \
  "v_mfma_f32_16x16x32_bf16 v[24:27], a[44:47], a[20:23], v[24:27]\n" \
  "ds_read_b64_tr_b16 a[108:109], v231 offset:40960\n" \
  "v_fmac_f32 v103, v153, v118\n" \
  "v_mfma_f32_16x16x32_bf16 v[28:31], a[44:47], a[28:31], v[28:31]\n" \
  "s_add_u32 m0, s92, 0\n" \
  "ds_read_b64_tr_b16 a[110:111], v231 offset:41984\n" \
  "v_fmac_f32 v119, v141, v102\n" \
  "v_fmac_f32 v72, v128, v71\n" \
  "v_mfma_f32_16x16x32_bf16 v[28:31], a[40:43], a[24:27], v[28:31]\n" \
  "buffer_load_dwordx4 v234, s[28:31], 0 offen sc1 lds\n" \
  "v_fmac_f32 v88, v128, v87\n" \
  "v_mfma_f32_16x16x32_bf16 v[32:35], a[48:51], a[0:3], v[32:35]\n" \
  "ds_read_b64_tr_b16 a[112:113], v232 offset:32768\n" \
  "v_fmac_f32 v104, v140, v103\n" \
  "v_fmac_f32 v120, v140, v119\n" \
  "v_mfma_f32_16x16x32_bf16 v[32:35], a[52:55], a[4:7], v[32:35]\n" \
  "ds_read_b64_tr_b16 a[114:115], v232 offset:33792\n" \
  "v_fmac_f32 v72, v152, v87\n" \
  "v_mfma_f32_16x16x32_bf16 v[36:39], a[52:55], a[12:15], v[36:39]\n" \
  "buffer_load_dwordx4 v234, s[28:31], 0 offen offset:1024 sc1 lds\n" \
  "v_fmac_f32 v88, v129, v71\n" \
  "v_mfma_f32_16x16x32_bf16 v[36:39], a[48:51], a[8:11], v[36:39]\n" \
  "ds_read_b64_tr_b16 a[116:117], v232 offset:40960\n" \
  "v_fmac_f32 v104, v153, v119\n" \
  "v_fmac_f32 v120, v141, v103\n" \
  "v_mfma_f32_16x16x32_bf16 v[40:43], a[48:51], a[16:19], v[40:43]\n" \
  "ds_read_b64_tr_b16 a[118:119], v232 offset:41984\n" \
  "v_cvt_pk_bf16_f32 v172, v64, v65\n" \
  "v_cvt_pk_bf16_f32 v173, v66, v67\n" \
  "v_mfma_f32_16x16x32_bf16 v[40:43], a[52:55], a[20:23], v[40:43]\n" \
  "s_add_u32 m0, m0, 2048\n" \
  "ds_read_b64_tr_b16 a[120:121], v233 offset:32768\n" \
  "v_cvt_pk_bf16_f32 v174, v68, v69\n" \
  "v_mfma_f32_16x16x32_bf16 v[44:47], a[52:55], a[28:31], v[44:47]\n" \
  "buffer_load_dwordx4 v234, s[28:31], s85 offen sc1 lds\n" \
  "v_cvt_pk_bf16_f32 v175, v70, v71\n" \
  "v_mfma_f32_16x16x32_bf16 v[44:47], a[48:51], a[24:27], v[44:47]\n" \
  "ds_read_b64_tr_b16 a[122:123], v233 offset:33792\n" \
  "s_mov_b32 s69, 0\n" \
  "v_mfma_f32_16x16x32_bf16 v[48:51], a[56:59], a[0:3], v[48:51]\n" \
  "ds_read_b64_tr_b16 a[124:125], v233 offset:40960\n" \
  "buffer_store_dwordx4 v[172:175], v220, s[36:39], s69 offen sc1\n" \
  "v_mfma_f32_16x16x32_bf16 v[48:51], a[60:63], a[4:7], v[48:51]\n" \
  "buffer_load_dwordx4 v234, s[28:31], s85 offen offset:1024 sc1 lds\n" \
  "v_cvt_pk_bf16_f32 v176, -v80, -v81\n" \
  "v_mfma_f32_16x16x32_bf16 v[52:55], a[60:63], a[12:15], v[52:55]\n" \
  "ds_read_b64_tr_b16 a[126:127], v233 offset:41984\n" \
  "v_cvt_pk_bf16_f32 v177, -v82, -v83\n" \
  "v_cvt_pk_bf16_f32 v178, -v84, -v85\n" \
  "v_mfma_f32_16x16x32_bf16 v[52:55], a[56:59], a[8:11], v[52:55]\n" \
  "v_cvt_pk_bf16_f32 v179, -v86, -v87\n" \
  "s_mov_b32 s69, 16384\n" \
  "v_mfma_f32_16x16x32_bf16 v[56:59], a[56:59], a[16:19], v[56:59]\n" \
  "s_add_u32 s28, s28, 2048\n" \
  "s_addc_u32 s29, s29, 0\n" \
  "v_mfma_f32_16x16x32_bf16 v[56:59], a[60:63], a[20:23], v[56:59]\n" \
  "buffer_store_dwordx4 v[176:179], v220, s[36:39], s69 offen sc1\n" \
  "v_mfma_f32_16x16x32_bf16 v[60:63], a[60:63], a[28:31], v[60:63]\n" \
  "s_add_u32 s32, s32, 458752\n" \
  "s_addc_u32 s33, s33, 0\n" \
  "v_cvt_pk_bf16_f32 v180, v96, v97\n" \
  "v_cvt_pk_bf16_f32 v181, v98, v99\n" \
  "v_mfma_f32_16x16x32_bf16 v[60:63], a[56:59], a[24:27], v[60:63]\n" \
  "v_cvt_pk_bf16_f32 v182, v100, v101\n" \
  "s_waitcnt lgkmcnt(0)\n" \
  "s_waitcnt vmcnt(18)\n" \
  "s_barrier\n" \
  "v_mfma_f32_16x16x32_bf16 v[0:3], a[96:99], a[64:67], v[0:3]\n" \
  "s_add_u32 m0, s93, 98304\n" \
  "ds_read_b64_tr_b16 a[32:33], v226 offset:32768\n" \
  "v_cvt_pk_bf16_f32 v183, v102, v103\n" \
  "s_mov_b32 s69, 4096\n" \
  "v_mfma_f32_16x16x32_bf16 v[0:3], a[100:103], a[68:71], v[0:3]\n" \
  "buffer_load_dwordx4 v235, s[32:35], s96 offen lds\n" \
  "v_mfma_f32_16x16x32_bf16 v[4:7], a[100:103], a[76:79], v[4:7]\n" \
  "ds_read_b64_tr_b16 a[34:35], v226 offset:33792\n" \
  "ds_read_b64_tr_b16 a[36:37], v226 offset:40960\n" \
  "v_mfma_f32_16x16x32_bf16 v[4:7], a[96:99], a[72:75], v[4:7]\n" \
  "s_add_u32 m0, m0, s82\n" \
  "ds_read_b64_tr_b16 a[38:39], v226 offset:41984\n" \
  "v_mfma_f32_16x16x32_bf16 v[8:11], a[96:99], a[80:83], v[8:11]\n" \
  "buffer_load_dwordx4 v235, s[32:35], s97 offen lds\n" \
  "v_mfma_f32_16x16x32_bf16 v[8:11], a[100:103], a[84:87], v[8:11]\n" \
  "ds_read_b128 a[0:3], v224 offset:32768\n" \
  "ds_read_b128 a[4:7], v224 offset:33792\n" \
  "v_mfma_f32_16x16x32_bf16 v[12:15], a[100:103], a[92:95], v[12:15]\n" \
  "ds_read_b128 a[8:11], v224 offset:34816\n" \
  "ds_read_b128 a[12:15], v224 offset:35840\n" \
  "v_mfma_f32_16x16x32_bf16 v[12:15], a[96:99], a[88:91], v[12:15]\n" \
  "s_add_u32 m0, m0, s82\n" \
  "ds_read_b128 a[16:19], v224 offset:36864\n" \
  "v_mfma_f32_16x16x32_bf16 v[16:19], a[104:107], a[64:67], v[16:19]\n" \
  "buffer_load_dwordx4 v235, s[32:35], s98 offen lds\n" \
  "v_mfma_f32_16x16x32_bf16 v[16:19], a[108:111], a[68:71], v[16:19]\n" \
  "ds_read_b128 a[20:23], v224 offset:37888\n" \
  "ds_read_b128 a[24:27], v224 offset:38912\n" \
  "v_mfma_f32_16x16x32_bf16 v[20:23], a[108:111], a[76:79], v[20:23]\n" \
  "s_add_u32 m0, m0, s82\n" \
  "ds_read_b128 a[28:31], v224 offset:39936\n" \
  "v_mfma_f32_16x16x32_bf16 v[20:23], a[104:107], a[72:75], v[20:23]\n" \
  "buffer_load_dwordx4 v235, s[32:35], s99 offen lds\n" \
  "v_mfma_f32_16x16x32_bf16 v[24:27], a[104:107], a[80:83], v[24:27]\n" \
  "ds_read_b64_tr_b16 a[40:41], v227 offset:32768\n" \
  "ds_read_b64_tr_b16 a[42:43], v227 offset:33792\n" \
  "v_mfma_f32_16x16x32_bf16 v[24:27], a[108:111], a[84:87], v[24:27]\n" \
  "ds_read_b64_tr_b16 a[44:45], v227 offset:40960\n" \
  "buffer_store_dwordx4 v[180:183], v220, s[36:39], s69 offen sc1\n" \
  "v_mfma_f32_16x16x32_bf16 v[28:31], a[108:111], a[92:95], v[28:31]\n" \
  "s_add_u32 m0, s92, 98304\n" \
  "ds_read_b64_tr_b16 a[46:47], v227 offset:41984\n" \
  "v_cvt_pk_bf16_f32 v184, -v112, -v113\n" \
  "v_cvt_pk_bf16_f32 v185, -v114, -v115\n" \
  "v_mfma_f32_16x16x32_bf16 v[28:31], a[104:107], a[88:91], v[28:31]\n" \
  "buffer_load_dwordx4 v234, s[28:31], 0 offen sc1 lds\n" \
  "v_cvt_pk_bf16_f32 v186, -v116, -v117\n" \
  "v_mfma_f32_16x16x32_bf16 v[32:35], a[112:115], a[64:67], v[32:35]\n" \
  "ds_read_b64_tr_b16 a[48:49], v228 offset:32768\n" \
  "v_cvt_pk_bf16_f32 v187, -v118, -v119\n" \
  "s_mov_b32 s69, 20480\n" \
  "v_mfma_f32_16x16x32_bf16 v[32:35], a[116:119], a[68:71], v[32:35]\n" \
  "ds_read_b64_tr_b16 a[50:51], v228 offset:33792\n" \
  "buffer_store_dwordx4 v[184:187], v220, s[36:39], s69 offen sc1\n" \
  "v_mfma_f32_16x16x32_bf16 v[36:39], a[116:119], a[76:79], v[36:39]\n" \
  "buffer_load_dwordx4 v234, s[28:31], 0 offen offset:1024 sc1 lds\n" \
  "v_fmac_f32 v73, v128, v72\n" \
  "v_mfma_f32_16x16x32_bf16 v[36:39], a[112:115], a[72:75], v[36:39]\n" \
  "ds_read_b64_tr_b16 a[52:53], v228 offset:40960\n" \
  "v_fmac_f32 v89, v128, v88\n" \
  "v_fmac_f32 v105, v140, v104\n" \
  "v_mfma_f32_16x16x32_bf16 v[40:43], a[112:115], a[80:83], v[40:43]\n" \
  "ds_read_b64_tr_b16 a[54:55], v228 offset:41984\n" \
  "v_fmac_f32 v121, v140, v120\n" \
  "v_mfma_f32_16x16x32_bf16 v[40:43], a[116:119], a[84:87], v[40:43]\n" \
  "s_add_u32 m0, m0, 2048\n" \
  "ds_read_b64_tr_b16 a[56:57], v229 offset:32768\n" \
  "v_fmac_f32 v73, v152, v88\n" \
  "v_fmac_f32 v89, v129, v72\n" \
  "v_mfma_f32_16x16x32_bf16 v[44:47], a[116:119], a[92:95], v[44:47]\n" \
  "buffer_load_dwordx4 v234, s[28:31], s85 offen sc1 lds\n" \
  "v_fmac_f32 v105, v153, v120\n" \
  "v_mfma_f32_16x16x32_bf16 v[44:47], a[112:115], a[88:91], v[44:47]\n" \
  "ds_read_b64_tr_b16 a[58:59], v229 offset:33792\n" \
  "v_fmac_f32 v121, v141, v104\n" \
  "v_fmac_f32 v74, v128, v73\n" \
  "v_mfma_f32_16x16x32_bf16 v[48:51], a[120:123], a[64:67], v[48:51]\n" \
  "ds_read_b64_tr_b16 a[60:61], v229 offset:40960\n" \
  "v_fmac_f32 v90, v128, v89\n" \
  "v_mfma_f32_16x16x32_bf16 v[48:51], a[124:127], a[68:71], v[48:51]\n" \
  "buffer_load_dwordx4 v234, s[28:31], s85 offen offset:1024 sc1 lds\n" \
  "v_fmac_f32 v106, v140, v105\n" \
  "v_mfma_f32_16x16x32_bf16 v[52:55], a[124:127], a[76:79], v[52:55]\n" \
  "ds_read_b64_tr_b16 a[62:63], v229 offset:41984\n" \
  "v_fmac_f32 v122, v140, v121\n" \
  "v_fmac_f32 v74, v152, v89\n" \
  "v_mfma_f32_16x16x32_bf16 v[52:55], a[120:123], a[72:75], v[52:55]\n" \
  "v_fmac_f32 v90, v129, v73\n" \
  "v_fmac_f32 v106, v153, v121\n" \
  "v_mfma_f32_16x16x32_bf16 v[56:59], a[120:123], a[80:83], v[56:59]\n" \
  "s_add_u32 s28, s28, 2048\n" \
  "s_addc_u32 s29, s29, 0\n" \
  "v_fmac_f32 v122, v141, v105\n" \
  "v_mfma_f32_16x16x32_bf16 v[56:59], a[124:127], a[84:87], v[56:59]\n" \
  "v_fmac_f32 v75, v128, v74\n" \
  "v_fmac_f32 v91, v128, v90\n" \
  "v_mfma_f32_16x16x32_bf16 v[60:63], a[124:127], a[92:95], v[60:63]\n" \
  "s_add_u32 s32, s32, 458752\n" \
  "s_addc_u32 s33, s33, 0\n" \
  "v_fmac_f32 v107, v140, v106\n" \
  "v_mfma_f32_16x16x32_bf16 v[60:63], a[120:123], a[88:91], v[60:63]\n" \
  "v_fmac_f32 v123, v140, v122\n" \
  "v_fmac_f32 v75, v152, v90\n" \
  "s_waitcnt lgkmcnt(0)\n" \
  "s_waitcnt vmcnt(20)\n" \
  "s_barrier\n" \
  "v_mfma_f32_16x16x32_bf16 v[0:3], a[32:35], a[0:3], v[0:3]\n" \
  "s_add_u32 m0, s93, 32768\n" \
  "ds_read_b64_tr_b16 a[96:97], v230 offset:0\n" \
  "v_fmac_f32 v91, v129, v74\n" \
  "v_mfma_f32_16x16x32_bf16 v[0:3], a[36:39], a[4:7], v[0:3]\n" \
  "buffer_load_dwordx4 v235, s[32:35], s96 offen lds\n" \
  "v_fmac_f32 v107, v153, v122\n" \
  "v_mfma_f32_16x16x32_bf16 v[4:7], a[36:39], a[12:15], v[4:7]\n" \
  "ds_read_b64_tr_b16 a[98:99], v230 offset:1024\n" \
  "ds_read_b64_tr_b16 a[100:101], v230 offset:8192\n" \
  "v_fmac_f32 v123, v141, v106\n" \
  "v_fmac_f32 v76, v128, v75\n" \
  "v_mfma_f32_16x16x32_bf16 v[4:7], a[32:35], a[8:11], v[4:7]\n" \
  "s_add_u32 m0, m0, s82\n" \
  "ds_read_b64_tr_b16 a[102:103], v230 offset:9216\n" \
  "v_fmac_f32 v92, v128, v91\n" \
  "v_fmac_f32 v108, v140, v107\n" \
  "v_mfma_f32_16x16x32_bf16 v[8:11], a[32:35], a[16:19], v[8:11]\n" \
  "buffer_load_dwordx4 v235, s[32:35], s97 offen lds\n" \
  "v_fmac_f32 v124, v140, v123\n" \
  "v_mfma_f32_16x16x32_bf16 v[8:11], a[36:39], a[20:23], v[8:11]\n" \
  "ds_read_b128 a[64:67], v225 offset:0\n" \
  "ds_read_b128 a[68:71], v225 offset:1024\n" \
  "v_fmac_f32 v76, v152, v91\n" \
  "v_fmac_f32 v92, v129, v75\n" \
  "v_mfma_f32_16x16x32_bf16 v[12:15], a[36:39], a[28:31], v[12:15]\n" \
  "ds_read_b128 a[72:75], v225 offset:2048\n" \
  "ds_read_b128 a[76:79], v225 offset:3072\n" \
  "v_fmac_f32 v108, v153, v123\n" \
  "v_mfma_f32_16x16x32_bf16 v[12:15], a[32:35], a[24:27], v[12:15]\n" \
  "s_add_u32 m0, m0, s82\n" \
  "ds_read_b128 a[80:83], v225 offset:4096\n" \
  "v_fmac_f32 v124, v141, v107\n" \
  "v_fmac_f32 v77, v128, v76\n" \
  "v_mfma_f32_16x16x32_bf16 v[16:19], a[40:43], a[0:3], v[16:19]\n" \
  "buffer_load_dwordx4 v235, s[32:35], s98 offen lds\n" \
  "v_fmac_f32 v93, v128, v92\n" \
  "v_mfma_f32_16x16x32_bf16 v[16:19], a[44:47], a[4:7], v[16:19]\n" \
  "ds_read_b128 a[84:87], v225 offset:5120\n" \
  "ds_read_b128 a[88:91], v225 offset:6144\n" \
  "v_fmac_f32 v109, v140, v108\n" \
  "v_fmac_f32 v125, v140, v124\n" \
  "v_mfma_f32_16x16x32_bf16 v[20:23], a[44:47], a[12:15], v[20:23]\n" \
  "s_add_u32 m0, m0, s82\n" \
  "ds_read_b128 a[92:95], v225 offset:7168\n" \
  "v_fmac_f32 v77, v152, v92\n" \
  "v_mfma_f32_16x16x32_bf16 v[20:23], a[40:43], a[8:11], v[20:23]\n" \
  "buffer_load_dwordx4 v235, s[32:35], s99 offen lds\n" \
  "v_fmac_f32 v93, v129, v76\n" \
  "v_mfma_f32_16x16x32_bf16 v[24:27], a[40:43], a[16:19], v[24:27]\n" \
  "ds_read_b64_tr_b16 a[104:105], v231 offset:0\n" \
  "ds_read_b64_tr_b16 a[106:107], v231 offset:1024\n" \
  "v_fmac_f32 v109, v153, v124\n" \
  "v_fmac_f32 v125, v141, v108\n" \
  "v_mfma_f32_16x16x32_bf16 v[24:27], a[44:47], a[20:23], v[24:27]\n" \
  "ds_read_b64_tr_b16 a[108:109], v231 offset:8192\n" \
  "v_fmac_f32 v78, v128, v77\n" \
  "v_fmac_f32 v94, v128, v93\n" \
  "v_mfma_f32_16x16x32_bf16 v[28:31], a[44:47], a[28:31], v[28:31]\n" \
  "s_add_u32 m0, s92, 32768\n" \
  "ds_read_b64_tr_b16 a[110:111], v231 offset:9216\n" \
  "v_fmac_f32 v110, v140, v109\n" \
  "v_mfma_f32_16x16x32_bf16 v[28:31], a[40:43], a[24:27], v[28:31]\n" \
  "buffer_load_dwordx4 v234, s[28:31], 0 offen sc1 lds\n" \
  "v_fmac_f32 v126, v140, v125\n" \
  "v_mfma_f32_16x16x32_bf16 v[32:35], a[48:51], a[0:3], v[32:35]\n" \
  "ds_read_b64_tr_b16 a[112:113], v232 offset:0\n" \
  "v_fmac_f32 v78, v152, v93\n" \
  "v_fmac_f32 v94, v129, v77\n" \
  "v_mfma_f32_16x16x32_bf16 v[32:35], a[52:55], a[4:7], v[32:35]\n" \
  "ds_read_b64_tr_b16 a[114:115], v232 offset:1024\n" \
  "v_fmac_f32 v110, v153, v125\n" \
  "v_fmac_f32 v126, v141, v109\n" \
  "v_mfma_f32_16x16x32_bf16 v[36:39], a[52:55], a[12:15], v[36:39]\n" \
  "buffer_load_dwordx4 v234, s[28:31], 0 offen offset:1024 sc1 lds\n" \
  "v_fmac_f32 v79, v128, v78\n" \
  "v_mfma_f32_16x16x32_bf16 v[36:39], a[48:51], a[8:11], v[36:39]\n" \
  "ds_read_b64_tr_b16 a[116:117], v232 offset:8192\n" \
  "v_fmac_f32 v95, v128, v94\n" \
  "v_fmac_f32 v111, v140, v110\n" \
  "v_mfma_f32_16x16x32_bf16 v[40:43], a[48:51], a[16:19], v[40:43]\n" \
  "ds_read_b64_tr_b16 a[118:119], v232 offset:9216\n" \
  "v_fmac_f32 v127, v140, v126\n" \
  "v_mfma_f32_16x16x32_bf16 v[40:43], a[52:55], a[20:23], v[40:43]\n" \
  "s_add_u32 m0, m0, 2048\n" \
  "ds_read_b64_tr_b16 a[120:121], v233 offset:0\n" \
  "v_fmac_f32 v79, v152, v94\n" \
  "v_fmac_f32 v95, v129, v78\n" \
  "v_mfma_f32_16x16x32_bf16 v[44:47], a[52:55], a[28:31], v[44:47]\n" \
  "buffer_load_dwordx4 v234, s[28:31], s85 offen sc1 lds\n" \
  "v_fmac_f32 v111, v153, v126\n" \
  "v_mfma_f32_16x16x32_bf16 v[44:47], a[48:51], a[24:27], v[44:47]\n" \
  "ds_read_b64_tr_b16 a[122:123], v233 offset:1024\n" \
  "v_fmac_f32 v127, v141, v110\n" \
  "v_cvt_pk_bf16_f32 v188, v72, v73\n" \
  "v_mfma_f32_16x16x32_bf16 v[48:51], a[56:59], a[0:3], v[48:51]\n" \
  "ds_read_b64_tr_b16 a[124:125], v233 offset:8192\n" \
  "v_cvt_pk_bf16_f32 v189, v74, v75\n" \
  "v_mfma_f32_16x16x32_bf16 v[48:51], a[60:63], a[4:7], v[48:51]\n" \
  "buffer_load_dwordx4 v234, s[28:31], s85 offen offset:1024 sc1 lds\n" \
  "v_cvt_pk_bf16_f32 v190, v76, v77\n" \
  "v_mfma_f32_16x16x32_bf16 v[52:55], a[60:63], a[12:15], v[52:55]\n" \
  "ds_read_b64_tr_b16 a[126:127], v233 offset:9216\n" \
  "v_cvt_pk_bf16_f32 v191, v78, v79\n" \
  "s_mov_b32 s69, 0\n" \
  "v_mfma_f32_16x16x32_bf16 v[52:55], a[56:59], a[8:11], v[52:55]\n" \
  "buffer_store_dwordx4 v[188:191], v220, s[36:39], s69 offen offset:1024 sc1\n" \
  "v_mfma_f32_16x16x32_bf16 v[56:59], a[56:59], a[16:19], v[56:59]\n" \
  "s_add_u32 s28, s28, 2048\n" \
  "s_addc_u32 s29, s29, 0\n" \
  "v_cvt_pk_bf16_f32 v192, -v88, -v89\n" \
  "v_mfma_f32_16x16x32_bf16 v[56:59], a[60:63], a[20:23], v[56:59]\n" \
  "v_cvt_pk_bf16_f32 v193, -v90, -v91\n" \
  "v_mfma_f32_16x16x32_bf16 v[60:63], a[60:63], a[28:31], v[60:63]\n" \
  "s_add_u32 s32, s32, 458752\n" \
  "s_addc_u32 s33, s33, 0\n" \
  "v_cvt_pk_bf16_f32 v194, -v92, -v93\n" \
  "v_cvt_pk_bf16_f32 v195, -v94, -v95\n" \
  "v_mfma_f32_16x16x32_bf16 v[60:63], a[56:59], a[24:27], v[60:63]\n" \
  "s_mov_b32 s69, 16384\n" \
  "s_waitcnt lgkmcnt(0)\n" \
  "s_waitcnt vmcnt(20)\n" \
  "s_barrier\n" \
  "v_mfma_f32_16x16x32_bf16 v[0:3], a[96:99], a[64:67], v[0:3]\n" \
  "s_add_u32 m0, s93, 65536\n" \
  "ds_read_b64_tr_b16 a[32:33], v226 offset:0\n" \
  "v_mfma_f32_16x16x32_bf16 v[0:3], a[100:103], a[68:71], v[0:3]\n" \
  "buffer_load_dwordx4 v235, s[32:35], s96 offen lds\n" \
  "v_mfma_f32_16x16x32_bf16 v[4:7], a[100:103], a[76:79], v[4:7]\n" \
  "ds_read_b64_tr_b16 a[34:35], v226 offset:1024\n" \
  "ds_read_b64_tr_b16 a[36:37], v226 offset:8192\n" \
  "v_mfma_f32_16x16x32_bf16 v[4:7], a[96:99], a[72:75], v[4:7]\n" \
  "s_add_u32 m0, m0, s82\n" \
  "ds_read_b64_tr_b16 a[38:39], v226 offset:9216\n" \
  "v_mfma_f32_16x16x32_bf16 v[8:11], a[96:99], a[80:83], v[8:11]\n" \
  "buffer_load_dwordx4 v235, s[32:35], s97 offen lds\n" \
  "v_mfma_f32_16x16x32_bf16 v[8:11], a[100:103], a[84:87], v[8:11]\n" \
  "ds_read_b128 a[0:3], v224 offset:0\n" \
  "ds_read_b128 a[4:7], v224 offset:1024\n" \
  "v_mfma_f32_16x16x32_bf16 v[12:15], a[100:103], a[92:95], v[12:15]\n" \
  "ds_read_b128 a[8:11], v224 offset:2048\n" \
  "ds_read_b128 a[12:15], v224 offset:3072\n" \
  "v_mfma_f32_16x16x32_bf16 v[12:15], a[96:99], a[88:91], v[12:15]\n" \
  "s_add_u32 m0, m0, s82\n" \
  "ds_read_b128 a[16:19], v224 offset:4096\n" \
  "v_mfma_f32_16x16x32_bf16 v[16:19], a[104:107], a[64:67], v[16:19]\n" \
  "buffer_load_dwordx4 v235, s[32:35], s98 offen lds\n" \
  "v_mfma_f32_16x16x32_bf16 v[16:19], a[108:111], a[68:71], v[16:19]\n" \
  "ds_read_b128 a[20:23], v224 offset:5120\n" \
  "ds_read_b128 a[24:27], v224 offset:6144\n" \
  "v_mfma_f32_16x16x32_bf16 v[20:23], a[108:111], a[76:79], v[20:23]\n" \
  "s_add_u32 m0, m0, s82\n" \
  "ds_read_b128 a[28:31], v224 offset:7168\n" \
  "v_mfma_f32_16x16x32_bf16 v[20:23], a[104:107], a[72:75], v[20:23]\n" \
  "buffer_load_dwordx4 v235, s[32:35], s99 offen lds\n" \
  "v_mfma_f32_16x16x32_bf16 v[24:27], a[104:107], a[80:83], v[24:27]\n" \
  "ds_read_b64_tr_b16 a[40:41], v227 offset:0\n" \
  "ds_read_b64_tr_b16 a[42:43], v227 offset:1024\n" \
  "v_mfma_f32_16x16x32_bf16 v[24:27], a[108:111], a[84:87], v[24:27]\n" \
  "ds_read_b64_tr_b16 a[44:45], v227 offset:8192\n" \
  "buffer_store_dwordx4 v[192:195], v220, s[36:39], s69 offen offset:1024 sc1\n" \
  "v_mfma_f32_16x16x32_bf16 v[28:31], a[108:111], a[92:95], v[28:31]\n" \
  "s_add_u32 m0, s92, 65536\n" \
  "ds_read_b64_tr_b16 a[46:47], v227 offset:9216\n" \
  "v_cvt_pk_bf16_f32 v196, v104, v105\n" \
  "v_cvt_pk_bf16_f32 v197, v106, v107\n" \
  "v_mfma_f32_16x16x32_bf16 v[28:31], a[104:107], a[88:91], v[28:31]\n" \
  "buffer_load_dwordx4 v234, s[28:31], 0 offen sc1 lds\n" \
  "v_cvt_pk_bf16_f32 v198, v108, v109\n" \
  "v_mfma_f32_16x16x32_bf16 v[32:35], a[112:115], a[64:67], v[32:35]\n" \
  "ds_read_b64_tr_b16 a[48:49], v228 offset:0\n" \
  "v_cvt_pk_bf16_f32 v199, v110, v111\n" \
  "s_mov_b32 s69, 4096\n" \
  "v_mfma_f32_16x16x32_bf16 v[32:35], a[116:119], a[68:71], v[32:35]\n" \
  "ds_read_b64_tr_b16 a[50:51], v228 offset:1024\n" \
  "buffer_store_dwordx4 v[196:199], v220, s[36:39], s69 offen offset:1024 sc1\n" \
  "v_mfma_f32_16x16x32_bf16 v[36:39], a[116:119], a[76:79], v[36:39]\n" \
  "buffer_load_dwordx4 v234, s[28:31], 0 offen offset:1024 sc1 lds\n" \
  "v_cvt_pk_bf16_f32 v200, -v120, -v121\n" \
  "v_mfma_f32_16x16x32_bf16 v[36:39], a[112:115], a[72:75], v[36:39]\n" \
  "ds_read_b64_tr_b16 a[52:53], v228 offset:8192\n" \
  "v_cvt_pk_bf16_f32 v201, -v122, -v123\n" \
  "v_cvt_pk_bf16_f32 v202, -v124, -v125\n" \
  "v_mfma_f32_16x16x32_bf16 v[40:43], a[112:115], a[80:83], v[40:43]\n" \
  "ds_read_b64_tr_b16 a[54:55], v228 offset:9216\n" \
  "v_cvt_pk_bf16_f32 v203, -v126, -v127\n" \
  "v_mfma_f32_16x16x32_bf16 v[40:43], a[116:119], a[84:87], v[40:43]\n" \
  "s_add_u32 m0, m0, 2048\n" \
  "ds_read_b64_tr_b16 a[56:57], v229 offset:0\n" \
  "s_mov_b32 s69, 20480\n" \
  "v_mfma_f32_16x16x32_bf16 v[44:47], a[116:119], a[92:95], v[44:47]\n" \
  "buffer_load_dwordx4 v234, s[28:31], s85 offen sc1 lds\n" \
  "v_mfma_f32_16x16x32_bf16 v[44:47], a[112:115], a[88:91], v[44:47]\n" \
  "ds_read_b64_tr_b16 a[58:59], v229 offset:1024\n" \
  "buffer_store_dwordx4 v[200:203], v220, s[36:39], s69 offen offset:1024 sc1\n" \
  "v_mfma_f32_16x16x32_bf16 v[48:51], a[120:123], a[64:67], v[48:51]\n" \
  "ds_read_b64_tr_b16 a[60:61], v229 offset:8192\n" \
  "v_accvgpr_read_b32 v164, a199\n" \
  "v_add_u32 v168, 65536, v164\n" \
  "v_accvgpr_read_b32 v165, a200\n" \
  "v_mfma_f32_16x16x32_bf16 v[48:51], a[124:127], a[68:71], v[48:51]\n" \
  "buffer_load_dwordx4 v234, s[28:31], s85 offen offset:1024 sc1 lds\n" \
  "v_add_u32 v169, 65536, v165\n" \
  "v_mfma_f32_16x16x32_bf16 v[52:55], a[124:127], a[76:79], v[52:55]\n" \
  "ds_read_b64_tr_b16 a[62:63], v229 offset:9216\n" \
  "v_accvgpr_read_b32 v166, a201\n" \
  "v_add_u32 v170, 65536, v166\n" \
  "v_mfma_f32_16x16x32_bf16 v[52:55], a[120:123], a[72:75], v[52:55]\n" \
  "v_accvgpr_read_b32 v167, a202\n" \
  "v_mfma_f32_16x16x32_bf16 v[56:59], a[120:123], a[80:83], v[56:59]\n" \
  "s_add_u32 s28, s28, 2048\n" \
  "s_addc_u32 s29, s29, 0\n" \
  "v_add_u32 v171, 65536, v167\n" \
  "v_accvgpr_read_b32 v250, a222\n" \
  "v_mfma_f32_16x16x32_bf16 v[56:59], a[124:127], a[84:87], v[56:59]\n" \
  "v_accvgpr_read_b32 v144, a223\n" \
  "v_mfma_f32_16x16x32_bf16 v[60:63], a[124:127], a[92:95], v[60:63]\n" \
  "s_sub_u32 s32, s32, 3211264\n" \
  "s_subb_u32 s33, s33, 0\n" \
  "v_xor_b32 v145, 32, v144\n" \
  "v_xor_b32 v146, 64, v144\n" \
  "v_mfma_f32_16x16x32_bf16 v[60:63], a[120:123], a[88:91], v[60:63]\n" \
  "s_add_u32 s96, s95, 196608\n" \
  "s_add_u32 s97, s95, 425984\n" \
  "s_add_u32 s98, s95, 655360\n" \
  "s_add_u32 s99, s95, 884736\n" \
  "s_waitcnt lgkmcnt(0)\n" \
  "s_waitcnt vmcnt(20)\n" \
  "s_barrier\n" \
  "v_mfma_f32_16x16x32_bf16 v[0:3], a[32:35], a[0:3], v[0:3]\n" \
  "s_add_u32 m0, s93, 0\n" \
  "ds_read_b64_tr_b16 a[96:97], v230 offset:32768\n" \
  "v_xor_b32 v147, 96, v144\n" \
  "v_accvgpr_read_b32 v160, a217\n" \
  "v_mfma_f32_16x16x32_bf16 v[0:3], a[36:39], a[4:7], v[0:3]\n" \
  "buffer_load_dwordx4 v235, s[32:35], s96 offen lds\n" \
  "v_accvgpr_read_b32 v161, a219\n" \
  "v_mfma_f32_16x16x32_bf16 v[4:7], a[36:39], a[12:15], v[4:7]\n" \
  "ds_read_b64_tr_b16 a[98:99], v230 offset:33792\n" \
  "ds_read_b64_tr_b16 a[100:101], v230 offset:40960\n" \
  "v_accvgpr_read_b32 v148, a248\n" \
  "v_lshlrev_b32 v96, 16, v148\n" \
  "v_mfma_f32_16x16x32_bf16 v[4:7], a[32:35], a[8:11], v[4:7]\n" \
  "s_add_u32 m0, m0, s82\n" \
  "ds_read_b64_tr_b16 a[102:103], v230 offset:41984\n" \
  "v_and_b32 v97, 0xffff0000, v148\n" \
  "v_accvgpr_read_b32 v148, a249\n" \
  "v_mfma_f32_16x16x32_bf16 v[8:11], a[32:35], a[16:19], v[8:11]\n" \
  "buffer_load_dwordx4 v235, s[32:35], s97 offen lds\n" \
  "v_lshlrev_b32 v98, 16, v148\n" \
  "v_mfma_f32_16x16x32_bf16 v[8:11], a[36:39], a[20:23], v[8:11]\n" \
  "ds_read_b128 a[64:67], v225 offset:32768\n" \
  "ds_read_b128 a[68:71], v225 offset:33792\n" \
  "v_and_b32 v99, 0xffff0000, v148\n" \
  "v_accvgpr_read_b32 v148, a250\n" \
  "v_mfma_f32_16x16x32_bf16 v[12:15], a[36:39], a[28:31], v[12:15]\n" \
  "ds_read_b128 a[72:75], v225 offset:34816\n" \
  "ds_read_b128 a[76:79], v225 offset:35840\n" \
  "v_lshlrev_b32 v100, 16, v148\n" \
  "v_and_b32 v101, 0xffff0000, v148\n" \
  "v_mfma_f32_16x16x32_bf16 v[12:15], a[32:35], a[24:27], v[12:15]\n" \
  "s_add_u32 m0, m0, s82\n" \
  "ds_read_b128 a[80:83], v225 offset:36864\n" \
  "v_accvgpr_read_b32 v148, a251\n" \
  "v_mfma_f32_16x16x32_bf16 v[16:19], a[40:43], a[0:3], v[16:19]\n" \
  "buffer_load_dwordx4 v235, s[32:35], s98 offen lds\n" \
  "v_lshlrev_b32 v102, 16, v148\n" \
  "v_mfma_f32_16x16x32_bf16 v[16:19], a[44:47], a[4:7], v[16:19]\n" \
  "ds_read_b128 a[84:87], v225 offset:37888\n" \
  "ds_read_b128 a[88:91], v225 offset:38912\n" \
  "v_and_b32 v103, 0xffff0000, v148\n" \
  "v_accvgpr_read_b32 v148, a252\n" \
  "v_mfma_f32_16x16x32_bf16 v[20:23], a[44:47], a[12:15], v[20:23]\n" \
  "s_add_u32 m0, m0, s82\n" \
  "ds_read_b128 a[92:95], v225 offset:39936\n" \
  "v_lshlrev_b32 v104, 16, v148\n" \
  "v_and_b32 v105, 0xffff0000, v148\n" \
  "v_mfma_f32_16x16x32_bf16 v[20:23], a[40:43], a[8:11], v[20:23]\n" \
  "buffer_load_dwordx4 v235, s[32:35], s99 offen lds\n" \
  "v_accvgpr_read_b32 v148, a253\n" \
  "v_mfma_f32_16x16x32_bf16 v[24:27], a[40:43], a[16:19], v[24:27]\n" \
  "ds_read_b64_tr_b16 a[104:105], v231 offset:32768\n" \
  "ds_read_b64_tr_b16 a[106:107], v231 offset:33792\n" \
  "v_lshlrev_b32 v106, 16, v148\n" \
  "v_and_b32 v107, 0xffff0000, v148\n" \
  "v_mfma_f32_16x16x32_bf16 v[24:27], a[44:47], a[20:23], v[24:27]\n" \
  "ds_read_b64_tr_b16 a[108:109], v231 offset:40960\n" \
  "v_accvgpr_read_b32 v148, a254\n" \
  "v_mfma_f32_16x16x32_bf16 v[28:31], a[44:47], a[28:31], v[28:31]\n" \
  "s_add_u32 m0, s92, 0\n" \
  "ds_read_b64_tr_b16 a[110:111], v231 offset:41984\n" \
  "v_lshlrev_b32 v108, 16, v148\n" \
  "v_and_b32 v109, 0xffff0000, v148\n" \
  "v_mfma_f32_16x16x32_bf16 v[28:31], a[40:43], a[24:27], v[28:31]\n" \
  "buffer_load_dwordx4 v234, s[28:31], 0 offen sc1 lds\n" \
  "v_accvgpr_read_b32 v148, a255\n" \
  "v_mfma_f32_16x16x32_bf16 v[32:35], a[48:51], a[0:3], v[32:35]\n" \
  "ds_read_b64_tr_b16 a[112:113], v232 offset:32768\n" \
  "v_lshlrev_b32 v110, 16, v148\n" \
  "v_and_b32 v111, 0xffff0000, v148\n" \
  "v_mfma_f32_16x16x32_bf16 v[32:35], a[52:55], a[4:7], v[32:35]\n" \
  "ds_read_b64_tr_b16 a[114:115], v232 offset:33792\n" \
  "v_mfma_f32_16x16x32_bf16 v[36:39], a[52:55], a[12:15], v[36:39]\n" \
  "buffer_load_dwordx4 v234, s[28:31], 0 offen offset:1024 sc1 lds\n" \
  "v_mfma_f32_16x16x32_bf16 v[36:39], a[48:51], a[8:11], v[36:39]\n" \
  "ds_read_b64_tr_b16 a[116:117], v232 offset:40960\n" \
  "v_mfma_f32_16x16x32_bf16 v[40:43], a[48:51], a[16:19], v[40:43]\n" \
  "ds_read_b64_tr_b16 a[118:119], v232 offset:41984\n" \
  "v_mfma_f32_16x16x32_bf16 v[40:43], a[52:55], a[20:23], v[40:43]\n" \
  "s_add_u32 m0, m0, 2048\n" \
  "ds_read_b64_tr_b16 a[120:121], v233 offset:32768\n" \
  "v_mfma_f32_16x16x32_bf16 v[44:47], a[52:55], a[28:31], v[44:47]\n" \
  "buffer_load_dwordx4 v234, s[28:31], s85 offen sc1 lds\n" \
  "v_mfma_f32_16x16x32_bf16 v[44:47], a[48:51], a[24:27], v[44:47]\n" \
  "ds_read_b64_tr_b16 a[122:123], v233 offset:33792\n" \
  "v_mfma_f32_16x16x32_bf16 v[48:51], a[56:59], a[0:3], v[48:51]\n" \
  "ds_read_b64_tr_b16 a[124:125], v233 offset:40960\n" \
  "v_mfma_f32_16x16x32_bf16 v[48:51], a[60:63], a[4:7], v[48:51]\n" \
  "buffer_load_dwordx4 v234, s[28:31], s85 offen offset:1024 sc1 lds\n" \
  "v_mfma_f32_16x16x32_bf16 v[52:55], a[60:63], a[12:15], v[52:55]\n" \
  "ds_read_b64_tr_b16 a[126:127], v233 offset:41984\n" \
  "v_mfma_f32_16x16x32_bf16 v[52:55], a[56:59], a[8:11], v[52:55]\n" \
  "v_mfma_f32_16x16x32_bf16 v[56:59], a[56:59], a[16:19], v[56:59]\n" \
  "s_add_u32 s28, s28, 2048\n" \
  "s_addc_u32 s29, s29, 0\n" \
  "v_mfma_f32_16x16x32_bf16 v[56:59], a[60:63], a[20:23], v[56:59]\n" \
  "v_mfma_f32_16x16x32_bf16 v[60:63], a[60:63], a[28:31], v[60:63]\n" \
  "s_add_u32 s32, s32, 917504\n" \
  "s_addc_u32 s33, s33, 0\n" \
  "v_mfma_f32_16x16x32_bf16 v[60:63], a[56:59], a[24:27], v[60:63]\n" \
  "v_accvgpr_read_b32 v253, a218\n" \
  "buffer_load_dwordx2 v[252:253], v253, s[52:55], 0 offen sc1\n" \
  "s_waitcnt lgkmcnt(0)\n" \
  "s_waitcnt vmcnt(21)\n" \
  "s_barrier\n" \
  "v_mfma_f32_16x16x32_bf16 v[0:3], a[96:99], a[64:67], v[0:3]\n" \
  "s_add_u32 m0, s93, 98304\n" \
  "ds_read_b64_tr_b16 a[32:33], v226 offset:32768\n" \
  "v_mfma_f32_16x16x32_bf16 v[0:3], a[100:103], a[68:71], v[0:3]\n" \
  "buffer_load_dwordx4 v235, s[32:35], s96 offen lds\n" \
  "v_mfma_f32_16x16x32_bf16 v[4:7], a[100:103], a[76:79], v[4:7]\n" \
  "ds_read_b64_tr_b16 a[34:35], v226 offset:33792\n" \
  "ds_read_b64_tr_b16 a[36:37], v226 offset:40960\n" \
  "s_waitcnt vmcnt(11)\n" \
  "buffer_store_dwordx2 v[254:255], v237, s[52:55], s2 offen sc1\n" \
  "v_mfma_f32_16x16x32_bf16 v[4:7], a[96:99], a[72:75], v[4:7]\n" \
  "s_add_u32 m0, m0, s82\n" \
  "ds_read_b64_tr_b16 a[38:39], v226 offset:41984\n" \
  "v_mfma_f32_16x16x32_bf16 v[8:11], a[96:99], a[80:83], v[8:11]\n" \
  "buffer_load_dwordx4 v235, s[32:35], s97 offen lds\n" \
  "v_mfma_f32_16x16x32_bf16 v[8:11], a[100:103], a[84:87], v[8:11]\n" \
  "ds_read_b128 a[0:3], v224 offset:32768\n" \
  "ds_read_b128 a[4:7], v224 offset:33792\n" \
  "v_mfma_f32_16x16x32_bf16 v[12:15], a[100:103], a[92:95], v[12:15]\n" \
  "ds_read_b128 a[8:11], v224 offset:34816\n" \
  "ds_read_b128 a[12:15], v224 offset:35840\n" \
  "v_mfma_f32_16x16x32_bf16 v[12:15], a[96:99], a[88:91], v[12:15]\n" \
  "s_add_u32 m0, m0, s82\n" \
  "ds_read_b128 a[16:19], v224 offset:36864\n" \
  "v_mfma_f32_16x16x32_bf16 v[16:19], a[104:107], a[64:67], v[16:19]\n" \
  "buffer_load_dwordx4 v235, s[32:35], s98 offen lds\n" \
  "v_mfma_f32_16x16x32_bf16 v[16:19], a[108:111], a[68:71], v[16:19]\n" \
  "ds_read_b128 a[20:23], v224 offset:37888\n" \
  "ds_read_b128 a[24:27], v224 offset:38912\n" \
  "v_mfma_f32_16x16x32_bf16 v[20:23], a[108:111], a[76:79], v[20:23]\n" \
  "s_add_u32 m0, m0, s82\n" \
  "ds_read_b128 a[28:31], v224 offset:39936\n" \
  "v_mfma_f32_16x16x32_bf16 v[20:23], a[104:107], a[72:75], v[20:23]\n" \
  "buffer_load_dwordx4 v235, s[32:35], s99 offen lds\n" \
  "v_mfma_f32_16x16x32_bf16 v[24:27], a[104:107], a[80:83], v[24:27]\n" \
  "ds_read_b64_tr_b16 a[40:41], v227 offset:32768\n" \
  "ds_read_b64_tr_b16 a[42:43], v227 offset:33792\n" \
  "v_mfma_f32_16x16x32_bf16 v[24:27], a[108:111], a[84:87], v[24:27]\n" \
  "ds_read_b64_tr_b16 a[44:45], v227 offset:40960\n" \
  "v_mfma_f32_16x16x32_bf16 v[28:31], a[108:111], a[92:95], v[28:31]\n" \
  "s_add_u32 m0, s92, 98304\n" \
  "ds_read_b64_tr_b16 a[46:47], v227 offset:41984\n" \
  "v_mfma_f32_16x16x32_bf16 v[28:31], a[104:107], a[88:91], v[28:31]\n" \
  "buffer_load_dwordx4 v234, s[28:31], 0 offen sc1 lds\n" \
  "v_mfma_f32_16x16x32_bf16 v[32:35], a[112:115], a[64:67], v[32:35]\n" \
  "ds_read_b64_tr_b16 a[48:49], v228 offset:32768\n" \
  "v_mfma_f32_16x16x32_bf16 v[32:35], a[116:119], a[68:71], v[32:35]\n" \
  "ds_read_b64_tr_b16 a[50:51], v228 offset:33792\n" \
  "v_mfma_f32_16x16x32_bf16 v[36:39], a[116:119], a[76:79], v[36:39]\n" \
  "buffer_load_dwordx4 v234, s[28:31], 0 offen offset:1024 sc1 lds\n" \
  "v_mfma_f32_16x16x32_bf16 v[36:39], a[112:115], a[72:75], v[36:39]\n" \
  "ds_read_b64_tr_b16 a[52:53], v228 offset:40960\n" \
  "v_mfma_f32_16x16x32_bf16 v[40:43], a[112:115], a[80:83], v[40:43]\n" \
  "ds_read_b64_tr_b16 a[54:55], v228 offset:41984\n" \
  "v_mfma_f32_16x16x32_bf16 v[40:43], a[116:119], a[84:87], v[40:43]\n" \
  "s_add_u32 m0, m0, 2048\n" \
  "ds_read_b64_tr_b16 a[56:57], v229 offset:32768\n" \
  "v_mfma_f32_16x16x32_bf16 v[44:47], a[116:119], a[92:95], v[44:47]\n" \
  "buffer_load_dwordx4 v234, s[28:31], s85 offen sc1 lds\n" \
  "v_mfma_f32_16x16x32_bf16 v[44:47], a[112:115], a[88:91], v[44:47]\n" \
  "ds_read_b64_tr_b16 a[58:59], v229 offset:33792\n" \
  "v_mfma_f32_16x16x32_bf16 v[48:51], a[120:123], a[64:67], v[48:51]\n" \
  "ds_read_b64_tr_b16 a[60:61], v229 offset:40960\n" \
  "v_mfma_f32_16x16x32_bf16 v[48:51], a[124:127], a[68:71], v[48:51]\n" \
  "buffer_load_dwordx4 v234, s[28:31], s85 offen offset:1024 sc1 lds\n" \
  "v_mfma_f32_16x16x32_bf16 v[52:55], a[124:127], a[76:79], v[52:55]\n" \
  "ds_read_b64_tr_b16 a[62:63], v229 offset:41984\n" \
  "v_mfma_f32_16x16x32_bf16 v[52:55], a[120:123], a[72:75], v[52:55]\n" \
  "v_mfma_f32_16x16x32_bf16 v[56:59], a[120:123], a[80:83], v[56:59]\n" \
  "s_add_u32 s28, s28, 2048\n" \
  "s_addc_u32 s29, s29, 0\n" \
  "v_mfma_f32_16x16x32_bf16 v[56:59], a[124:127], a[84:87], v[56:59]\n" \
  "v_mfma_f32_16x16x32_bf16 v[60:63], a[124:127], a[92:95], v[60:63]\n" \
  "s_add_u32 s32, s32, 917504\n" \
  "s_addc_u32 s33, s33, 0\n" \
  "v_mfma_f32_16x16x32_bf16 v[60:63], a[120:123], a[88:91], v[60:63]\n" \
  "s_waitcnt lgkmcnt(0)\n" \
  "s_waitcnt vmcnt(18)\n" \
  "s_barrier\n" \
  "s_waitcnt vmcnt(9)\n" \
  "v_xor_b32 v252, v252, v254\n" \
  "v_xor_b32 v253, v253, v255\n" \
  "v_or_b32 v252, v252, v253\n" \
  "v_cmp_ne_u32 vcc, 0, v252\n" \
  "s_nop 4\n" \
  "s_cbranch_vccnz Lslow4_%=\n" \
  "Lres4_%=:\n" \
  "v_mfma_f32_16x16x32_bf16 v[0:3], a[32:35], a[0:3], v[0:3]\n" \
  "s_add_u32 m0, s93, 32768\n" \
  "ds_read_b64_tr_b16 a[96:97], v230 offset:0\n" \
  "v_mfma_f32_16x16x32_bf16 v[0:3], a[36:39], a[4:7], v[0:3]\n" \
  "buffer_load_dwordx4 v235, s[32:35], s96 offen lds\n" \
  "v_mfma_f32_16x16x32_bf16 v[4:7], a[36:39], a[12:15], v[4:7]\n" \
  "ds_read_b64_tr_b16 a[98:99], v230 offset:1024\n" \
  "ds_read_b64_tr_b16 a[100:101], v230 offset:8192\n" \
  "v_mfma_f32_16x16x32_bf16 v[4:7], a[32:35], a[8:11], v[4:7]\n" \
  "s_add_u32 m0, m0, s82\n" \
  "ds_read_b64_tr_b16 a[102:103], v230 offset:9216\n" \
  "v_mfma_f32_16x16x32_bf16 v[8:11], a[32:35], a[16:19], v[8:11]\n" \
  "buffer_load_dwordx4 v235, s[32:35], s97 offen lds\n" \
  "v_mfma_f32_16x16x32_bf16 v[8:11], a[36:39], a[20:23], v[8:11]\n" \
  "ds_read_b128 a[64:67], v225 offset:0\n" \
  "ds_read_b128 a[68:71], v225 offset:1024\n" \
  "v_mfma_f32_16x16x32_bf16 v[12:15], a[36:39], a[28:31], v[12:15]\n" \
  "ds_read_b128 a[72:75], v225 offset:2048\n" \
  "ds_read_b128 a[76:79], v225 offset:3072\n" \
  "v_mfma_f32_16x16x32_bf16 v[12:15], a[32:35], a[24:27], v[12:15]\n" \
  "s_add_u32 m0, m0, s82\n" \
  "ds_read_b128 a[80:83], v225 offset:4096\n" \
  "v_mfma_f32_16x16x32_bf16 v[16:19], a[40:43], a[0:3], v[16:19]\n" \
  "buffer_load_dwordx4 v235, s[32:35], s98 offen lds\n" \
  "v_mfma_f32_16x16x32_bf16 v[16:19], a[44:47], a[4:7], v[16:19]\n" \
  "ds_read_b128 a[84:87], v225 offset:5120\n" \
  "ds_read_b128 a[88:91], v225 offset:6144\n" \
  "v_mfma_f32_16x16x32_bf16 v[20:23], a[44:47], a[12:15], v[20:23]\n" \
  "s_add_u32 m0, m0, s82\n" \
  "ds_read_b128 a[92:95], v225 offset:7168\n" \
  "v_mfma_f32_16x16x32_bf16 v[20:23], a[40:43], a[8:11], v[20:23]\n" \
  "buffer_load_dwordx4 v235, s[32:35], s99 offen lds\n" \
  "v_mfma_f32_16x16x32_bf16 v[24:27], a[40:43], a[16:19], v[24:27]\n" \
  "ds_read_b64_tr_b16 a[104:105], v231 offset:0\n" \
  "ds_read_b64_tr_b16 a[106:107], v231 offset:1024\n" \
  "v_mfma_f32_16x16x32_bf16 v[24:27], a[44:47], a[20:23], v[24:27]\n" \
  "ds_read_b64_tr_b16 a[108:109], v231 offset:8192\n" \
  "v_mfma_f32_16x16x32_bf16 v[28:31], a[44:47], a[28:31], v[28:31]\n" \
  "s_add_u32 m0, s92, 32768\n" \
  "ds_read_b64_tr_b16 a[110:111], v231 offset:9216\n" \
  "v_mfma_f32_16x16x32_bf16 v[28:31], a[40:43], a[24:27], v[28:31]\n" \
  "buffer_load_dwordx4 v234, s[28:31], 0 offen sc1 lds\n" \
  "v_mfma_f32_16x16x32_bf16 v[32:35], a[48:51], a[0:3], v[32:35]\n" \
  "ds_read_b64_tr_b16 a[112:113], v232 offset:0\n" \
  "v_mfma_f32_16x16x32_bf16 v[32:35], a[52:55], a[4:7], v[32:35]\n" \
  "ds_read_b64_tr_b16 a[114:115], v232 offset:1024\n" \
  "v_mfma_f32_16x16x32_bf16 v[36:39], a[52:55], a[12:15], v[36:39]\n" \
  "buffer_load_dwordx4 v234, s[28:31], 0 offen offset:1024 sc1 lds\n" \
  "v_mfma_f32_16x16x32_bf16 v[36:39], a[48:51], a[8:11], v[36:39]\n" \
  "ds_read_b64_tr_b16 a[116:117], v232 offset:8192\n" \
  "v_mfma_f32_16x16x32_bf16 v[40:43], a[48:51], a[16:19], v[40:43]\n" \
  "ds_read_b64_tr_b16 a[118:119], v232 offset:9216\n" \
  "v_mfma_f32_16x16x32_bf16 v[40:43], a[52:55], a[20:23], v[40:43]\n" \
  "s_add_u32 m0, m0, 2048\n" \
  "ds_read_b64_tr_b16 a[120:121], v233 offset:0\n" \
  "v_mfma_f32_16x16x32_bf16 v[44:47], a[52:55], a[28:31], v[44:47]\n" \
  "buffer_load_dwordx4 v234, s[28:31], s85 offen sc1 lds\n" \
  "v_mfma_f32_16x16x32_bf16 v[44:47], a[48:51], a[24:27], v[44:47]\n" \
  "ds_read_b64_tr_b16 a[122:123], v233 offset:1024\n" \
  "v_mfma_f32_16x16x32_bf16 v[48:51], a[56:59], a[0:3], v[48:51]\n" \
  "ds_read_b64_tr_b16 a[124:125], v233 offset:8192\n" \
  "v_mfma_f32_16x16x32_bf16 v[48:51], a[60:63], a[4:7], v[48:51]\n" \
  "buffer_load_dwordx4 v234, s[28:31], s85 offen offset:1024 sc1 lds\n" \
  "v_mfma_f32_16x16x32_bf16 v[52:55], a[60:63], a[12:15], v[52:55]\n" \
  "ds_read_b64_tr_b16 a[126:127], v233 offset:9216\n" \
  "v_mfma_f32_16x16x32_bf16 v[52:55], a[56:59], a[8:11], v[52:55]\n" \
  "v_mfma_f32_16x16x32_bf16 v[56:59], a[56:59], a[16:19], v[56:59]\n" \
  "s_add_u32 s28, s28, 2048\n" \
  "s_addc_u32 s29, s29, 0\n" \
  "v_mfma_f32_16x16x32_bf16 v[56:59], a[60:63], a[20:23], v[56:59]\n" \
  "v_mfma_f32_16x16x32_bf16 v[60:63], a[60:63], a[28:31], v[60:63]\n" \
  "s_add_u32 s32, s32, 917504\n" \
  "s_addc_u32 s33, s33, 0\n" \
  "v_mfma_f32_16x16x32_bf16 v[60:63], a[56:59], a[24:27], v[60:63]\n" \
  "v_accvgpr_read_b32 v253, a212\n" \
  "buffer_load_dwordx2 v[252:253], v253, s[52:55], s1 offen sc1\n" \
  "s_waitcnt lgkmcnt(0)\n" \
  "s_barrier\n" \
  "s_cmp_eq_u32 s7, 32768\n" \
  "s_cselect_b32 s68, 0, 0x40000000\n" \
  "buffer_store_dword v254, v237, s[52:55], s68 offen sc1\n" \
  "s_mov_b32 s89, 3\n" \
  "v_mfma_f32_16x16x32_bf16 v[0:3], a[96:99], a[64:67], v[0:3]\n" \
  "s_add_u32 m0, s93, 65536\n" \
  "ds_read_b64_tr_b16 a[32:33], v226 offset:0\n" \
  "v_mfma_f32_16x16x32_bf16 v[0:3], a[100:103], a[68:71], v[0:3]\n" \
  "buffer_load_dwordx4 v235, s[32:35], s96 offen lds\n" \
  "v_mfma_f32_16x16x32_bf16 v[4:7], a[100:103], a[76:79], v[4:7]\n" \
  "ds_read_b64_tr_b16 a[34:35], v226 offset:1024\n" \
  "ds_read_b64_tr_b16 a[36:37], v226 offset:8192\n" \
  "v_mfma_f32_16x16x32_bf16 v[4:7], a[96:99], a[72:75], v[4:7]\n" \
  "s_add_u32 m0, m0, s82\n" \
  "ds_read_b64_tr_b16 a[38:39], v226 offset:9216\n" \
  "v_mfma_f32_16x16x32_bf16 v[8:11], a[96:99], a[80:83], v[8:11]\n" \
  "buffer_load_dwordx4 v235, s[32:35], s97 offen lds\n" \
  "v_mfma_f32_16x16x32_bf16 v[8:11], a[100:103], a[84:87], v[8:11]\n" \
  "ds_read_b128 a[0:3], v224 offset:0\n" \
  "ds_read_b128 a[4:7], v224 offset:1024\n" \
  "v_mfma_f32_16x16x32_bf16 v[12:15], a[100:103], a[92:95], v[12:15]\n" \
  "ds_read_b128 a[8:11], v224 offset:2048\n" \
  "ds_read_b128 a[12:15], v224 offset:3072\n" \
  "v_mfma_f32_16x16x32_bf16 v[12:15], a[96:99], a[88:91], v[12:15]\n" \
  "s_add_u32 m0, m0, s82\n" \
  "ds_read_b128 a[16:19], v224 offset:4096\n" \
  "v_mfma_f32_16x16x32_bf16 v[16:19], a[104:107], a[64:67], v[16:19]\n" \
  "buffer_load_dwordx4 v235, s[32:35], s98 offen lds\n" \
  "v_mfma_f32_16x16x32_bf16 v[16:19], a[108:111], a[68:71], v[16:19]\n" \
  "ds_read_b128 a[20:23], v224 offset:5120\n" \
  "ds_read_b128 a[24:27], v224 offset:6144\n" \
  "v_mfma_f32_16x16x32_bf16 v[20:23], a[108:111], a[76:79], v[20:23]\n" \
  "s_add_u32 m0, m0, s82\n" \
  "ds_read_b128 a[28:31], v224 offset:7168\n" \
  "v_mfma_f32_16x16x32_bf16 v[20:23], a[104:107], a[72:75], v[20:23]\n" \
  "buffer_load_dwordx4 v235, s[32:35], s99 offen lds\n" \
  "v_mfma_f32_16x16x32_bf16 v[24:27], a[104:107], a[80:83], v[24:27]\n" \
  "ds_read_b64_tr_b16 a[40:41], v227 offset:0\n" \
  "ds_read_b64_tr_b16 a[42:43], v227 offset:1024\n" \
  "v_mfma_f32_16x16x32_bf16 v[24:27], a[108:111], a[84:87], v[24:27]\n" \
  "ds_read_b64_tr_b16 a[44:45], v227 offset:8192\n" \
  "v_mfma_f32_16x16x32_bf16 v[28:31], a[108:111], a[92:95], v[28:31]\n" \
  "s_add_u32 m0, s92, 65536\n" \
  "ds_read_b64_tr_b16 a[46:47], v227 offset:9216\n" \
  "v_mfma_f32_16x16x32_bf16 v[28:31], a[104:107], a[88:91], v[28:31]\n" \
  "buffer_load_dwordx4 v234, s[28:31], 0 offen sc1 lds\n" \
  "v_mfma_f32_16x16x32_bf16 v[32:35], a[112:115], a[64:67], v[32:35]\n" \
  "ds_read_b64_tr_b16 a[48:49], v228 offset:0\n" \
  "v_mfma_f32_16x16x32_bf16 v[32:35], a[116:119], a[68:71], v[32:35]\n" \
  "ds_read_b64_tr_b16 a[50:51], v228 offset:1024\n" \
  "v_mfma_f32_16x16x32_bf16 v[36:39], a[116:119], a[76:79], v[36:39]\n" \
  "buffer_load_dwordx4 v234, s[28:31], 0 offen offset:1024 sc1 lds\n" \
  "v_mfma_f32_16x16x32_bf16 v[36:39], a[112:115], a[72:75], v[36:39]\n" \
  "ds_read_b64_tr_b16 a[52:53], v228 offset:8192\n" \
  "v_mfma_f32_16x16x32_bf16 v[40:43], a[112:115], a[80:83], v[40:43]\n" \
  "ds_read_b64_tr_b16 a[54:55], v228 offset:9216\n" \
  "v_mfma_f32_16x16x32_bf16 v[40:43], a[116:119], a[84:87], v[40:43]\n" \
  "s_add_u32 m0, m0, 2048\n" \
  "ds_read_b64_tr_b16 a[56:57], v229 offset:0\n" \
  "v_mfma_f32_16x16x32_bf16 v[44:47], a[116:119], a[92:95], v[44:47]\n" \
  "buffer_load_dwordx4 v234, s[28:31], s85 offen sc1 lds\n" \
  "v_mfma_f32_16x16x32_bf16 v[44:47], a[112:115], a[88:91], v[44:47]\n" \
  "ds_read_b64_tr_b16 a[58:59], v229 offset:1024\n" \
  "v_mfma_f32_16x16x32_bf16 v[48:51], a[120:123], a[64:67], v[48:51]\n" \
  "ds_read_b64_tr_b16 a[60:61], v229 offset:8192\n" \
  "v_mfma_f32_16x16x32_bf16 v[48:51], a[124:127], a[68:71], v[48:51]\n" \
  "buffer_load_dwordx4 v234, s[28:31], s85 offen offset:1024 sc1 lds\n" \
  "v_mfma_f32_16x16x32_bf16 v[52:55], a[124:127], a[76:79], v[52:55]\n" \
  "ds_read_b64_tr_b16 a[62:63], v229 offset:9216\n" \
  "v_mfma_f32_16x16x32_bf16 v[52:55], a[120:123], a[72:75], v[52:55]\n" \
  "v_mfma_f32_16x16x32_bf16 v[56:59], a[120:123], a[80:83], v[56:59]\n" \
  "s_add_u32 s28, s28, 2048\n" \
  "s_addc_u32 s29, s29, 0\n" \
  "v_mfma_f32_16x16x32_bf16 v[56:59], a[124:127], a[84:87], v[56:59]\n" \
  "v_mfma_f32_16x16x32_bf16 v[60:63], a[124:127], a[92:95], v[60:63]\n" \
  "v_mfma_f32_16x16x32_bf16 v[60:63], a[120:123], a[88:91], v[60:63]\n" \
  "s_waitcnt lgkmcnt(0)\n" \
  "s_waitcnt vmcnt(18)\n" \
  "s_barrier\n" \
  "s_waitcnt vmcnt(9)\n" \
  "v_xor_b32 v252, v252, v254\n" \
  "v_xor_b32 v253, v253, v255\n" \
  "v_or_b32 v252, v252, v253\n" \
  "v_cmp_ne_u32 vcc, 0, v252\n" \
  "s_nop 4\n" \
  "s_cbranch_vccnz Lslow5_%=\n" \
  "Lres5_%=:\n" \
  "s_add_u32 s1, s1, 8192\n" \
  "v_mfma_f32_16x16x32_bf16 v[0:3], a[32:35], a[0:3], v[0:3]\n" \
  "s_add_u32 m0, s90, 16384\n" \
  "ds_read_b64_tr_b16 a[96:97], v230 offset:32768\n" \
  "v_mfma_f32_16x16x32_bf16 v[0:3], a[36:39], a[4:7], v[0:3]\n" \
  "buffer_load_dwordx4 v249, s[44:47], 0 offen lds\n" \
  "v_mfma_f32_16x16x32_bf16 v[4:7], a[36:39], a[12:15], v[4:7]\n" \
  "ds_read_b64_tr_b16 a[98:99], v230 offset:33792\n" \
  "ds_read_b64_tr_b16 a[100:101], v230 offset:40960\n" \
  "v_mfma_f32_16x16x32_bf16 v[4:7], a[32:35], a[8:11], v[4:7]\n" \
  "s_add_u32 m0, m0, 2048\n" \
  "ds_read_b64_tr_b16 a[102:103], v230 offset:41984\n" \
  "v_mfma_f32_16x16x32_bf16 v[8:11], a[32:35], a[16:19], v[8:11]\n" \
  "buffer_load_dwordx4 v249, s[44:47], s72 offen lds\n" \
  "v_mfma_f32_16x16x32_bf16 v[8:11], a[36:39], a[20:23], v[8:11]\n" \
  "ds_read_b128 a[64:67], v225 offset:32768\n" \
  "ds_read_b128 a[68:71], v225 offset:33792\n" \
  "v_mfma_f32_16x16x32_bf16 v[12:15], a[36:39], a[28:31], v[12:15]\n" \
  "ds_read_b128 a[72:75], v225 offset:34816\n" \
  "ds_read_b128 a[76:79], v225 offset:35840\n" \
  "v_mfma_f32_16x16x32_bf16 v[12:15], a[32:35], a[24:27], v[12:15]\n" \
  "s_add_u32 m0, m0, 2048\n" \
  "ds_read_b128 a[80:83], v225 offset:36864\n" \
  "v_mfma_f32_16x16x32_bf16 v[16:19], a[40:43], a[0:3], v[16:19]\n" \
  "buffer_load_dwordx4 v249, s[44:47], s73 offen lds\n" \
  "v_mfma_f32_16x16x32_bf16 v[16:19], a[44:47], a[4:7], v[16:19]\n" \
  "ds_read_b128 a[84:87], v225 offset:37888\n" \
  "ds_read_b128 a[88:91], v225 offset:38912\n" \
  "v_mfma_f32_16x16x32_bf16 v[20:23], a[44:47], a[12:15], v[20:23]\n" \
  "s_add_u32 m0, m0, 2048\n" \
  "ds_read_b128 a[92:95], v225 offset:39936\n" \
  "v_mfma_f32_16x16x32_bf16 v[20:23], a[40:43], a[8:11], v[20:23]\n" \
  "buffer_load_dwordx4 v249, s[44:47], s74 offen lds\n" \
  "v_mfma_f32_16x16x32_bf16 v[24:27], a[40:43], a[16:19], v[24:27]\n" \
  "ds_read_b64_tr_b16 a[104:105], v231 offset:32768\n" \
  "ds_read_b64_tr_b16 a[106:107], v231 offset:33792\n" \
  "v_mfma_f32_16x16x32_bf16 v[24:27], a[44:47], a[20:23], v[24:27]\n" \
  "ds_read_b64_tr_b16 a[108:109], v231 offset:40960\n" \
  "v_mfma_f32_16x16x32_bf16 v[28:31], a[44:47], a[28:31], v[28:31]\n" \
  "s_add_u32 m0, s88, 0\n" \
  "ds_read_b64_tr_b16 a[110:111], v231 offset:41984\n" \
  "v_mfma_f32_16x16x32_bf16 v[28:31], a[40:43], a[24:27], v[28:31]\n" \
  "buffer_load_dwordx4 v248, s[40:43], 0 offen sc1 lds\n" \
  "v_mfma_f32_16x16x32_bf16 v[32:35], a[48:51], a[0:3], v[32:35]\n" \
  "ds_read_b64_tr_b16 a[112:113], v232 offset:32768\n" \
  "v_mfma_f32_16x16x32_bf16 v[32:35], a[52:55], a[4:7], v[32:35]\n" \
  "s_add_u32 m0, m0, s82\n" \
  "ds_read_b64_tr_b16 a[114:115], v232 offset:33792\n" \
  "v_mfma_f32_16x16x32_bf16 v[36:39], a[52:55], a[12:15], v[36:39]\n" \
  "buffer_load_dwordx4 v248, s[40:43], s82 offen sc1 lds\n" \
  "v_mfma_f32_16x16x32_bf16 v[36:39], a[48:51], a[8:11], v[36:39]\n" \
  "ds_read_b64_tr_b16 a[116:117], v232 offset:40960\n" \
  "v_mfma_f32_16x16x32_bf16 v[40:43], a[48:51], a[16:19], v[40:43]\n" \
  "ds_read_b64_tr_b16 a[118:119], v232 offset:41984\n" \
  "v_mfma_f32_16x16x32_bf16 v[40:43], a[52:55], a[20:23], v[40:43]\n" \
  "s_add_u32 m0, m0, s82\n" \
  "ds_read_b64_tr_b16 a[120:121], v233 offset:32768\n" \
  "v_mfma_f32_16x16x32_bf16 v[44:47], a[52:55], a[28:31], v[44:47]\n" \
  "buffer_load_dwordx4 v248, s[40:43], s83 offen sc1 lds\n" \
  "v_mfma_f32_16x16x32_bf16 v[44:47], a[48:51], a[24:27], v[44:47]\n" \
  "ds_read_b64_tr_b16 a[122:123], v233 offset:33792\n" \
  "v_mfma_f32_16x16x32_bf16 v[48:51], a[56:59], a[0:3], v[48:51]\n" \
  "s_add_u32 m0, m0, s82\n" \
  "ds_read_b64_tr_b16 a[124:125], v233 offset:40960\n" \
  "v_mfma_f32_16x16x32_bf16 v[48:51], a[60:63], a[4:7], v[48:51]\n" \
  "buffer_load_dwordx4 v248, s[40:43], s84 offen sc1 lds\n" \
  "v_mfma_f32_16x16x32_bf16 v[52:55], a[60:63], a[12:15], v[52:55]\n" \
  "ds_read_b64_tr_b16 a[126:127], v233 offset:41984\n" \
  "v_mfma_f32_16x16x32_bf16 v[52:55], a[56:59], a[8:11], v[52:55]\n" \
  "v_mfma_f32_16x16x32_bf16 v[56:59], a[56:59], a[16:19], v[56:59]\n" \
  "s_add_u32 s40, s40, 16384\n" \
  "s_addc_u32 s41, s41, 0\n" \
  "v_mfma_f32_16x16x32_bf16 v[56:59], a[60:63], a[20:23], v[56:59]\n" \
  "v_mfma_f32_16x16x32_bf16 v[60:63], a[60:63], a[28:31], v[60:63]\n" \
  "s_add_u32 s44, s44, 128\n" \
  "s_addc_u32 s45, s45, 0\n" \
  "v_mfma_f32_16x16x32_bf16 v[60:63], a[56:59], a[24:27], v[60:63]\n" \
  "s_waitcnt lgkmcnt(0)\n" \
  "s_barrier\n" \
  "v_mfma_f32_16x16x32_bf16 v[0:3], a[96:99], a[64:67], v[0:3]\n" \
  "s_add_u32 m0, s90, 114688\n" \
  "ds_read_b64_tr_b16 a[32:33], v226 offset:32768\n" \
  "v_mfma_f32_16x16x32_bf16 v[0:3], a[100:103], a[68:71], v[0:3]\n" \
  "buffer_load_dwordx4 v249, s[48:51], 0 offen lds\n" \
  "v_mfma_f32_16x16x32_bf16 v[4:7], a[100:103], a[76:79], v[4:7]\n" \
  "ds_read_b64_tr_b16 a[34:35], v226 offset:33792\n" \
  "ds_read_b64_tr_b16 a[36:37], v226 offset:40960\n" \
  "v_mfma_f32_16x16x32_bf16 v[4:7], a[96:99], a[72:75], v[4:7]\n" \
  "s_add_u32 m0, m0, 2048\n" \
  "ds_read_b64_tr_b16 a[38:39], v226 offset:41984\n" \
  "v_mfma_f32_16x16x32_bf16 v[8:11], a[96:99], a[80:83], v[8:11]\n" \
  "buffer_load_dwordx4 v249, s[48:51], s72 offen lds\n" \
  "v_mfma_f32_16x16x32_bf16 v[8:11], a[100:103], a[84:87], v[8:11]\n" \
  "ds_read_b128 a[0:3], v224 offset:32768\n" \
  "ds_read_b128 a[4:7], v224 offset:33792\n" \
  "v_mfma_f32_16x16x32_bf16 v[12:15], a[100:103], a[92:95], v[12:15]\n" \
  "ds_read_b128 a[8:11], v224 offset:34816\n" \
  "ds_read_b128 a[12:15], v224 offset:35840\n" \
  "v_mfma_f32_16x16x32_bf16 v[12:15], a[96:99], a[88:91], v[12:15]\n" \
  "s_add_u32 m0, m0, 2048\n" \
  "ds_read_b128 a[16:19], v224 offset:36864\n" \
  "v_mfma_f32_16x16x32_bf16 v[16:19], a[104:107], a[64:67], v[16:19]\n" \
  "buffer_load_dwordx4 v249, s[48:51], s73 offen lds\n" \
  "v_mfma_f32_16x16x32_bf16 v[16:19], a[108:111], a[68:71], v[16:19]\n" \
  "ds_read_b128 a[20:23], v224 offset:37888\n" \
  "ds_read_b128 a[24:27], v224 offset:38912\n" \
  "v_mfma_f32_16x16x32_bf16 v[20:23], a[108:111], a[76:79], v[20:23]\n" \
  "s_add_u32 m0, m0, 2048\n" \
  "ds_read_b128 a[28:31], v224 offset:39936\n" \
  "v_mfma_f32_16x16x32_bf16 v[20:23], a[104:107], a[72:75], v[20:23]\n" \
  "buffer_load_dwordx4 v249, s[48:51], s74 offen lds\n" \
  "v_mfma_f32_16x16x32_bf16 v[24:27], a[104:107], a[80:83], v[24:27]\n" \
  "ds_read_b64_tr_b16 a[40:41], v227 offset:32768\n" \
  "ds_read_b64_tr_b16 a[42:43], v227 offset:33792\n" \
  "v_mfma_f32_16x16x32_bf16 v[24:27], a[108:111], a[84:87], v[24:27]\n" \
  "ds_read_b64_tr_b16 a[44:45], v227 offset:40960\n" \
  "v_mfma_f32_16x16x32_bf16 v[28:31], a[108:111], a[92:95], v[28:31]\n" \
  "s_add_u32 m0, s88, 98304\n" \
  "ds_read_b64_tr_b16 a[46:47], v227 offset:41984\n" \
  "v_mfma_f32_16x16x32_bf16 v[28:31], a[104:107], a[88:91], v[28:31]\n" \
  "buffer_load_dwordx4 v248, s[40:43], 0 offen sc1 lds\n" \
  "v_mfma_f32_16x16x32_bf16 v[32:35], a[112:115], a[64:67], v[32:35]\n" \
  "ds_read_b64_tr_b16 a[48:49], v228 offset:32768\n" \
  "v_mfma_f32_16x16x32_bf16 v[32:35], a[116:119], a[68:71], v[32:35]\n" \
  "s_add_u32 m0, m0, s82\n" \
  "ds_read_b64_tr_b16 a[50:51], v228 offset:33792\n" \
  "v_mfma_f32_16x16x32_bf16 v[36:39], a[116:119], a[76:79], v[36:39]\n" \
  "buffer_load_dwordx4 v248, s[40:43], s82 offen sc1 lds\n" \
  "v_mfma_f32_16x16x32_bf16 v[36:39], a[112:115], a[72:75], v[36:39]\n" \
  "ds_read_b64_tr_b16 a[52:53], v228 offset:40960\n" \
  "v_mfma_f32_16x16x32_bf16 v[40:43], a[112:115], a[80:83], v[40:43]\n" \
  "ds_read_b64_tr_b16 a[54:55], v228 offset:41984\n" \
  "v_mfma_f32_16x16x32_bf16 v[40:43], a[116:119], a[84:87], v[40:43]\n" \
  "s_add_u32 m0, m0, s82\n" \
  "ds_read_b64_tr_b16 a[56:57], v229 offset:32768\n" \
  "v_mfma_f32_16x16x32_bf16 v[44:47], a[116:119], a[92:95], v[44:47]\n" \
  "buffer_load_dwordx4 v248, s[40:43], s83 offen sc1 lds\n" \
  "v_mfma_f32_16x16x32_bf16 v[44:47], a[112:115], a[88:91], v[44:47]\n" \
  "ds_read_b64_tr_b16 a[58:59], v229 offset:33792\n" \
  "v_mfma_f32_16x16x32_bf16 v[48:51], a[120:123], a[64:67], v[48:51]\n" \
  "s_add_u32 m0, m0, s82\n" \
  "ds_read_b64_tr_b16 a[60:61], v229 offset:40960\n" \
  "v_mfma_f32_16x16x32_bf16 v[48:51], a[124:127], a[68:71], v[48:51]\n" \
  "buffer_load_dwordx4 v248, s[40:43], s84 offen sc1 lds\n" \
  "v_mfma_f32_16x16x32_bf16 v[52:55], a[124:127], a[76:79], v[52:55]\n" \
  "ds_read_b64_tr_b16 a[62:63], v229 offset:41984\n" \
  "v_mfma_f32_16x16x32_bf16 v[52:55], a[120:123], a[72:75], v[52:55]\n" \
  "v_mfma_f32_16x16x32_bf16 v[56:59], a[120:123], a[80:83], v[56:59]\n" \
  "s_add_u32 s40, s40, 16384\n" \
  "s_addc_u32 s41, s41, 0\n" \
  "v_mfma_f32_16x16x32_bf16 v[56:59], a[124:127], a[84:87], v[56:59]\n" \
  "v_mfma_f32_16x16x32_bf16 v[60:63], a[124:127], a[92:95], v[60:63]\n" \
  "s_add_u32 s48, s48, 128\n" \
  "s_addc_u32 s49, s49, 0\n" \
  "v_mfma_f32_16x16x32_bf16 v[60:63], a[120:123], a[88:91], v[60:63]\n" \
  "s_waitcnt lgkmcnt(0)\n" \
  "s_waitcnt vmcnt(16)\n" \
  "s_barrier\n" \
  "v_mfma_f32_16x16x32_bf16 v[0:3], a[32:35], a[0:3], v[0:3]\n" \
  "s_add_u32 m0, s90, 49152\n" \
  "ds_read_b64_tr_b16 a[96:97], v230 offset:0\n" \
  "v_mfma_f32_16x16x32_bf16 v[0:3], a[36:39], a[4:7], v[0:3]\n" \
  "buffer_load_dwordx4 v249, s[44:47], 0 offen lds\n" \
  "v_mfma_f32_16x16x32_bf16 v[4:7], a[36:39], a[12:15], v[4:7]\n" \
  "ds_read_b64_tr_b16 a[98:99], v230 offset:1024\n" \
  "ds_read_b64_tr_b16 a[100:101], v230 offset:8192\n" \
  "v_mfma_f32_16x16x32_bf16 v[4:7], a[32:35], a[8:11], v[4:7]\n" \
  "s_add_u32 m0, m0, 2048\n" \
  "ds_read_b64_tr_b16 a[102:103], v230 offset:9216\n" \
  "v_mfma_f32_16x16x32_bf16 v[8:11], a[32:35], a[16:19], v[8:11]\n" \
  "buffer_load_dwordx4 v249, s[44:47], s72 offen lds\n" \
  "v_mfma_f32_16x16x32_bf16 v[8:11], a[36:39], a[20:23], v[8:11]\n" \
  "ds_read_b128 a[64:67], v225 offset:0\n" \
  "ds_read_b128 a[68:71], v225 offset:1024\n" \
  "v_mfma_f32_16x16x32_bf16 v[12:15], a[36:39], a[28:31], v[12:15]\n" \
  "ds_read_b128 a[72:75], v225 offset:2048\n" \
  "ds_read_b128 a[76:79], v225 offset:3072\n" \
  "v_mfma_f32_16x16x32_bf16 v[12:15], a[32:35], a[24:27], v[12:15]\n" \
  "s_add_u32 m0, m0, 2048\n" \
  "ds_read_b128 a[80:83], v225 offset:4096\n" \
  "v_mfma_f32_16x16x32_bf16 v[16:19], a[40:43], a[0:3], v[16:19]\n" \
  "buffer_load_dwordx4 v249, s[44:47], s73 offen lds\n" \
  "v_mfma_f32_16x16x32_bf16 v[16:19], a[44:47], a[4:7], v[16:19]\n" \
  "ds_read_b128 a[84:87], v225 offset:5120\n" \
  "ds_read_b128 a[88:91], v225 offset:6144\n" \
  "v_mfma_f32_16x16x32_bf16 v[20:23], a[44:47], a[12:15], v[20:23]\n" \
  "s_add_u32 m0, m0, 2048\n" \
  "ds_read_b128 a[92:95], v225 offset:7168\n" \
  "v_mfma_f32_16x16x32_bf16 v[20:23], a[40:43], a[8:11], v[20:23]\n" \
  "buffer_load_dwordx4 v249, s[44:47], s74 offen lds\n" \
  "v_mfma_f32_16x16x32_bf16 v[24:27], a[40:43], a[16:19], v[24:27]\n" \
  "ds_read_b64_tr_b16 a[104:105], v231 offset:0\n" \
  "ds_read_b64_tr_b16 a[106:107], v231 offset:1024\n" \
  "v_mfma_f32_16x16x32_bf16 v[24:27], a[44:47], a[20:23], v[24:27]\n" \
  "ds_read_b64_tr_b16 a[108:109], v231 offset:8192\n" \
  "v_mfma_f32_16x16x32_bf16 v[28:31], a[44:47], a[28:31], v[28:31]\n" \
  "s_add_u32 m0, s88, 32768\n" \
  "ds_read_b64_tr_b16 a[110:111], v231 offset:9216\n" \
  "v_mfma_f32_16x16x32_bf16 v[28:31], a[40:43], a[24:27], v[28:31]\n" \
  "buffer_load_dwordx4 v248, s[40:43], 0 offen sc1 lds\n" \
  "v_mfma_f32_16x16x32_bf16 v[32:35], a[48:51], a[0:3], v[32:35]\n" \
  "ds_read_b64_tr_b16 a[112:113], v232 offset:0\n" \
  "v_mfma_f32_16x16x32_bf16 v[32:35], a[52:55], a[4:7], v[32:35]\n" \
  "s_add_u32 m0, m0, s82\n" \
  "ds_read_b64_tr_b16 a[114:115], v232 offset:1024\n" \
  "v_mfma_f32_16x16x32_bf16 v[36:39], a[52:55], a[12:15], v[36:39]\n" \
  "buffer_load_dwordx4 v248, s[40:43], s82 offen sc1 lds\n" \
  "v_mfma_f32_16x16x32_bf16 v[36:39], a[48:51], a[8:11], v[36:39]\n" \
  "ds_read_b64_tr_b16 a[116:117], v232 offset:8192\n" \
  "v_mfma_f32_16x16x32_bf16 v[40:43], a[48:51], a[16:19], v[40:43]\n" \
  "ds_read_b64_tr_b16 a[118:119], v232 offset:9216\n" \
  "v_mfma_f32_16x16x32_bf16 v[40:43], a[52:55], a[20:23], v[40:43]\n" \
  "s_add_u32 m0, m0, s82\n" \
  "ds_read_b64_tr_b16 a[120:121], v233 offset:0\n" \
  "v_mfma_f32_16x16x32_bf16 v[44:47], a[52:55], a[28:31], v[44:47]\n" \
  "buffer_load_dwordx4 v248, s[40:43], s83 offen sc1 lds\n" \
  "v_mfma_f32_16x16x32_bf16 v[44:47], a[48:51], a[24:27], v[44:47]\n" \
  "ds_read_b64_tr_b16 a[122:123], v233 offset:1024\n" \
  "v_mfma_f32_16x16x32_bf16 v[48:51], a[56:59], a[0:3], v[48:51]\n" \
  "s_add_u32 m0, m0, s82\n" \
  "ds_read_b64_tr_b16 a[124:125], v233 offset:8192\n" \
  "v_mfma_f32_16x16x32_bf16 v[48:51], a[60:63], a[4:7], v[48:51]\n" \
  "buffer_load_dwordx4 v248, s[40:43], s84 offen sc1 lds\n" \
  "v_mfma_f32_16x16x32_bf16 v[52:55], a[60:63], a[12:15], v[52:55]\n" \
  "ds_read_b64_tr_b16 a[126:127], v233 offset:9216\n" \
  "v_mfma_f32_16x16x32_bf16 v[52:55], a[56:59], a[8:11], v[52:55]\n" \
  "v_mfma_f32_16x16x32_bf16 v[56:59], a[56:59], a[16:19], v[56:59]\n" \
  "s_add_u32 s40, s40, 16384\n" \
  "s_addc_u32 s41, s41, 0\n" \
  "v_mfma_f32_16x16x32_bf16 v[56:59], a[60:63], a[20:23], v[56:59]\n" \
  "v_mfma_f32_16x16x32_bf16 v[60:63], a[60:63], a[28:31], v[60:63]\n" \
  "s_add_u32 s44, s44, 128\n" \
  "s_addc_u32 s45, s45, 0\n" \
  "v_mfma_f32_16x16x32_bf16 v[60:63], a[56:59], a[24:27], v[60:63]\n" \
  "s_waitcnt lgkmcnt(0)\n" \
  "s_waitcnt vmcnt(16)\n" \
  "s_barrier\n" \
  "v_mfma_f32_16x16x32_bf16 v[0:3], a[96:99], a[64:67], v[0:3]\n" \
  "s_add_u32 m0, s90, 81920\n" \
  "ds_read_b128 a[32:35], v244 offset:0\n" \
  "v_mfma_f32_16x16x32_bf16 v[0:3], a[100:103], a[68:71], v[0:3]\n" \
  "buffer_load_dwordx4 v249, s[48:51], 0 offen lds\n" \
  "v_mfma_f32_16x16x32_bf16 v[4:7], a[100:103], a[76:79], v[4:7]\n" \
  "ds_read_b128 a[36:39], v245 offset:0\n" \
  "ds_read_b64_tr_b16 a[0:1], v164 offset:0\n" \
  "v_mfma_f32_16x16x32_bf16 v[4:7], a[96:99], a[72:75], v[4:7]\n" \
  "s_add_u32 m0, m0, 2048\n" \
  "ds_read_b64_tr_b16 a[2:3], v164 offset:256\n" \
  "v_mfma_f32_16x16x32_bf16 v[8:11], a[96:99], a[80:83], v[8:11]\n" \
  "buffer_load_dwordx4 v249, s[48:51], s72 offen lds\n" \
  "v_mfma_f32_16x16x32_bf16 v[8:11], a[100:103], a[84:87], v[8:11]\n" \
  "ds_read_b64_tr_b16 a[4:5], v164 offset:8192\n" \
  "ds_read_b64_tr_b16 a[6:7], v164 offset:8448\n" \
  "v_mfma_f32_16x16x32_bf16 v[12:15], a[100:103], a[92:95], v[12:15]\n" \
  "ds_read_b64_tr_b16 a[8:9], v165 offset:0\n" \
  "ds_read_b64_tr_b16 a[10:11], v165 offset:256\n" \
  "v_mfma_f32_16x16x32_bf16 v[12:15], a[96:99], a[88:91], v[12:15]\n" \
  "s_add_u32 m0, m0, 2048\n" \
  "ds_read_b64_tr_b16 a[12:13], v165 offset:8192\n" \
  "v_mfma_f32_16x16x32_bf16 v[16:19], a[104:107], a[64:67], v[16:19]\n" \
  "buffer_load_dwordx4 v249, s[48:51], s73 offen lds\n" \
  "v_mfma_f32_16x16x32_bf16 v[16:19], a[108:111], a[68:71], v[16:19]\n" \
  "ds_read_b64_tr_b16 a[14:15], v165 offset:8448\n" \
  "ds_read_b64_tr_b16 a[16:17], v166 offset:0\n" \
  "v_mfma_f32_16x16x32_bf16 v[20:23], a[108:111], a[76:79], v[20:23]\n" \
  "s_add_u32 m0, m0, 2048\n" \
  "ds_read_b64_tr_b16 a[18:19], v166 offset:256\n" \
  "v_mfma_f32_16x16x32_bf16 v[20:23], a[104:107], a[72:75], v[20:23]\n" \
  "buffer_load_dwordx4 v249, s[48:51], s74 offen lds\n" \
  "v_mfma_f32_16x16x32_bf16 v[24:27], a[104:107], a[80:83], v[24:27]\n" \
  "ds_read_b64_tr_b16 a[20:21], v166 offset:8192\n" \
  "ds_read_b64_tr_b16 a[22:23], v166 offset:8448\n" \
  "v_mfma_f32_16x16x32_bf16 v[24:27], a[108:111], a[84:87], v[24:27]\n" \
  "ds_read_b64_tr_b16 a[24:25], v167 offset:0\n" \
  "v_mfma_f32_16x16x32_bf16 v[28:31], a[108:111], a[92:95], v[28:31]\n" \
  "s_add_u32 m0, s88, 65536\n" \
  "ds_read_b64_tr_b16 a[26:27], v167 offset:256\n" \
  "v_mfma_f32_16x16x32_bf16 v[28:31], a[104:107], a[88:91], v[28:31]\n" \
  "buffer_load_dwordx4 v248, s[40:43], 0 offen sc1 lds\n" \
  "v_mfma_f32_16x16x32_bf16 v[32:35], a[112:115], a[64:67], v[32:35]\n" \
  "ds_read_b64_tr_b16 a[28:29], v167 offset:8192\n" \
  "v_mfma_f32_16x16x32_bf16 v[32:35], a[116:119], a[68:71], v[32:35]\n" \
  "s_add_u32 m0, m0, s82\n" \
  "ds_read_b64_tr_b16 a[30:31], v167 offset:8448\n" \
  "v_mfma_f32_16x16x32_bf16 v[36:39], a[116:119], a[76:79], v[36:39]\n" \
  "buffer_load_dwordx4 v248, s[40:43], s82 offen sc1 lds\n" \
  "v_mfma_f32_16x16x32_bf16 v[36:39], a[112:115], a[72:75], v[36:39]\n" \
  "ds_read_b128 a[40:43], v244 offset:2048\n" \
  "v_mfma_f32_16x16x32_bf16 v[40:43], a[112:115], a[80:83], v[40:43]\n" \
  "ds_read_b128 a[44:47], v245 offset:2048\n" \
  "v_mfma_f32_16x16x32_bf16 v[40:43], a[116:119], a[84:87], v[40:43]\n" \
  "s_add_u32 m0, m0, s82\n" \
  "ds_read_b128 a[48:51], v244 offset:4096\n" \
  "v_mfma_f32_16x16x32_bf16 v[44:47], a[116:119], a[92:95], v[44:47]\n" \
  "buffer_load_dwordx4 v248, s[40:43], s83 offen sc1 lds\n" \
  "v_mfma_f32_16x16x32_bf16 v[44:47], a[112:115], a[88:91], v[44:47]\n" \
  "ds_read_b128 a[52:55], v245 offset:4096\n" \
  "v_mfma_f32_16x16x32_bf16 v[48:51], a[120:123], a[64:67], v[48:51]\n" \
  "s_add_u32 m0, m0, s82\n" \
  "ds_read_b128 a[56:59], v244 offset:6144\n" \
  "v_mfma_f32_16x16x32_bf16 v[48:51], a[124:127], a[68:71], v[48:51]\n" \
  "buffer_load_dwordx4 v248, s[40:43], s84 offen sc1 lds\n" \
  "v_mfma_f32_16x16x32_bf16 v[52:55], a[124:127], a[76:79], v[52:55]\n" \
  "ds_read_b128 a[60:63], v245 offset:6144\n" \
  "v_mfma_f32_16x16x32_bf16 v[52:55], a[120:123], a[72:75], v[52:55]\n" \
  "v_mfma_f32_16x16x32_bf16 v[56:59], a[120:123], a[80:83], v[56:59]\n" \
  "s_add_u32 s40, s40, 16384\n" \
  "s_addc_u32 s41, s41, 0\n" \
  "v_mfma_f32_16x16x32_bf16 v[56:59], a[124:127], a[84:87], v[56:59]\n" \
  "v_mfma_f32_16x16x32_bf16 v[60:63], a[124:127], a[92:95], v[60:63]\n" \
  "s_add_u32 s48, s48, 128\n" \
  "s_addc_u32 s49, s49, 0\n" \
  "v_mfma_f32_16x16x32_bf16 v[60:63], a[120:123], a[88:91], v[60:63]\n" \
  ".p2align 4\n" \
  "LloopB_%=:\n" \
  "s_waitcnt lgkmcnt(0)\n" \
  "s_waitcnt vmcnt(16)\n" \
  "s_barrier\n" \
  "v_mfma_f32_16x16x32_bf16 v[0:3], a[32:35], a[0:3], v[0:3]\n" \
  "s_add_u32 m0, s90, 16384\n" \
  "ds_read_b128 a[96:99], v246 offset:32768\n" \
  "v_mfma_f32_16x16x32_bf16 v[0:3], a[36:39], a[4:7], v[0:3]\n" \
  "buffer_load_dwordx4 v249, s[44:47], 0 offen lds\n" \
  "v_mfma_f32_16x16x32_bf16 v[4:7], a[36:39], a[12:15], v[4:7]\n" \
  "ds_read_b128 a[100:103], v247 offset:32768\n" \
  "ds_read_b64_tr_b16 a[64:65], v168 offset:32768\n" \
  "v_mfma_f32_16x16x32_bf16 v[4:7], a[32:35], a[8:11], v[4:7]\n" \
  "s_add_u32 m0, m0, 2048\n" \
  "ds_read_b64_tr_b16 a[66:67], v168 offset:33024\n" \
  "v_mfma_f32_16x16x32_bf16 v[8:11], a[32:35], a[16:19], v[8:11]\n" \
  "buffer_load_dwordx4 v249, s[44:47], s72 offen lds\n" \
  "v_mfma_f32_16x16x32_bf16 v[8:11], a[36:39], a[20:23], v[8:11]\n" \
  "ds_read_b64_tr_b16 a[68:69], v168 offset:40960\n" \
  "ds_read_b64_tr_b16 a[70:71], v168 offset:41216\n" \
  "v_mfma_f32_16x16x32_bf16 v[12:15], a[36:39], a[28:31], v[12:15]\n" \
  "ds_read_b64_tr_b16 a[72:73], v169 offset:32768\n" \
  "ds_read_b64_tr_b16 a[74:75], v169 offset:33024\n" \
  "v_mfma_f32_16x16x32_bf16 v[12:15], a[32:35], a[24:27], v[12:15]\n" \
  "s_add_u32 m0, m0, 2048\n" \
  "ds_read_b64_tr_b16 a[76:77], v169 offset:40960\n" \
  "v_mfma_f32_16x16x32_bf16 v[16:19], a[40:43], a[0:3], v[16:19]\n" \
  "buffer_load_dwordx4 v249, s[44:47], s73 offen lds\n" \
  "v_mfma_f32_16x16x32_bf16 v[16:19], a[44:47], a[4:7], v[16:19]\n" \
  "ds_read_b64_tr_b16 a[78:79], v169 offset:41216\n" \
  "ds_read_b64_tr_b16 a[80:81], v170 offset:32768\n" \
  "v_mfma_f32_16x16x32_bf16 v[20:23], a[44:47], a[12:15], v[20:23]\n" \
  "s_add_u32 m0, m0, 2048\n" \
  "ds_read_b64_tr_b16 a[82:83], v170 offset:33024\n" \
  "v_mfma_f32_16x16x32_bf16 v[20:23], a[40:43], a[8:11], v[20:23]\n" \
  "buffer_load_dwordx4 v249, s[44:47], s74 offen lds\n" \
  "v_mfma_f32_16x16x32_bf16 v[24:27], a[40:43], a[16:19], v[24:27]\n" \
  "ds_read_b64_tr_b16 a[84:85], v170 offset:40960\n" \
  "ds_read_b64_tr_b16 a[86:87], v170 offset:41216\n" \
  "v_mfma_f32_16x16x32_bf16 v[24:27], a[44:47], a[20:23], v[24:27]\n" \
  "ds_read_b64_tr_b16 a[88:89], v171 offset:32768\n" \
  "v_mfma_f32_16x16x32_bf16 v[28:31], a[44:47], a[28:31], v[28:31]\n" \
  "s_add_u32 m0, s88, 0\n" \
  "ds_read_b64_tr_b16 a[90:91], v171 offset:33024\n" \
  "v_mfma_f32_16x16x32_bf16 v[28:31], a[40:43], a[24:27], v[28:31]\n" \
  "buffer_load_dwordx4 v248, s[40:43], 0 offen sc1 lds\n" \
  "v_mfma_f32_16x16x32_bf16 v[32:35], a[48:51], a[0:3], v[32:35]\n" \
  "ds_read_b64_tr_b16 a[92:93], v171 offset:40960\n" \
  "v_mfma_f32_16x16x32_bf16 v[32:35], a[52:55], a[4:7], v[32:35]\n" \
  "s_add_u32 m0, m0, s82\n" \
  "ds_read_b64_tr_b16 a[94:95], v171 offset:41216\n" \
  "v_mfma_f32_16x16x32_bf16 v[36:39], a[52:55], a[12:15], v[36:39]\n" \
  "buffer_load_dwordx4 v248, s[40:43], s82 offen sc1 lds\n" \
  "v_mfma_f32_16x16x32_bf16 v[36:39], a[48:51], a[8:11], v[36:39]\n" \
  "ds_read_b128 a[104:107], v246 offset:34816\n" \
  "v_mfma_f32_16x16x32_bf16 v[40:43], a[48:51], a[16:19], v[40:43]\n" \
  "ds_read_b128 a[108:111], v247 offset:34816\n" \
  "v_mfma_f32_16x16x32_bf16 v[40:43], a[52:55], a[20:23], v[40:43]\n" \
  "s_add_u32 m0, m0, s82\n" \
  "ds_read_b128 a[112:115], v246 offset:36864\n" \
  "v_mfma_f32_16x16x32_bf16 v[44:47], a[52:55], a[28:31], v[44:47]\n" \
  "buffer_load_dwordx4 v248, s[40:43], s83 offen sc1 lds\n" \
  "v_mfma_f32_16x16x32_bf16 v[44:47], a[48:51], a[24:27], v[44:47]\n" \
  "ds_read_b128 a[116:119], v247 offset:36864\n" \
  "v_mfma_f32_16x16x32_bf16 v[48:51], a[56:59], a[0:3], v[48:51]\n" \
  "s_add_u32 m0, m0, s82\n" \
  "ds_read_b128 a[120:123], v246 offset:38912\n" \
  "v_mfma_f32_16x16x32_bf16 v[48:51], a[60:63], a[4:7], v[48:51]\n" \
  "buffer_load_dwordx4 v248, s[40:43], s84 offen sc1 lds\n" \
  "v_mfma_f32_16x16x32_bf16 v[52:55], a[60:63], a[12:15], v[52:55]\n" \
  "ds_read_b128 a[124:127], v247 offset:38912\n" \
  "v_mfma_f32_16x16x32_bf16 v[52:55], a[56:59], a[8:11], v[52:55]\n" \
  "v_mfma_f32_16x16x32_bf16 v[56:59], a[56:59], a[16:19], v[56:59]\n" \
  "s_add_u32 s40, s40, 16384\n" \
  "s_addc_u32 s41, s41, 0\n" \
  "v_mfma_f32_16x16x32_bf16 v[56:59], a[60:63], a[20:23], v[56:59]\n" \
  "v_mfma_f32_16x16x32_bf16 v[60:63], a[60:63], a[28:31], v[60:63]\n" \
  "s_add_u32 s44, s44, 128\n" \
  "s_addc_u32 s45, s45, 0\n" \
  "v_mfma_f32_16x16x32_bf16 v[60:63], a[56:59], a[24:27], v[60:63]\n" \
  "s_waitcnt lgkmcnt(0)\n" \
  "s_waitcnt vmcnt(16)\n" \
  "s_barrier\n" \
  "v_mfma_f32_16x16x32_bf16 v[0:3], a[96:99], a[64:67], v[0:3]\n" \
  "s_add_u32 m0, s90, 114688\n" \
  "ds_read_b128 a[32:35], v244 offset:32768\n" \
  "v_mfma_f32_16x16x32_bf16 v[0:3], a[100:103], a[68:71], v[0:3]\n" \
  "buffer_load_dwordx4 v249, s[48:51], 0 offen lds\n" \
  "v_mfma_f32_16x16x32_bf16 v[4:7], a[100:103], a[76:79], v[4:7]\n" \
  "ds_read_b128 a[36:39], v245 offset:32768\n" \
  "ds_read_b64_tr_b16 a[0:1], v164 offset:32768\n" \
  "v_mfma_f32_16x16x32_bf16 v[4:7], a[96:99], a[72:75], v[4:7]\n" \
  "s_add_u32 m0, m0, 2048\n" \
  "ds_read_b64_tr_b16 a[2:3], v164 offset:33024\n" \
  "v_mfma_f32_16x16x32_bf16 v[8:11], a[96:99], a[80:83], v[8:11]\n" \
  "buffer_load_dwordx4 v249, s[48:51], s72 offen lds\n" \
  "v_mfma_f32_16x16x32_bf16 v[8:11], a[100:103], a[84:87], v[8:11]\n" \
  "ds_read_b64_tr_b16 a[4:5], v164 offset:40960\n" \
  "ds_read_b64_tr_b16 a[6:7], v164 offset:41216\n" \
  "v_mfma_f32_16x16x32_bf16 v[12:15], a[100:103], a[92:95], v[12:15]\n" \
  "ds_read_b64_tr_b16 a[8:9], v165 offset:32768\n" \
  "ds_read_b64_tr_b16 a[10:11], v165 offset:33024\n" \
  "v_mfma_f32_16x16x32_bf16 v[12:15], a[96:99], a[88:91], v[12:15]\n" \
  "s_add_u32 m0, m0, 2048\n" \
  "ds_read_b64_tr_b16 a[12:13], v165 offset:40960\n" \
  "v_mfma_f32_16x16x32_bf16 v[16:19], a[104:107], a[64:67], v[16:19]\n" \
  "buffer_load_dwordx4 v249, s[48:51], s73 offen lds\n" \
  "v_mfma_f32_16x16x32_bf16 v[16:19], a[108:111], a[68:71], v[16:19]\n" \
  "ds_read_b64_tr_b16 a[14:15], v165 offset:41216\n" \
  "ds_read_b64_tr_b16 a[16:17], v166 offset:32768\n" \
  "v_mfma_f32_16x16x32_bf16 v[20:23], a[108:111], a[76:79], v[20:23]\n" \
  "s_add_u32 m0, m0, 2048\n" \
  "ds_read_b64_tr_b16 a[18:19], v166 offset:33024\n" \
  "v_mfma_f32_16x16x32_bf16 v[20:23], a[104:107], a[72:75], v[20:23]\n" \
  "buffer_load_dwordx4 v249, s[48:51], s74 offen lds\n" \
  "v_mfma_f32_16x16x32_bf16 v[24:27], a[104:107], a[80:83], v[24:27]\n" \
  "ds_read_b64_tr_b16 a[20:21], v166 offset:40960\n" \
  "ds_read_b64_tr_b16 a[22:23], v166 offset:41216\n" \
  "v_mfma_f32_16x16x32_bf16 v[24:27], a[108:111], a[84:87], v[24:27]\n" \
  "ds_read_b64_tr_b16 a[24:25], v167 offset:32768\n" \
  "v_mfma_f32_16x16x32_bf16 v[28:31], a[108:111], a[92:95], v[28:31]\n" \
  "s_add_u32 m0, s88, 98304\n" \
  "ds_read_b64_tr_b16 a[26:27], v167 offset:33024\n" \
  "v_mfma_f32_16x16x32_bf16 v[28:31], a[104:107], a[88:91], v[28:31]\n" \
  "buffer_load_dwordx4 v248, s[40:43], 0 offen sc1 lds\n" \
  "v_mfma_f32_16x16x32_bf16 v[32:35], a[112:115], a[64:67], v[32:35]\n" \
  "ds_read_b64_tr_b16 a[28:29], v167 offset:40960\n" \
  "v_mfma_f32_16x16x32_bf16 v[32:35], a[116:119], a[68:71], v[32:35]\n" \
  "s_add_u32 m0, m0, s82\n" \
  "ds_read_b64_tr_b16 a[30:31], v167 offset:41216\n" \
  "v_mfma_f32_16x16x32_bf16 v[36:39], a[116:119], a[76:79], v[36:39]\n" \
  "buffer_load_dwordx4 v248, s[40:43], s82 offen sc1 lds\n" \
  "v_mfma_f32_16x16x32_bf16 v[36:39], a[112:115], a[72:75], v[36:39]\n" \
  "ds_read_b128 a[40:43], v244 offset:34816\n" \
  "v_mfma_f32_16x16x32_bf16 v[40:43], a[112:115], a[80:83], v[40:43]\n" \
  "ds_read_b128 a[44:47], v245 offset:34816\n" \
  "v_mfma_f32_16x16x32_bf16 v[40:43], a[116:119], a[84:87], v[40:43]\n" \
  "s_add_u32 m0, m0, s82\n" \
  "ds_read_b128 a[48:51], v244 offset:36864\n" \
  "v_mfma_f32_16x16x32_bf16 v[44:47], a[116:119], a[92:95], v[44:47]\n" \
  "buffer_load_dwordx4 v248, s[40:43], s83 offen sc1 lds\n" \
  "v_mfma_f32_16x16x32_bf16 v[44:47], a[112:115], a[88:91], v[44:47]\n" \
  "ds_read_b128 a[52:55], v245 offset:36864\n" \
  "v_mfma_f32_16x16x32_bf16 v[48:51], a[120:123], a[64:67], v[48:51]\n" \
  "s_add_u32 m0, m0, s82\n" \
  "ds_read_b128 a[56:59], v244 offset:38912\n" \
  "v_mfma_f32_16x16x32_bf16 v[48:51], a[124:127], a[68:71], v[48:51]\n" \
  "buffer_load_dwordx4 v248, s[40:43], s84 offen sc1 lds\n" \
  "v_mfma_f32_16x16x32_bf16 v[52:55], a[124:127], a[76:79], v[52:55]\n" \
  "ds_read_b128 a[60:63], v245 offset:38912\n" \
  "v_mfma_f32_16x16x32_bf16 v[52:55], a[120:123], a[72:75], v[52:55]\n" \
  "v_mfma_f32_16x16x32_bf16 v[56:59], a[120:123], a[80:83], v[56:59]\n" \
  "s_add_u32 s40, s40, 16384\n" \
  "s_addc_u32 s41, s41, 0\n" \
  "v_mfma_f32_16x16x32_bf16 v[56:59], a[124:127], a[84:87], v[56:59]\n" \
  "v_mfma_f32_16x16x32_bf16 v[60:63], a[124:127], a[92:95], v[60:63]\n" \
  "s_add_u32 s48, s48, 128\n" \
  "s_addc_u32 s49, s49, 0\n" \
  "v_mfma_f32_16x16x32_bf16 v[60:63], a[120:123], a[88:91], v[60:63]\n" \
  "s_waitcnt lgkmcnt(0)\n" \
  "s_waitcnt vmcnt(16)\n" \
  "s_barrier\n" \
  "v_mfma_f32_16x16x32_bf16 v[0:3], a[32:35], a[0:3], v[0:3]\n" \
  "s_add_u32 m0, s90, 49152\n" \
  "ds_read_b128 a[96:99], v246 offset:0\n" \
  "v_mfma_f32_16x16x32_bf16 v[0:3], a[36:39], a[4:7], v[0:3]\n" \
  "buffer_load_dwordx4 v249, s[44:47], 0 offen lds\n" \
  "v_mfma_f32_16x16x32_bf16 v[4:7], a[36:39], a[12:15], v[4:7]\n" \
  "ds_read_b128 a[100:103], v247 offset:0\n" \
  "ds_read_b64_tr_b16 a[64:65], v168 offset:0\n" \
  "v_mfma_f32_16x16x32_bf16 v[4:7], a[32:35], a[8:11], v[4:7]\n" \
  "s_add_u32 m0, m0, 2048\n" \
  "ds_read_b64_tr_b16 a[66:67], v168 offset:256\n" \
  "v_mfma_f32_16x16x32_bf16 v[8:11], a[32:35], a[16:19], v[8:11]\n" \
  "buffer_load_dwordx4 v249, s[44:47], s72 offen lds\n" \
  "v_mfma_f32_16x16x32_bf16 v[8:11], a[36:39], a[20:23], v[8:11]\n" \
  "ds_read_b64_tr_b16 a[68:69], v168 offset:8192\n" \
  "ds_read_b64_tr_b16 a[70:71], v168 offset:8448\n" \
  "v_mfma_f32_16x16x32_bf16 v[12:15], a[36:39], a[28:31], v[12:15]\n" \
  "ds_read_b64_tr_b16 a[72:73], v169 offset:0\n" \
  "ds_read_b64_tr_b16 a[74:75], v169 offset:256\n" \
  "v_mfma_f32_16x16x32_bf16 v[12:15], a[32:35], a[24:27], v[12:15]\n" \
  "s_add_u32 m0, m0, 2048\n" \
  "ds_read_b64_tr_b16 a[76:77], v169 offset:8192\n" \
  "v_mfma_f32_16x16x32_bf16 v[16:19], a[40:43], a[0:3], v[16:19]\n" \
  "buffer_load_dwordx4 v249, s[44:47], s73 offen lds\n" \
  "v_mfma_f32_16x16x32_bf16 v[16:19], a[44:47], a[4:7], v[16:19]\n" \
  "ds_read_b64_tr_b16 a[78:79], v169 offset:8448\n" \
  "ds_read_b64_tr_b16 a[80:81], v170 offset:0\n" \
  "v_mfma_f32_16x16x32_bf16 v[20:23], a[44:47], a[12:15], v[20:23]\n" \
  "s_add_u32 m0, m0, 2048\n" \
  "ds_read_b64_tr_b16 a[82:83], v170 offset:256\n" \
  "v_mfma_f32_16x16x32_bf16 v[20:23], a[40:43], a[8:11], v[20:23]\n" \
  "buffer_load_dwordx4 v249, s[44:47], s74 offen lds\n" \
  "v_mfma_f32_16x16x32_bf16 v[24:27], a[40:43], a[16:19], v[24:27]\n" \
  "ds_read_b64_tr_b16 a[84:85], v170 offset:8192\n" \
  "ds_read_b64_tr_b16 a[86:87], v170 offset:8448\n" \
  "v_mfma_f32_16x16x32_bf16 v[24:27], a[44:47], a[20:23], v[24:27]\n" \
  "ds_read_b64_tr_b16 a[88:89], v171 offset:0\n" \
  "v_mfma_f32_16x16x32_bf16 v[28:31], a[44:47], a[28:31], v[28:31]\n" \
  "s_add_u32 m0, s88, 32768\n" \
  "ds_read_b64_tr_b16 a[90:91], v171 offset:256\n" \
  "v_mfma_f32_16x16x32_bf16 v[28:31], a[40:43], a[24:27], v[28:31]\n" \
  "buffer_load_dwordx4 v248, s[40:43], 0 offen sc1 lds\n" \
  "v_mfma_f32_16x16x32_bf16 v[32:35], a[48:51], a[0:3], v[32:35]\n" \
  "ds_read_b64_tr_b16 a[92:93], v171 offset:8192\n" \
  "v_mfma_f32_16x16x32_bf16 v[32:35], a[52:55], a[4:7], v[32:35]\n" \
  "s_add_u32 m0, m0, s82\n" \
  "ds_read_b64_tr_b16 a[94:95], v171 offset:8448\n" \
  "v_mfma_f32_16x16x32_bf16 v[36:39], a[52:55], a[12:15], v[36:39]\n" \
  "buffer_load_dwordx4 v248, s[40:43], s82 offen sc1 lds\n" \
  "v_mfma_f32_16x16x32_bf16 v[36:39], a[48:51], a[8:11], v[36:39]\n" \
  "ds_read_b128 a[104:107], v246 offset:2048\n" \
  "v_mfma_f32_16x16x32_bf16 v[40:43], a[48:51], a[16:19], v[40:43]\n" \
  "ds_read_b128 a[108:111], v247 offset:2048\n" \
  "v_mfma_f32_16x16x32_bf16 v[40:43], a[52:55], a[20:23], v[40:43]\n" \
  "s_add_u32 m0, m0, s82\n" \
  "ds_read_b128 a[112:115], v246 offset:4096\n" \
  "v_mfma_f32_16x16x32_bf16 v[44:47], a[52:55], a[28:31], v[44:47]\n" \
  "buffer_load_dwordx4 v248, s[40:43], s83 offen sc1 lds\n" \
  "v_mfma_f32_16x16x32_bf16 v[44:47], a[48:51], a[24:27], v[44:47]\n" \
  "ds_read_b128 a[116:119], v247 offset:4096\n" \
  "v_mfma_f32_16x16x32_bf16 v[48:51], a[56:59], a[0:3], v[48:51]\n" \
  "s_add_u32 m0, m0, s82\n" \
  "ds_read_b128 a[120:123], v246 offset:6144\n" \
  "v_mfma_f32_16x16x32_bf16 v[48:51], a[60:63], a[4:7], v[48:51]\n" \
  "buffer_load_dwordx4 v248, s[40:43], s84 offen sc1 lds\n" \
  "v_mfma_f32_16x16x32_bf16 v[52:55], a[60:63], a[12:15], v[52:55]\n" \
  "ds_read_b128 a[124:127], v247 offset:6144\n" \
  "v_mfma_f32_16x16x32_bf16 v[52:55], a[56:59], a[8:11], v[52:55]\n" \
  "v_mfma_f32_16x16x32_bf16 v[56:59], a[56:59], a[16:19], v[56:59]\n" \
  "s_add_u32 s40, s40, 16384\n" \
  "s_addc_u32 s41, s41, 0\n" \
  "v_mfma_f32_16x16x32_bf16 v[56:59], a[60:63], a[20:23], v[56:59]\n" \
  "v_mfma_f32_16x16x32_bf16 v[60:63], a[60:63], a[28:31], v[60:63]\n" \
  "s_add_u32 s44, s44, 128\n" \
  "s_addc_u32 s45, s45, 0\n" \
  "v_mfma_f32_16x16x32_bf16 v[60:63], a[56:59], a[24:27], v[60:63]\n" \
  "s_waitcnt lgkmcnt(0)\n" \
  "s_waitcnt vmcnt(16)\n" \
  "s_barrier\n" \
  "v_mfma_f32_16x16x32_bf16 v[0:3], a[96:99], a[64:67], v[0:3]\n" \
  "s_add_u32 m0, s90, 81920\n" \
  "ds_read_b128 a[32:35], v244 offset:0\n" \
  "v_mfma_f32_16x16x32_bf16 v[0:3], a[100:103], a[68:71], v[0:3]\n" \
  "buffer_load_dwordx4 v249, s[48:51], 0 offen lds\n" \
  "v_mfma_f32_16x16x32_bf16 v[4:7], a[100:103], a[76:79], v[4:7]\n" \
  "ds_read_b128 a[36:39], v245 offset:0\n" \
  "ds_read_b64_tr_b16 a[0:1], v164 offset:0\n" \
  "v_mfma_f32_16x16x32_bf16 v[4:7], a[96:99], a[72:75], v[4:7]\n" \
  "s_add_u32 m0, m0, 2048\n" \
  "ds_read_b64_tr_b16 a[2:3], v164 offset:256\n" \
  "v_mfma_f32_16x16x32_bf16 v[8:11], a[96:99], a[80:83], v[8:11]\n" \
  "buffer_load_dwordx4 v249, s[48:51], s72 offen lds\n" \
  "v_mfma_f32_16x16x32_bf16 v[8:11], a[100:103], a[84:87], v[8:11]\n" \
  "ds_read_b64_tr_b16 a[4:5], v164 offset:8192\n" \
  "ds_read_b64_tr_b16 a[6:7], v164 offset:8448\n" \
  "v_mfma_f32_16x16x32_bf16 v[12:15], a[100:103], a[92:95], v[12:15]\n" \
  "ds_read_b64_tr_b16 a[8:9], v165 offset:0\n" \
  "ds_read_b64_tr_b16 a[10:11], v165 offset:256\n" \
  "v_mfma_f32_16x16x32_bf16 v[12:15], a[96:99], a[88:91], v[12:15]\n" \
  "s_add_u32 m0, m0, 2048\n" \
  "ds_read_b64_tr_b16 a[12:13], v165 offset:8192\n" \
  "v_mfma_f32_16x16x32_bf16 v[16:19], a[104:107], a[64:67], v[16:19]\n" \
  "buffer_load_dwordx4 v249, s[48:51], s73 offen lds\n" \
  "v_mfma_f32_16x16x32_bf16 v[16:19], a[108:111], a[68:71], v[16:19]\n" \
  "ds_read_b64_tr_b16 a[14:15], v165 offset:8448\n" \
  "ds_read_b64_tr_b16 a[16:17], v166 offset:0\n" \
  "v_mfma_f32_16x16x32_bf16 v[20:23], a[108:111], a[76:79], v[20:23]\n" \
  "s_add_u32 m0, m0, 2048\n" \
  "ds_read_b64_tr_b16 a[18:19], v166 offset:256\n" \
  "v_mfma_f32_16x16x32_bf16 v[20:23], a[104:107], a[72:75], v[20:23]\n" \
  "buffer_load_dwordx4 v249, s[48:51], s74 offen lds\n" \
  "v_mfma_f32_16x16x32_bf16 v[24:27], a[104:107], a[80:83], v[24:27]\n" \
  "ds_read_b64_tr_b16 a[20:21], v166 offset:8192\n" \
  "ds_read_b64_tr_b16 a[22:23], v166 offset:8448\n" \
  "v_mfma_f32_16x16x32_bf16 v[24:27], a[108:111], a[84:87], v[24:27]\n" \
  "ds_read_b64_tr_b16 a[24:25], v167 offset:0\n" \
  "v_mfma_f32_16x16x32_bf16 v[28:31], a[108:111], a[92:95], v[28:31]\n" \
  "s_add_u32 m0, s88, 65536\n" \
  "ds_read_b64_tr_b16 a[26:27], v167 offset:256\n" \
  "v_mfma_f32_16x16x32_bf16 v[28:31], a[104:107], a[88:91], v[28:31]\n" \
  "buffer_load_dwordx4 v248, s[40:43], 0 offen sc1 lds\n" \
  "v_mfma_f32_16x16x32_bf16 v[32:35], a[112:115], a[64:67], v[32:35]\n" \
  "ds_read_b64_tr_b16 a[28:29], v167 offset:8192\n" \
  "v_mfma_f32_16x16x32_bf16 v[32:35], a[116:119], a[68:71], v[32:35]\n" \
  "s_add_u32 m0, m0, s82\n" \
  "ds_read_b64_tr_b16 a[30:31], v167 offset:8448\n" \
  "v_mfma_f32_16x16x32_bf16 v[36:39], a[116:119], a[76:79], v[36:39]\n" \
  "buffer_load_dwordx4 v248, s[40:43], s82 offen sc1 lds\n" \
  "v_mfma_f32_16x16x32_bf16 v[36:39], a[112:115], a[72:75], v[36:39]\n" \
  "ds_read_b128 a[40:43], v244 offset:2048\n" \
  "v_mfma_f32_16x16x32_bf16 v[40:43], a[112:115], a[80:83], v[40:43]\n" \
  "ds_read_b128 a[44:47], v245 offset:2048\n" \
  "v_mfma_f32_16x16x32_bf16 v[40:43], a[116:119], a[84:87], v[40:43]\n" \
  "s_add_u32 m0, m0, s82\n" \
  "ds_read_b128 a[48:51], v244 offset:4096\n" \
  "v_mfma_f32_16x16x32_bf16 v[44:47], a[116:119], a[92:95], v[44:47]\n" \
  "buffer_load_dwordx4 v248, s[40:43], s83 offen sc1 lds\n" \
  "v_mfma_f32_16x16x32_bf16 v[44:47], a[112:115], a[88:91], v[44:47]\n" \
  "ds_read_b128 a[52:55], v245 offset:4096\n" \
  "v_mfma_f32_16x16x32_bf16 v[48:51], a[120:123], a[64:67], v[48:51]\n" \
  "s_add_u32 m0, m0, s82\n" \
  "ds_read_b128 a[56:59], v244 offset:6144\n" \
  "v_mfma_f32_16x16x32_bf16 v[48:51], a[124:127], a[68:71], v[48:51]\n" \
  "buffer_load_dwordx4 v248, s[40:43], s84 offen sc1 lds\n" \
  "v_mfma_f32_16x16x32_bf16 v[52:55], a[124:127], a[76:79], v[52:55]\n" \
  "ds_read_b128 a[60:63], v245 offset:6144\n" \
  "v_mfma_f32_16x16x32_bf16 v[52:55], a[120:123], a[72:75], v[52:55]\n" \
  "v_mfma_f32_16x16x32_bf16 v[56:59], a[120:123], a[80:83], v[56:59]\n" \
  "s_add_u32 s40, s40, 16384\n" \
  "s_addc_u32 s41, s41, 0\n" \
  "v_mfma_f32_16x16x32_bf16 v[56:59], a[124:127], a[84:87], v[56:59]\n" \
  "v_mfma_f32_16x16x32_bf16 v[60:63], a[124:127], a[92:95], v[60:63]\n" \
  "s_add_u32 s48, s48, 128\n" \
  "s_addc_u32 s49, s49, 0\n" \
  "v_mfma_f32_16x16x32_bf16 v[60:63], a[120:123], a[88:91], v[60:63]\n" \
  "s_sub_u32 s89, s89, 1\n" \
  "s_cmp_lg_u32 s89, 0\n" \
  "s_cbranch_scc1 LloopB_%=\n" \
  "s_waitcnt lgkmcnt(0)\n" \
  "s_waitcnt vmcnt(16)\n" \
  "s_barrier\n" \
  "v_mfma_f32_16x16x32_bf16 v[0:3], a[32:35], a[0:3], v[0:3]\n" \
  "s_add_u32 m0, s88, 0\n" \
  "ds_read_b128 a[96:99], v246 offset:32768\n" \
  "v_mfma_f32_16x16x32_bf16 v[0:3], a[36:39], a[4:7], v[0:3]\n" \
  "buffer_load_dwordx4 v250, s[56:59], 0 offen lds\n" \
  "v_mfma_f32_16x16x32_bf16 v[4:7], a[36:39], a[12:15], v[4:7]\n" \
  "ds_read_b128 a[100:103], v247 offset:32768\n" \
  "ds_read_b64_tr_b16 a[64:65], v168 offset:32768\n" \
  "v_mfma_f32_16x16x32_bf16 v[4:7], a[32:35], a[8:11], v[4:7]\n" \
  "s_add_u32 m0, m0, s82\n" \
  "ds_read_b64_tr_b16 a[66:67], v168 offset:33024\n" \
  "v_mfma_f32_16x16x32_bf16 v[8:11], a[32:35], a[16:19], v[8:11]\n" \
  "buffer_load_dwordx4 v250, s[56:59], s72 offen lds\n" \
  "v_mfma_f32_16x16x32_bf16 v[8:11], a[36:39], a[20:23], v[8:11]\n" \
  "ds_read_b64_tr_b16 a[68:69], v168 offset:40960\n" \
  "ds_read_b64_tr_b16 a[70:71], v168 offset:41216\n" \
  "v_mfma_f32_16x16x32_bf16 v[12:15], a[36:39], a[28:31], v[12:15]\n" \
  "ds_read_b64_tr_b16 a[72:73], v169 offset:32768\n" \
  "ds_read_b64_tr_b16 a[74:75], v169 offset:33024\n" \
  "v_mfma_f32_16x16x32_bf16 v[12:15], a[32:35], a[24:27], v[12:15]\n" \
  "s_add_u32 m0, m0, s82\n" \
  "ds_read_b64_tr_b16 a[76:77], v169 offset:40960\n" \
  "v_mfma_f32_16x16x32_bf16 v[16:19], a[40:43], a[0:3], v[16:19]\n" \
  "buffer_load_dwordx4 v250, s[56:59], s75 offen lds\n" \
  "v_mfma_f32_16x16x32_bf16 v[16:19], a[44:47], a[4:7], v[16:19]\n" \
  "ds_read_b64_tr_b16 a[78:79], v169 offset:41216\n" \
  "ds_read_b64_tr_b16 a[80:81], v170 offset:32768\n" \
  "v_mfma_f32_16x16x32_bf16 v[20:23], a[44:47], a[12:15], v[20:23]\n" \
  "s_add_u32 m0, m0, s82\n" \
  "ds_read_b64_tr_b16 a[82:83], v170 offset:33024\n" \
  "v_mfma_f32_16x16x32_bf16 v[20:23], a[40:43], a[8:11], v[20:23]\n" \
  "buffer_load_dwordx4 v250, s[56:59], s76 offen lds\n" \
  "v_mfma_f32_16x16x32_bf16 v[24:27], a[40:43], a[16:19], v[24:27]\n" \
  "ds_read_b64_tr_b16 a[84:85], v170 offset:40960\n" \
  "ds_read_b64_tr_b16 a[86:87], v170 offset:41216\n" \
  "v_mfma_f32_16x16x32_bf16 v[24:27], a[44:47], a[20:23], v[24:27]\n" \
  "ds_read_b64_tr_b16 a[88:89], v171 offset:32768\n" \
  "v_mfma_f32_16x16x32_bf16 v[28:31], a[44:47], a[28:31], v[28:31]\n" \
  "s_add_u32 m0, m0, s82\n" \
  "ds_read_b64_tr_b16 a[90:91], v171 offset:33024\n" \
  "v_mfma_f32_16x16x32_bf16 v[28:31], a[40:43], a[24:27], v[28:31]\n" \
  "buffer_load_dwordx4 v250, s[16:19], 0 offen sc1 lds\n" \
  "v_mfma_f32_16x16x32_bf16 v[32:35], a[48:51], a[0:3], v[32:35]\n" \
  "ds_read_b64_tr_b16 a[92:93], v171 offset:40960\n" \
  "v_mfma_f32_16x16x32_bf16 v[32:35], a[52:55], a[4:7], v[32:35]\n" \
  "s_add_u32 m0, m0, s82\n" \
  "ds_read_b64_tr_b16 a[94:95], v171 offset:41216\n" \
  "v_mfma_f32_16x16x32_bf16 v[36:39], a[52:55], a[12:15], v[36:39]\n" \
  "buffer_load_dwordx4 v250, s[16:19], s72 offen sc1 lds\n" \
  "v_mfma_f32_16x16x32_bf16 v[36:39], a[48:51], a[8:11], v[36:39]\n" \
  "ds_read_b128 a[104:107], v246 offset:34816\n" \
  "v_mfma_f32_16x16x32_bf16 v[40:43], a[48:51], a[16:19], v[40:43]\n" \
  "ds_read_b128 a[108:111], v247 offset:34816\n" \
  "v_mfma_f32_16x16x32_bf16 v[40:43], a[52:55], a[20:23], v[40:43]\n" \
  "s_add_u32 m0, m0, s82\n" \
  "ds_read_b128 a[112:115], v246 offset:36864\n" \
  "v_mfma_f32_16x16x32_bf16 v[44:47], a[52:55], a[28:31], v[44:47]\n" \
  "buffer_load_dwordx4 v250, s[16:19], s75 offen sc1 lds\n" \
  "v_mfma_f32_16x16x32_bf16 v[44:47], a[48:51], a[24:27], v[44:47]\n" \
  "ds_read_b128 a[116:119], v247 offset:36864\n" \
  "v_mfma_f32_16x16x32_bf16 v[48:51], a[56:59], a[0:3], v[48:51]\n" \
  "s_add_u32 m0, m0, s82\n" \
  "ds_read_b128 a[120:123], v246 offset:38912\n" \
  "v_mfma_f32_16x16x32_bf16 v[48:51], a[60:63], a[4:7], v[48:51]\n" \
  "buffer_load_dwordx4 v250, s[16:19], s76 offen sc1 lds\n" \
  "v_mfma_f32_16x16x32_bf16 v[52:55], a[60:63], a[12:15], v[52:55]\n" \
  "ds_read_b128 a[124:127], v247 offset:38912\n" \
  "v_mfma_f32_16x16x32_bf16 v[52:55], a[56:59], a[8:11], v[52:55]\n" \
  "v_mfma_f32_16x16x32_bf16 v[56:59], a[56:59], a[16:19], v[56:59]\n" \
  "v_mfma_f32_16x16x32_bf16 v[56:59], a[60:63], a[20:23], v[56:59]\n" \
  "v_mfma_f32_16x16x32_bf16 v[60:63], a[60:63], a[28:31], v[60:63]\n" \
  "v_mfma_f32_16x16x32_bf16 v[60:63], a[56:59], a[24:27], v[60:63]\n" \
  "s_waitcnt lgkmcnt(0)\n" \
  "s_waitcnt vmcnt(16)\n" \
  "s_barrier\n" \
  "v_mfma_f32_16x16x32_bf16 v[0:3], a[96:99], a[64:67], v[0:3]\n" \
  "ds_read_b128 a[32:35], v244 offset:32768\n" \
  "v_mfma_f32_16x16x32_bf16 v[0:3], a[100:103], a[68:71], v[0:3]\n" \
  "ds_read_b128 a[36:39], v245 offset:32768\n" \
  "v_mfma_f32_16x16x32_bf16 v[4:7], a[100:103], a[76:79], v[4:7]\n" \
  "ds_read_b64_tr_b16 a[0:1], v164 offset:32768\n" \
  "v_mfma_f32_16x16x32_bf16 v[4:7], a[96:99], a[72:75], v[4:7]\n" \
  "ds_read_b64_tr_b16 a[2:3], v164 offset:33024\n" \
  "v_mfma_f32_16x16x32_bf16 v[8:11], a[96:99], a[80:83], v[8:11]\n" \
  "ds_read_b64_tr_b16 a[4:5], v164 offset:40960\n" \
  "v_mfma_f32_16x16x32_bf16 v[8:11], a[100:103], a[84:87], v[8:11]\n" \
  "ds_read_b64_tr_b16 a[6:7], v164 offset:41216\n" \
  "v_mfma_f32_16x16x32_bf16 v[12:15], a[100:103], a[92:95], v[12:15]\n" \
  "ds_read_b64_tr_b16 a[8:9], v165 offset:32768\n" \
  "v_mfma_f32_16x16x32_bf16 v[12:15], a[96:99], a[88:91], v[12:15]\n" \
  "ds_read_b64_tr_b16 a[10:11], v165 offset:33024\n" \
  "v_mfma_f32_16x16x32_bf16 v[16:19], a[104:107], a[64:67], v[16:19]\n" \
  "ds_read_b64_tr_b16 a[12:13], v165 offset:40960\n" \
  "v_mfma_f32_16x16x32_bf16 v[16:19], a[108:111], a[68:71], v[16:19]\n" \
  "ds_read_b64_tr_b16 a[14:15], v165 offset:41216\n" \
  "v_mfma_f32_16x16x32_bf16 v[20:23], a[108:111], a[76:79], v[20:23]\n" \
  "ds_read_b64_tr_b16 a[16:17], v166 offset:32768\n" \
  "v_mfma_f32_16x16x32_bf16 v[20:23], a[104:107], a[72:75], v[20:23]\n" \
  "ds_read_b64_tr_b16 a[18:19], v166 offset:33024\n" \
  "v_mfma_f32_16x16x32_bf16 v[24:27], a[104:107], a[80:83], v[24:27]\n" \
  "ds_read_b64_tr_b16 a[20:21], v166 offset:40960\n" \
  "v_mfma_f32_16x16x32_bf16 v[24:27], a[108:111], a[84:87], v[24:27]\n" \
  "ds_read_b64_tr_b16 a[22:23], v166 offset:41216\n" \
  "v_mfma_f32_16x16x32_bf16 v[28:31], a[108:111], a[92:95], v[28:31]\n" \
  "ds_read_b64_tr_b16 a[24:25], v167 offset:32768\n" \
  "v_mfma_f32_16x16x32_bf16 v[28:31], a[104:107], a[88:91], v[28:31]\n" \
  "ds_read_b64_tr_b16 a[26:27], v167 offset:33024\n" \
  "v_mfma_f32_16x16x32_bf16 v[32:35], a[112:115], a[64:67], v[32:35]\n" \
  "ds_read_b64_tr_b16 a[28:29], v167 offset:40960\n" \
  "v_mfma_f32_16x16x32_bf16 v[32:35], a[116:119], a[68:71], v[32:35]\n" \
  "ds_read_b64_tr_b16 a[30:31], v167 offset:41216\n" \
  "v_mfma_f32_16x16x32_bf16 v[36:39], a[116:119], a[76:79], v[36:39]\n" \
  "ds_read_b128 a[40:43], v244 offset:34816\n" \
  "v_mfma_f32_16x16x32_bf16 v[36:39], a[112:115], a[72:75], v[36:39]\n" \
  "ds_read_b128 a[44:47], v245 offset:34816\n" \
  "v_mfma_f32_16x16x32_bf16 v[40:43], a[112:115], a[80:83], v[40:43]\n" \
  "ds_read_b128 a[48:51], v244 offset:36864\n" \
  "v_mfma_f32_16x16x32_bf16 v[40:43], a[116:119], a[84:87], v[40:43]\n" \
  "ds_read_b128 a[52:55], v245 offset:36864\n" \
  "v_mfma_f32_16x16x32_bf16 v[44:47], a[116:119], a[92:95], v[44:47]\n" \
  "ds_read_b128 a[56:59], v244 offset:38912\n" \
  "v_mfma_f32_16x16x32_bf16 v[44:47], a[112:115], a[88:91], v[44:47]\n" \
  "ds_read_b128 a[60:63], v245 offset:38912\n" \
  "v_mfma_f32_16x16x32_bf16 v[48:51], a[120:123], a[64:67], v[48:51]\n" \
  "v_mfma_f32_16x16x32_bf16 v[48:51], a[124:127], a[68:71], v[48:51]\n" \
  "v_mfma_f32_16x16x32_bf16 v[52:55], a[124:127], a[76:79], v[52:55]\n" \
  "v_mfma_f32_16x16x32_bf16 v[52:55], a[120:123], a[72:75], v[52:55]\n" \
  "v_mfma_f32_16x16x32_bf16 v[56:59], a[120:123], a[80:83], v[56:59]\n" \
  "v_mfma_f32_16x16x32_bf16 v[56:59], a[124:127], a[84:87], v[56:59]\n" \
  "v_mfma_f32_16x16x32_bf16 v[60:63], a[124:127], a[92:95], v[60:63]\n" \
  "v_mfma_f32_16x16x32_bf16 v[60:63], a[120:123], a[88:91], v[60:63]\n" \
  "s_waitcnt lgkmcnt(0)\n" \
  "s_waitcnt vmcnt(8)\n" \
  "s_barrier\n" \
  "v_mfma_f32_16x16x32_bf16 v[0:3], a[32:35], a[0:3], v[0:3]\n" \
  "ds_read_b128 a[96:99], v246 offset:0\n" \
  "v_mfma_f32_16x16x32_bf16 v[0:3], a[36:39], a[4:7], v[0:3]\n" \
  "ds_read_b128 a[100:103], v247 offset:0\n" \
  "v_mfma_f32_16x16x32_bf16 v[4:7], a[36:39], a[12:15], v[4:7]\n" \
  "ds_read_b64_tr_b16 a[64:65], v168 offset:0\n" \
  "v_mfma_f32_16x16x32_bf16 v[4:7], a[32:35], a[8:11], v[4:7]\n" \
  "ds_read_b64_tr_b16 a[66:67], v168 offset:256\n" \
  "v_mfma_f32_16x16x32_bf16 v[8:11], a[32:35], a[16:19], v[8:11]\n" \
  "ds_read_b64_tr_b16 a[68:69], v168 offset:8192\n" \
  "v_mfma_f32_16x16x32_bf16 v[8:11], a[36:39], a[20:23], v[8:11]\n" \
  "ds_read_b64_tr_b16 a[70:71], v168 offset:8448\n" \
  "v_mfma_f32_16x16x32_bf16 v[12:15], a[36:39], a[28:31], v[12:15]\n" \
  "ds_read_b64_tr_b16 a[72:73], v169 offset:0\n" \
  "v_mfma_f32_16x16x32_bf16 v[12:15], a[32:35], a[24:27], v[12:15]\n" \
  "ds_read_b64_tr_b16 a[74:75], v169 offset:256\n" \
  "v_mfma_f32_16x16x32_bf16 v[16:19], a[40:43], a[0:3], v[16:19]\n" \
  "ds_read_b64_tr_b16 a[76:77], v169 offset:8192\n" \
  "v_mfma_f32_16x16x32_bf16 v[16:19], a[44:47], a[4:7], v[16:19]\n" \
  "ds_read_b64_tr_b16 a[78:79], v169 offset:8448\n" \
  "v_mfma_f32_16x16x32_bf16 v[20:23], a[44:47], a[12:15], v[20:23]\n" \
  "ds_read_b64_tr_b16 a[80:81], v170 offset:0\n" \
  "v_mfma_f32_16x16x32_bf16 v[20:23], a[40:43], a[8:11], v[20:23]\n" \
  "ds_read_b64_tr_b16 a[82:83], v170 offset:256\n" \
  "v_mfma_f32_16x16x32_bf16 v[24:27], a[40:43], a[16:19], v[24:27]\n" \
  "ds_read_b64_tr_b16 a[84:85], v170 offset:8192\n" \
  "v_mfma_f32_16x16x32_bf16 v[24:27], a[44:47], a[20:23], v[24:27]\n" \
  "ds_read_b64_tr_b16 a[86:87], v170 offset:8448\n" \
  "v_mfma_f32_16x16x32_bf16 v[28:31], a[44:47], a[28:31], v[28:31]\n" \
  "ds_read_b64_tr_b16 a[88:89], v171 offset:0\n" \
  "v_mfma_f32_16x16x32_bf16 v[28:31], a[40:43], a[24:27], v[28:31]\n" \
  "ds_read_b64_tr_b16 a[90:91], v171 offset:256\n" \
  "v_mfma_f32_16x16x32_bf16 v[32:35], a[48:51], a[0:3], v[32:35]\n" \
  "ds_read_b64_tr_b16 a[92:93], v171 offset:8192\n" \
  "v_mfma_f32_16x16x32_bf16 v[32:35], a[52:55], a[4:7], v[32:35]\n" \
  "ds_read_b64_tr_b16 a[94:95], v171 offset:8448\n" \
  "v_mfma_f32_16x16x32_bf16 v[36:39], a[52:55], a[12:15], v[36:39]\n" \
  "ds_read_b128 a[104:107], v246 offset:2048\n" \
  "v_mfma_f32_16x16x32_bf16 v[36:39], a[48:51], a[8:11], v[36:39]\n" \
  "ds_read_b128 a[108:111], v247 offset:2048\n" \
  "v_mfma_f32_16x16x32_bf16 v[40:43], a[48:51], a[16:19], v[40:43]\n" \
  "ds_read_b128 a[112:115], v246 offset:4096\n" \
  "v_mfma_f32_16x16x32_bf16 v[40:43], a[52:55], a[20:23], v[40:43]\n" \
  "ds_read_b128 a[116:119], v247 offset:4096\n" \
  "v_mfma_f32_16x16x32_bf16 v[44:47], a[52:55], a[28:31], v[44:47]\n" \
  "ds_read_b128 a[120:123], v246 offset:6144\n" \
  "v_mfma_f32_16x16x32_bf16 v[44:47], a[48:51], a[24:27], v[44:47]\n" \
  "ds_read_b128 a[124:127], v247 offset:6144\n" \
  "v_mfma_f32_16x16x32_bf16 v[48:51], a[56:59], a[0:3], v[48:51]\n" \
  "v_mfma_f32_16x16x32_bf16 v[48:51], a[60:63], a[4:7], v[48:51]\n" \
  "v_mfma_f32_16x16x32_bf16 v[52:55], a[60:63], a[12:15], v[52:55]\n" \
  "v_mfma_f32_16x16x32_bf16 v[52:55], a[56:59], a[8:11], v[52:55]\n" \
  "v_mfma_f32_16x16x32_bf16 v[56:59], a[56:59], a[16:19], v[56:59]\n" \
  "v_mfma_f32_16x16x32_bf16 v[56:59], a[60:63], a[20:23], v[56:59]\n" \
  "v_mfma_f32_16x16x32_bf16 v[60:63], a[60:63], a[28:31], v[60:63]\n" \
  "v_mfma_f32_16x16x32_bf16 v[60:63], a[56:59], a[24:27], v[60:63]\n" \
  "s_waitcnt lgkmcnt(0)\n" \
  "s_waitcnt vmcnt(0)\n" \
  "s_barrier\n" \
  "v_mfma_f32_16x16x32_bf16 v[0:3], a[96:99], a[64:67], v[0:3]\n" \
  "ds_read_b64 v[64:65], v144 offset:0\n" \
  "ds_read_b64 v[80:81], v144 offset:16384\n" \
  "ds_read_b64 v[66:67], v144 offset:4096\n" \
  "ds_read_b64 v[82:83], v144 offset:20480\n" \
  "v_mfma_f32_16x16x32_bf16 v[0:3], a[100:103], a[68:71], v[0:3]\n" \
  "ds_read_b64 v[68:69], v145 offset:0\n" \
  "ds_read_b64 v[84:85], v145 offset:16384\n" \
  "ds_read_b64 v[70:71], v145 offset:4096\n" \
  "ds_read_b64 v[86:87], v145 offset:20480\n" \
  "v_mfma_f32_16x16x32_bf16 v[4:7], a[100:103], a[76:79], v[4:7]\n" \
  "ds_read_b64 v[72:73], v146 offset:0\n" \
  "ds_read_b64 v[88:89], v146 offset:16384\n" \
  "ds_read_b64 v[74:75], v146 offset:4096\n" \
  "ds_read_b64 v[90:91], v146 offset:20480\n" \
  "v_mfma_f32_16x16x32_bf16 v[4:7], a[96:99], a[72:75], v[4:7]\n" \
  "ds_read_b64 v[76:77], v147 offset:0\n" \
  "ds_read_b64 v[92:93], v147 offset:16384\n" \
  "ds_read_b64 v[78:79], v147 offset:4096\n" \
  "ds_read_b64 v[94:95], v147 offset:20480\n" \
  "v_mfma_f32_16x16x32_bf16 v[8:11], a[96:99], a[80:83], v[8:11]\n" \
  "v_mfma_f32_16x16x32_bf16 v[8:11], a[100:103], a[84:87], v[8:11]\n" \
  "v_mfma_f32_16x16x32_bf16 v[12:15], a[100:103], a[92:95], v[12:15]\n" \
  "v_mfma_f32_16x16x32_bf16 v[12:15], a[96:99], a[88:91], v[12:15]\n" \
  "v_mfma_f32_16x16x32_bf16 v[16:19], a[104:107], a[64:67], v[16:19]\n" \
  "v_mfma_f32_16x16x32_bf16 v[16:19], a[108:111], a[68:71], v[16:19]\n" \
  "v_mfma_f32_16x16x32_bf16 v[20:23], a[108:111], a[76:79], v[20:23]\n" \
  "v_mfma_f32_16x16x32_bf16 v[20:23], a[104:107], a[72:75], v[20:23]\n" \
  "v_mfma_f32_16x16x32_bf16 v[24:27], a[104:107], a[80:83], v[24:27]\n" \
  "v_mfma_f32_16x16x32_bf16 v[24:27], a[108:111], a[84:87], v[24:27]\n" \
  "v_mfma_f32_16x16x32_bf16 v[28:31], a[108:111], a[92:95], v[28:31]\n" \
  "v_mfma_f32_16x16x32_bf16 v[28:31], a[104:107], a[88:91], v[28:31]\n" \
  "v_mfma_f32_16x16x32_bf16 v[32:35], a[112:115], a[64:67], v[32:35]\n" \
  "v_mfma_f32_16x16x32_bf16 v[32:35], a[116:119], a[68:71], v[32:35]\n" \
  "v_mfma_f32_16x16x32_bf16 v[36:39], a[116:119], a[76:79], v[36:39]\n" \
  "v_mfma_f32_16x16x32_bf16 v[36:39], a[112:115], a[72:75], v[36:39]\n" \
  "v_mfma_f32_16x16x32_bf16 v[40:43], a[112:115], a[80:83], v[40:43]\n" \
  "v_mfma_f32_16x16x32_bf16 v[40:43], a[116:119], a[84:87], v[40:43]\n" \
  "v_mfma_f32_16x16x32_bf16 v[44:47], a[116:119], a[92:95], v[44:47]\n" \
  "v_mfma_f32_16x16x32_bf16 v[44:47], a[112:115], a[88:91], v[44:47]\n" \
  "v_mfma_f32_16x16x32_bf16 v[48:51], a[120:123], a[64:67], v[48:51]\n" \
  "v_mfma_f32_16x16x32_bf16 v[48:51], a[124:127], a[68:71], v[48:51]\n" \
  "v_mfma_f32_16x16x32_bf16 v[52:55], a[124:127], a[76:79], v[52:55]\n" \
  "v_mfma_f32_16x16x32_bf16 v[52:55], a[120:123], a[72:75], v[52:55]\n" \
  "v_mfma_f32_16x16x32_bf16 v[56:59], a[120:123], a[80:83], v[56:59]\n" \
  "v_mfma_f32_16x16x32_bf16 v[56:59], a[124:127], a[84:87], v[56:59]\n" \
  "v_mfma_f32_16x16x32_bf16 v[60:63], a[124:127], a[92:95], v[60:63]\n" \
  "v_mfma_f32_16x16x32_bf16 v[60:63], a[120:123], a[88:91], v[60:63]\n" \
  "buffer_store_dwordx4 v[8:11], v161, s[52:55], s5 offen sc1\n" \
  "buffer_store_dwordx4 v[12:15], v161, s[52:55], s5 offen offset:1024 sc1\n" \
  "buffer_store_dwordx4 v[24:27], v161, s[52:55], s5 offen offset:2048 sc1\n" \
  "buffer_store_dwordx4 v[28:31], v161, s[52:55], s5 offen offset:3072 sc1\n" \
  "s_add_u32 s5, s5, 4096\n" \
  "buffer_store_dwordx4 v[40:43], v161, s[52:55], s5 offen sc1\n" \
  "buffer_store_dwordx4 v[44:47], v161, s[52:55], s5 offen offset:1024 sc1\n" \
  "buffer_store_dwordx4 v[56:59], v161, s[52:55], s5 offen offset:2048 sc1\n" \
  "buffer_store_dwordx4 v[60:63], v161, s[52:55], s5 offen offset:3072 sc1\n" \
  "s_waitcnt lgkmcnt(15)\n" \
  "v_lshlrev_b32 v112, 16, v64\n" \
  "v_and_b32 v113, 0xffff0000, v64\n" \
  "s_waitcnt lgkmcnt(14)\n" \
  "v_lshlrev_b32 v162, 16, v80\n" \
  "v_and_b32 v163, 0xffff0000, v80\n" \
  "v_fmac_f32 v112, v162, v96\n" \
  "v_fmac_f32 v113, v163, v97\n" \
  "v_lshlrev_b32 v114, 16, v65\n" \
  "v_and_b32 v115, 0xffff0000, v65\n" \
  "v_lshlrev_b32 v162, 16, v81\n" \
  "v_and_b32 v163, 0xffff0000, v81\n" \
  "v_fmac_f32 v114, v162, v98\n" \
  "v_fmac_f32 v115, v163, v99\n" \
  "s_waitcnt lgkmcnt(13)\n" \
  "v_lshlrev_b32 v116, 16, v66\n" \
  "v_and_b32 v117, 0xffff0000, v66\n" \
  "s_waitcnt lgkmcnt(12)\n" \
  "v_lshlrev_b32 v162, 16, v82\n" \
  "v_and_b32 v163, 0xffff0000, v82\n" \
  "v_fmac_f32 v116, v162, v96\n" \
  "v_fmac_f32 v117, v163, v97\n" \
  "v_lshlrev_b32 v118, 16, v67\n" \
  "v_and_b32 v119, 0xffff0000, v67\n" \
  "v_lshlrev_b32 v162, 16, v83\n" \
  "v_and_b32 v163, 0xffff0000, v83\n" \
  "v_fmac_f32 v118, v162, v98\n" \
  "v_fmac_f32 v119, v163, v99\n" \
  "s_waitcnt lgkmcnt(11)\n" \
  "v_lshlrev_b32 v120, 16, v68\n" \
  "v_and_b32 v121, 0xffff0000, v68\n" \
  "s_waitcnt lgkmcnt(10)\n" \
  "v_lshlrev_b32 v162, 16, v84\n" \
  "v_and_b32 v163, 0xffff0000, v84\n" \
  "v_fmac_f32 v120, v162, v100\n" \
  "v_fmac_f32 v121, v163, v101\n" \
  "v_lshlrev_b32 v122, 16, v69\n" \
  "v_and_b32 v123, 0xffff0000, v69\n" \
  "v_lshlrev_b32 v162, 16, v85\n" \
  "v_and_b32 v163, 0xffff0000, v85\n" \
  "v_fmac_f32 v122, v162, v102\n" \
  "v_fmac_f32 v123, v163, v103\n" \
  "s_waitcnt lgkmcnt(9)\n" \
  "v_lshlrev_b32 v124, 16, v70\n" \
  "v_and_b32 v125, 0xffff0000, v70\n" \
  "s_waitcnt lgkmcnt(8)\n" \
  "v_lshlrev_b32 v162, 16, v86\n" \
  "v_and_b32 v163, 0xffff0000, v86\n" \
  "v_fmac_f32 v124, v162, v100\n" \
  "v_fmac_f32 v125, v163, v101\n" \
  "v_lshlrev_b32 v126, 16, v71\n" \
  "v_and_b32 v127, 0xffff0000, v71\n" \
  "v_lshlrev_b32 v162, 16, v87\n" \
  "v_and_b32 v163, 0xffff0000, v87\n" \
  "v_fmac_f32 v126, v162, v102\n" \
  "v_fmac_f32 v127, v163, v103\n" \
  "s_waitcnt lgkmcnt(7)\n" \
  "v_lshlrev_b32 v128, 16, v72\n" \
  "v_and_b32 v129, 0xffff0000, v72\n" \
  "s_waitcnt lgkmcnt(6)\n" \
  "v_lshlrev_b32 v162, 16, v88\n" \
  "v_and_b32 v163, 0xffff0000, v88\n" \
  "v_fmac_f32 v128, v162, v104\n" \
  "v_fmac_f32 v129, v163, v105\n" \
  "v_lshlrev_b32 v130, 16, v73\n" \
  "v_and_b32 v131, 0xffff0000, v73\n" \
  "v_lshlrev_b32 v162, 16, v89\n" \
  "v_and_b32 v163, 0xffff0000, v89\n" \
  "v_fmac_f32 v130, v162, v106\n" \
  "v_fmac_f32 v131, v163, v107\n" \
  "s_waitcnt lgkmcnt(5)\n" \
  "v_lshlrev_b32 v132, 16, v74\n" \
  "v_and_b32 v133, 0xffff0000, v74\n" \
  "s_waitcnt lgkmcnt(4)\n" \
  "v_lshlrev_b32 v162, 16, v90\n" \
  "v_and_b32 v163, 0xffff0000, v90\n" \
  "v_fmac_f32 v132, v162, v104\n" \
  "v_fmac_f32 v133, v163, v105\n" \
  "v_lshlrev_b32 v134, 16, v75\n" \
  "v_and_b32 v135, 0xffff0000, v75\n" \
  "v_lshlrev_b32 v162, 16, v91\n" \
  "v_and_b32 v163, 0xffff0000, v91\n" \
  "v_fmac_f32 v134, v162, v106\n" \
  "v_fmac_f32 v135, v163, v107\n" \
  "s_waitcnt lgkmcnt(3)\n" \
  "v_lshlrev_b32 v136, 16, v76\n" \
  "v_and_b32 v137, 0xffff0000, v76\n" \
  "s_waitcnt lgkmcnt(2)\n" \
  "v_lshlrev_b32 v162, 16, v92\n" \
  "v_and_b32 v163, 0xffff0000, v92\n" \
  "v_fmac_f32 v136, v162, v108\n" \
  "v_fmac_f32 v137, v163, v109\n" \
  "v_lshlrev_b32 v138, 16, v77\n" \
  "v_and_b32 v139, 0xffff0000, v77\n" \
  "v_lshlrev_b32 v162, 16, v93\n" \
  "v_and_b32 v163, 0xffff0000, v93\n" \
  "v_fmac_f32 v138, v162, v110\n" \
  "v_fmac_f32 v139, v163, v111\n" \
  "s_waitcnt lgkmcnt(1)\n" \
  "v_lshlrev_b32 v140, 16, v78\n" \
  "v_and_b32 v141, 0xffff0000, v78\n" \
  "s_waitcnt lgkmcnt(0)\n" \
  "v_lshlrev_b32 v162, 16, v94\n" \
  "v_and_b32 v163, 0xffff0000, v94\n" \
  "v_fmac_f32 v140, v162, v108\n" \
  "v_fmac_f32 v141, v163, v109\n" \
  "v_lshlrev_b32 v142, 16, v79\n" \
  "v_and_b32 v143, 0xffff0000, v79\n" \
  "v_lshlrev_b32 v162, 16, v95\n" \
  "v_and_b32 v163, 0xffff0000, v95\n" \
  "v_fmac_f32 v142, v162, v110\n" \
  "v_fmac_f32 v143, v163, v111\n" \
  "v_add_f32 v0, v0, v112\n" \
  "v_add_f32 v1, v1, v113\n" \
  "v_add_f32 v2, v2, v114\n" \
  "v_add_f32 v3, v3, v115\n" \
  "v_add_f32 v4, v4, v116\n" \
  "v_add_f32 v5, v5, v117\n" \
  "v_add_f32 v6, v6, v118\n" \
  "v_add_f32 v7, v7, v119\n" \
  "v_add_f32 v16, v16, v120\n" \
  "v_add_f32 v17, v17, v121\n" \
  "v_add_f32 v18, v18, v122\n" \
  "v_add_f32 v19, v19, v123\n" \
  "v_add_f32 v20, v20, v124\n" \
  "v_add_f32 v21, v21, v125\n" \
  "v_add_f32 v22, v22, v126\n" \
  "v_add_f32 v23, v23, v127\n" \
  "v_add_f32 v32, v32, v128\n" \
  "v_add_f32 v33, v33, v129\n" \
  "v_add_f32 v34, v34, v130\n" \
  "v_add_f32 v35, v35, v131\n" \
  "v_add_f32 v36, v36, v132\n" \
  "v_add_f32 v37, v37, v133\n" \
  "v_add_f32 v38, v38, v134\n" \
  "v_add_f32 v39, v39, v135\n" \
  "v_add_f32 v48, v48, v136\n" \
  "v_add_f32 v49, v49, v137\n" \
  "v_add_f32 v50, v50, v138\n" \
  "v_add_f32 v51, v51, v139\n" \
  "v_add_f32 v52, v52, v140\n" \
  "v_add_f32 v53, v53, v141\n" \
  "v_add_f32 v54, v54, v142\n" \
  "v_add_f32 v55, v55, v143\n" \
  "s_xor_b32 s5, s5, 36864\n" \
  "s_add_u32 s69, s3, 65536\n" \
  "s_waitcnt vmcnt(0)\n" \
  "buffer_store_dwordx2 v[254:255], v237, s[52:55], s7 offen sc1\n" \
  "s_xor_b32 s7, s7, 256\n" \
  "buffer_load_dwordx2 v[252:253], off, s[52:55], s7 sc1\n" \
  "s_waitcnt vmcnt(0)\n" \
  "v_xor_b32 v252, v252, v254\n" \
  "v_xor_b32 v253, v253, v255\n" \
  "v_or_b32 v252, v252, v253\n" \
  "v_cmp_ne_u32 vcc, 0, v252\n" \
  "s_nop 4\n" \
  "s_cbranch_vccnz Lslow6_%=\n" \
  "Lres6_%=:\n" \
  "buffer_load_dwordx4 v[64:67], v161, s[52:55], s5 offen sc1\n" \
  "buffer_load_dwordx4 v[68:71], v161, s[52:55], s5 offen offset:1024 sc1\n" \
  "buffer_load_dwordx4 v[72:75], v161, s[52:55], s5 offen offset:2048 sc1\n" \
  "buffer_load_dwordx4 v[76:79], v161, s[52:55], s5 offen offset:3072 sc1\n" \
  "s_add_u32 s5, s5, 4096\n" \
  "buffer_load_dwordx4 v[80:83], v161, s[52:55], s5 offen sc1\n" \
  "buffer_load_dwordx4 v[84:87], v161, s[52:55], s5 offen offset:1024 sc1\n" \
  "buffer_load_dwordx4 v[88:91], v161, s[52:55], s5 offen offset:2048 sc1\n" \
  "buffer_load_dwordx4 v[92:95], v161, s[52:55], s5 offen offset:3072 sc1\n" \
  "s_waitcnt vmcnt(7)\n" \
  "v_add_f32 v0, v0, v64\n" \
  "v_add_f32 v1, v1, v65\n" \
  "v_add_f32 v2, v2, v66\n" \
  "v_add_f32 v3, v3, v67\n" \
  "buffer_store_dwordx4 v[0:3], v160, s[64:67], s3 offen\n" \
  "s_waitcnt vmcnt(7)\n" \
  "v_add_f32 v4, v4, v68\n" \
  "v_add_f32 v5, v5, v69\n" \
  "v_add_f32 v6, v6, v70\n" \
  "v_add_f32 v7, v7, v71\n" \
  "buffer_store_dwordx4 v[4:7], v160, s[64:67], s69 offen\n" \
  "s_waitcnt vmcnt(7)\n" \
  "v_add_f32 v16, v16, v72\n" \
  "v_add_f32 v17, v17, v73\n" \
  "v_add_f32 v18, v18, v74\n" \
  "v_add_f32 v19, v19, v75\n" \
  "buffer_store_dwordx4 v[16:19], v160, s[64:67], s3 offen offset:64\n" \
  "s_waitcnt vmcnt(7)\n" \
  "v_add_f32 v20, v20, v76\n" \
  "v_add_f32 v21, v21, v77\n" \
  "v_add_f32 v22, v22, v78\n" \
  "v_add_f32 v23, v23, v79\n" \
  "buffer_store_dwordx4 v[20:23], v160, s[64:67], s69 offen offset:64\n" \
  "s_waitcnt vmcnt(7)\n" \
  "v_add_f32 v32, v32, v80\n" \
  "v_add_f32 v33, v33, v81\n" \
  "v_add_f32 v34, v34, v82\n" \
  "v_add_f32 v35, v35, v83\n" \
  "buffer_store_dwordx4 v[32:35], v160, s[64:67], s3 offen offset:128\n" \
  "s_waitcnt vmcnt(7)\n" \
  "v_add_f32 v36, v36, v84\n" \
  "v_add_f32 v37, v37, v85\n" \
  "v_add_f32 v38, v38, v86\n" \
  "v_add_f32 v39, v39, v87\n" \
  "buffer_store_dwordx4 v[36:39], v160, s[64:67], s69 offen offset:128\n" \
  "s_waitcnt vmcnt(7)\n" \
  "v_add_f32 v48, v48, v88\n" \
  "v_add_f32 v49, v49, v89\n" \
  "v_add_f32 v50, v50, v90\n" \
  "v_add_f32 v51, v51, v91\n" \
  "buffer_store_dwordx4 v[48:51], v160, s[64:67], s3 offen offset:192\n" \
  "s_waitcnt vmcnt(7)\n" \
  "v_add_f32 v52, v52, v92\n" \
  "v_add_f32 v53, v53, v93\n" \
  "v_add_f32 v54, v54, v94\n" \
  "v_add_f32 v55, v55, v95\n" \
  "buffer_store_dwordx4 v[52:55], v160, s[64:67], s69 offen offset:192\n" \
  "s_branch Lend_%=\n" \
  "Lslow0_%=:\n" \
  "s_mov_b32 s87, 0x20000\n" \
  "Lspin0_%=:\n" \
  "s_sleep 1\n" \
  "buffer_load_dwordx4 v[156:159], v221, s[52:55], 0 offen sc1\n" \
  "buffer_load_dwordx4 v[160:163], v221, s[52:55], 0 offen offset:256 sc1\n" \
  "buffer_load_dwordx4 v[164:167], v221, s[52:55], s82 offen sc1\n" \
  "buffer_load_dwordx4 v[168:171], v221, s[52:55], s82 offen offset:256 sc1\n" \
  "s_waitcnt vmcnt(0)\n" \
  "v_xor_b32 v157, v157, v254\n" \
  "v_xor_b32 v159, v159, v254\n" \
  "v_xor_b32 v161, v161, v254\n" \
  "v_xor_b32 v163, v163, v254\n" \
  "v_xor_b32 v165, v165, v254\n" \
  "v_xor_b32 v167, v167, v254\n" \
  "v_xor_b32 v169, v169, v254\n" \
  "v_xor_b32 v171, v171, v254\n" \
  "v_or3_b32 v212, v157, v159, v165\n" \
  "v_or3_b32 v213, v161, v163, v169\n" \
  "v_or_b32 v212, v212, v167\n" \
  "v_or_b32 v213, v213, v171\n" \
  "v_min_u32 v212, 1, v212\n" \
  "v_min_u32 v213, 1, v213\n" \
  "v_lshl_or_b32 v212, v213, 1, v212\n" \
  "v_and_b32 v212, v212, v223\n" \
  "v_cmp_ne_u32 vcc, 0, v212\n" \
  "s_nop 4\n" \
  "s_cbranch_vccz Lres0_%=\n" \
  "s_sub_u32 s87, s87, 1\n" \
  "s_cmp_lg_u32 s87, 0\n" \
  "s_cbranch_scc1 Lspin0_%=\n" \
  "s_branch Lres0_%=\n" \
  "Lslow1_%=:\n" \
  "s_mov_b32 s87, 0x20000\n" \
  "Lspin1_%=:\n" \
  "s_sleep 1\n" \
  "v_accvgpr_read_b32 v253, a212\n" \
  "buffer_load_dwordx2 v[252:253], v253, s[52:55], s71 offen sc1\n" \
  "s_waitcnt vmcnt(0)\n" \
  "v_xor_b32 v252, v252, v254\n" \
  "v_xor_b32 v253, v253, v255\n" \
  "v_or_b32 v252, v252, v253\n" \
  "v_cmp_ne_u32 vcc, 0, v252\n" \
  "s_nop 4\n" \
  "s_cbranch_vccz Lres1_%=\n" \
  "s_sub_u32 s87, s87, 1\n" \
  "s_cmp_lg_u32 s87, 0\n" \
  "s_cbranch_scc1 Lspin1_%=\n" \
  "s_branch Lres1_%=\n" \
  "Lslow2_%=:\n" \
  "s_mov_b32 s87, 0x20000\n" \
  "Lspin2_%=:\n" \
  "s_sleep 1\n" \
  "v_accvgpr_read_b32 v253, a212\n" \
  "buffer_load_dwordx2 v[252:253], v253, s[52:55], s1 offen sc1\n" \
  "s_waitcnt vmcnt(0)\n" \
  "v_xor_b32 v252, v252, v254\n" \
  "v_xor_b32 v253, v253, v255\n" \
  "v_or_b32 v252, v252, v253\n" \
  "v_cmp_ne_u32 vcc, 0, v252\n" \
  "s_nop 4\n" \
  "s_cbranch_vccz Lres2_%=\n" \
  "s_sub_u32 s87, s87, 1\n" \
  "s_cmp_lg_u32 s87, 0\n" \
  "s_cbranch_scc1 Lspin2_%=\n" \
  "s_branch Lres2_%=\n" \
  "Lslow3_%=:\n" \
  "s_mov_b32 s87, 0x20000\n" \
  "Lspin3_%=:\n" \
  "s_sleep 1\n" \
  "v_accvgpr_read_b32 v253, a212\n" \
  "buffer_load_dwordx2 v[252:253], v253, s[52:55], s1 offen sc1\n" \
  "s_waitcnt vmcnt(0)\n" \
  "v_xor_b32 v252, v252, v254\n" \
  "v_xor_b32 v253, v253, v255\n" \
  "v_or_b32 v252, v252, v253\n" \
  "v_cmp_ne_u32 vcc, 0, v252\n" \
  "s_nop 4\n" \
  "s_cbranch_vccz Lres3_%=\n" \
  "s_sub_u32 s87, s87, 1\n" \
  "s_cmp_lg_u32 s87, 0\n" \
  "s_cbranch_scc1 Lspin3_%=\n" \
  "s_branch Lres3_%=\n" \
  "Lslow4_%=:\n" \
  "s_mov_b32 s87, 0x20000\n" \
  "Lspin4_%=:\n" \
  "s_sleep 1\n" \
  "v_accvgpr_read_b32 v253, a218\n" \
  "buffer_load_dwordx2 v[252:253], v253, s[52:55], 0 offen sc1\n" \
  "s_waitcnt vmcnt(0)\n" \
  "v_xor_b32 v252, v252, v254\n" \
  "v_xor_b32 v253, v253, v255\n" \
  "v_or_b32 v252, v252, v253\n" \
  "v_cmp_ne_u32 vcc, 0, v252\n" \
  "s_nop 4\n" \
  "s_cbranch_vccz Lres4_%=\n" \
  "s_sub_u32 s87, s87, 1\n" \
  "s_cmp_lg_u32 s87, 0\n" \
  "s_cbranch_scc1 Lspin4_%=\n" \
  "s_branch Lres4_%=\n" \
  "Lslow5_%=:\n" \
  "s_mov_b32 s87, 0x20000\n" \
  "Lspin5_%=:\n" \
  "s_sleep 1\n" \
  "v_accvgpr_read_b32 v253, a212\n" \
  "buffer_load_dwordx2 v[252:253], v253, s[52:55], s1 offen sc1\n" \
  "s_waitcnt vmcnt(0)\n" \
  "v_xor_b32 v252, v252, v254\n" \
  "v_xor_b32 v253, v253, v255\n" \
  "v_or_b32 v252, v252, v253\n" \
  "v_cmp_ne_u32 vcc, 0, v252\n" \
  "s_nop 4\n" \
  "s_cbranch_vccz Lres5_%=\n" \
  "s_sub_u32 s87, s87, 1\n" \
  "s_cmp_lg_u32 s87, 0\n" \
  "s_cbranch_scc1 Lspin5_%=\n" \
  "s_branch Lres5_%=\n" \
  "Lslow6_%=:\n" \
  "s_mov_b32 s87, 0x20000\n" \
  "Lspin6_%=:\n" \
  "s_sleep 1\n" \
  "buffer_load_dwordx2 v[252:253], off, s[52:55], s7 sc1\n" \
  "s_waitcnt vmcnt(0)\n" \
  "v_xor_b32 v252, v252, v254\n" \
  "v_xor_b32 v253, v253, v255\n" \
  "v_or_b32 v252, v252, v253\n" \
  "v_cmp_ne_u32 vcc, 0, v252\n" \
  "s_nop 4\n" \
  "s_cbranch_vccz Lres6_%=\n" \
  "s_sub_u32 s87, s87, 1\n" \
  "s_cmp_lg_u32 s87, 0\n" \
  "s_cbranch_scc1 Lspin6_%=\n" \
  "s_branch Lres6_%=\n" \
  "Lend_%=:\n"

#define P2_K0 1920
#define CODE_BYTES 49692
#define CLOB_V "v0","v1","v2","v3","v4","v5","v6","v7","v8","v9","v10","v11","v12","v13","v14","v15","v16","v17","v18","v19","v20","v21","v22","v23","v24","v25","v26","v27","v28","v29","v30","v31","v32","v33","v34","v35","v36","v37","v38","v39","v40","v41","v42","v43","v44","v45","v46","v47","v48","v49","v50","v51","v52","v53","v54","v55","v56","v57","v58","v59","v60","v61","v62","v63","v64","v65","v66","v67","v68","v69","v70","v71","v72","v73","v74","v75","v76","v77","v78","v79","v80","v81","v82","v83","v84","v85","v86","v87","v88","v89","v90","v91","v92","v93","v94","v95","v96","v97","v98","v99","v100","v101","v102","v103","v104","v105","v106","v107","v108","v109","v110","v111","v112","v113","v114","v115","v116","v117","v118","v119","v120","v121","v122","v123","v124","v125","v126","v127","v128","v129","v130","v131","v132","v133","v134","v135","v136","v137","v138","v139","v140","v141","v142","v143","v144","v145","v146","v147","v148","v149","v150","v151","v152","v153","v154","v155","v156","v157","v158","v159","v160","v161","v162","v163","v164","v165","v166","v167","v168","v169","v170","v171","v172","v173","v174","v175","v176","v177","v178","v179","v180","v181","v182","v183","v184","v185","v186","v187","v188","v189","v190","v191","v192","v193","v194","v195","v196","v197","v198","v199","v200","v201","v202","v203","v204","v205","v206","v207","v208","v209","v210","v211","v212","v213","v214","v215","v216","v217","v218","v219","v220","v221","v222","v223"
#define CLOB_A "a0","a1","a2","a3","a4","a5","a6","a7","a8","a9","a10","a11","a12","a13","a14","a15","a16","a17","a18","a19","a20","a21","a22","a23","a24","a25","a26","a27","a28","a29","a30","a31","a32","a33","a34","a35","a36","a37","a38","a39","a40","a41","a42","a43","a44","a45","a46","a47","a48","a49","a50","a51","a52","a53","a54","a55","a56","a57","a58","a59","a60","a61","a62","a63","a64","a65","a66","a67","a68","a69","a70","a71","a72","a73","a74","a75","a76","a77","a78","a79","a80","a81","a82","a83","a84","a85","a86","a87","a88","a89","a90","a91","a92","a93","a94","a95","a96","a97","a98","a99","a100","a101","a102","a103","a104","a105","a106","a107","a108","a109","a110","a111","a112","a113","a114","a115","a116","a117","a118","a119","a120","a121","a122","a123","a124","a125","a126","a127","a128","a129","a130","a131","a132","a133","a134","a135","a136","a137","a138","a139","a140","a141","a142","a143","a144","a145","a146","a147","a148","a149","a150","a151","a152","a153","a154","a155","a156","a157","a158","a159","a160","a161","a162","a163","a164","a165","a166","a167","a168","a169","a170","a171","a172","a173","a174","a175","a176","a177","a178","a179","a180","a181","a182","a183","a184","a185","a186","a187","a188","a189","a190","a191"
typedef unsigned short u16;
typedef unsigned u32x2 __attribute__((ext_vector_type(2)));
typedef unsigned u32x4 __attribute__((ext_vector_type(4)));
typedef unsigned u32x8 __attribute__((ext_vector_type(8)));
typedef unsigned u32x16 __attribute__((ext_vector_type(16)));
typedef unsigned u32x32 __attribute__((ext_vector_type(32)));

constexpr int L_ = 2048, D_ = 1024, P_ = 1024, F_ = 3584;
constexpr unsigned MI_XF = 1024, MI_HF = 8192, MI_SF = 24576, MI_FLAG = 32768, MI_AGG = 524288, MI_EX = 1048576;
constexpr size_t WS_XN = (size_t)MI_EX + (8u << 20);
constexpr size_t WS_HT = WS_XN + (size_t)L_ * D_ * 2;
constexpr size_t WS_ST = WS_HT + (size_t)L_ * F_ * 2;
constexpr unsigned HROW = 114688, OOB = 0x80000000u;
constexpr int LDS_BYTES = 163840;

__device__ __forceinline__ float bf2f(unsigned v) { return __uint_as_float(v << 16); }
__device__ __forceinline__ unsigned f2bf(float f) {
  unsigned u = __float_as_uint(f);
  u += 0x7FFF + ((u >> 16) & 1);
  return u >> 16;
}
__device__ __forceinline__ float rbf(float f) { return bf2f(f2bf(f)); }
__device__ __forceinline__ unsigned pkbf(float lo, float hi) {
  typedef float f32x2 __attribute__((ext_vector_type(2)));
  typedef __bf16 bf16x2 __attribute__((ext_vector_type(2)));
  return __builtin_bit_cast(unsigned, __builtin_convertvector((f32x2{lo, hi}), bf16x2));
}
__device__ __forceinline__ float rbf2(float f) { return __uint_as_float(pkbf(f, f) << 16); }
template <int CTRL> __device__ __forceinline__ float dpp_add(float v) {
  return v + __uint_as_float(__builtin_amdgcn_update_dpp(0u, __float_as_uint(v), CTRL, 0xf, 0xf, true));
}
__device__ __forceinline__ float wave_sum(float v) {
  v = dpp_add<0xB1>(v);
  v = dpp_add<0x4E>(v);
  v = dpp_add<0x141>(v);
  v = dpp_add<0x140>(v);
  auto a = __builtin_amdgcn_permlane16_swap(__float_as_uint(v), __float_as_uint(v), false, false);
  v = __uint_as_float(a[0]) + __uint_as_float(a[1]);
  auto b = __builtin_amdgcn_permlane32_swap(__float_as_uint(v), __float_as_uint(v), false, false);
  return __uint_as_float(b[0]) + __uint_as_float(b[1]);
}
__device__ __forceinline__ unsigned uni(unsigned v) { return __builtin_amdgcn_readfirstlane(v); }
__device__ __forceinline__ u32x4 mk_srd(const void* base, unsigned off, unsigned total) {
  const size_t p = (size_t)base + off;
  return u32x4{uni((unsigned)p), uni((unsigned)(p >> 32) & 0xffffu), uni(total - off), 0x20000u};
}
__device__ __forceinline__ unsigned swap23(unsigned x) { return (x & ~12u) | ((x & 4) << 1) | ((x & 8) >> 1); }
struct cf { float r, i; };
__device__ __forceinline__ cf cmul(cf a, cf b) { return cf{a.r * b.r - a.i * b.i, a.r * b.i + a.i * b.r}; }
__device__ __forceinline__ cf csel(bool c, cf a, cf b) { return cf{c ? a.r : b.r, c ? a.i : b.i}; }

typedef float f32x2 __attribute__((ext_vector_type(2)));
__device__ __forceinline__ void ln_rows(u32x4 (&a)[4]) {
  f32x2 v[2][8], s[2], q[2];
#pragma unroll
  for (int r = 0; r < 2; ++r) {
#pragma unroll
    for (int i = 0; i < 8; ++i) { const unsigned u = a[2 * r + (i >> 2)][i & 3]; v[r][i] = f32x2{__uint_as_float(u << 16), __uint_as_float(u & 0xffff0000u)}; }
    s[r] = ((v[r][0] + v[r][1]) + (v[r][2] + v[r][3])) + ((v[r][4] + v[r][5]) + (v[r][6] + v[r][7]));
  }
  float mean[2], var[2];
#pragma unroll
  for (int r = 0; r < 2; ++r) mean[r] = wave_sum(s[r][0] + s[r][1]) * (1.0f / D_);
#pragma unroll
  for (int r = 0; r < 2; ++r) {
    q[r] = f32x2{0.f, 0.f};
#pragma unroll
    for (int i = 0; i < 8; ++i) { const f32x2 d = v[r][i] - mean[r]; q[r] = __builtin_elementwise_fma(d, d, q[r]); }
  }
#pragma unroll
  for (int r = 0; r < 2; ++r) var[r] = wave_sum(q[r][0] + q[r][1]) * (1.0f / D_);
#pragma unroll
  for (int r = 0; r < 2; ++r) {
    const float mb = rbf2(mean[r]), sd = rbf2(__builtin_amdgcn_sqrtf(rbf2(rbf2(var[r]) + 1e-5f)));
    float rs = __builtin_amdgcn_rcpf(sd);
    rs = fmaf(fmaf(-sd, rs, 1.0f), rs, rs);
#pragma unroll
    for (int i = 0; i < 8; ++i) {
      const f32x2 d = v[r][i] - mb;
      const unsigned cc = pkbf(d[0], d[1]);
      const f32x2 n = f32x2{__uint_as_float(cc << 16), __uint_as_float(cc & 0xffff0000u)} * rs;
      a[2 * r + (i >> 2)][i & 3] = pkbf(n[0], n[1]);
    }
  }
}
__device__ __forceinline__ float exp_f(float x) {
  const float hi = x * 1.44269504088896341f;
  const float lo = fmaf(x, 1.44269504088896341f, -hi) + x * 1.92596299112661746e-8f;
  return __builtin_amdgcn_exp2f(hi) * fmaf(lo, 0.693147180559945309f, 1.0f);
}
__device__ __forceinline__ cf cis(float x) {
  const float k = __builtin_rintf(x * 0.636619772367581343f);
  float r = fmaf(k, -1.57079601287841796875f, x);
  r = fmaf(k, -3.13916473533982038e-7f, r);
  r = fmaf(k, -5.39030252995776476e-15f, r);
  const float z = r * r;
  const float sn = fmaf(r * z, fmaf(z, fmaf(z, fmaf(z, 2.86567956e-6f, -1.98559923e-4f), 8.33338592e-3f), -1.66666672e-1f), r);
  const float cs = fmaf(z, fmaf(z, fmaf(z, fmaf(z, 2.44677067e-5f, -1.38877297e-3f), 4.16666567e-2f), -0.5f), 1.0f);
  const int qd = (int)k;
  const float a = (qd & 1) ? sn : cs, b = (qd & 1) ? cs : sn;
  return cf{((qd + 1) & 2) ? -a : a, (qd & 2) ? -b : b};
}
__device__ __forceinline__ cf csq(cf a) { return cf{fmaf(a.r, a.r, -a.i * a.i), (a.r + a.r) * a.i}; }

struct Args { const u16 *x, *lre, *lim, *logdt, *wg, *wu, *dsk; char* ws; const u16 *bre, *bim, *cre, *cim, *wd; float* out; };

__global__ __launch_bounds__(256, 1) void fused_kernel(Args ar) {
  const u16 *x = ar.x, *lre = ar.lre, *lim = ar.lim, *logdt = ar.logdt, *wg = ar.wg, *wu = ar.wu, *dsk = ar.dsk;
  const u16 *bre = ar.bre, *bim = ar.bim, *cre = ar.cre, *cim = ar.cim, *wd = ar.wd;
  char* ws = ar.ws;
  asm volatile("" : "+s"(x), "+s"(lre), "+s"(lim), "+s"(logdt), "+s"(wg), "+s"(wu), "+s"(dsk), "+s"(ws), "+s"(bre), "+s"(bim), "+s"(cre), "+s"(cim), "+s"(wd));
  const unsigned tid = threadIdx.x;
  unsigned l = tid & 63;
  unsigned w = __builtin_amdgcn_readfirstlane(tid >> 6);
  asm volatile("" : "+s"(w));
  const unsigned w0 = w & 1, w1 = w >> 1;
  const unsigned bid = blockIdx.x, xcd = bid & 7, idx = bid >> 3, ra = xcd >> 2, qb = xcd & 3;
  const unsigned pm = 4 * ra + (idx & 3), pn = 8 * qb + (idx >> 2);
  const unsigned mb = 8 * ra + (idx & 7), sb = 4 * qb + (idx >> 3);
  const unsigned nb = 4 * (qb & 1) + (idx >> 3), h = qb >> 1;
  constexpr unsigned SZ_X = L_ * D_ * 2, SZ_W = D_ * F_ * 2, SZ_B = P_ * D_ * 2, SZ_H = L_ * F_ * 2, SZ_S = L_ * P_ * 4;
  const char* xn = ws + WS_XN;

  const unsigned xrow = (256 * pm + 8 * pn + 2 * w) * 2048;
  u32x4 xr[4];
  unsigned ep, pl[6];
  u32x2 dk[4];
  {
    const u32x4 sx = mk_srd(x, xrow, SZ_X);
    asm volatile("buffer_load_dwordx4 %0, %4, %5, 0 offen\n buffer_load_dwordx4 %1, %4, %5, 0 offen offset:1024\n"
                 "buffer_load_dwordx4 %2, %4, %5, 0 offen offset:2048\n buffer_load_dwordx4 %3, %4, %5, 0 offen offset:3072\n s_barrier"
                 : "=&v"(xr[0]), "=&v"(xr[1]), "=&v"(xr[2]), "=&v"(xr[3]) : "v"(l * 16), "s"(sx) : "memory");
  }
  asm volatile("" : "+v"(l));
  const unsigned g = l >> 4, c = l & 15, q4 = (l >> 2) & 3, p4 = l & 3;
  const unsigned seg = 4 * w1 + g, stt = 64 * sb + 32 * w0 + c;
  {
    asm volatile("global_load_dword %0, %7, %8 sc1\n"
                 "global_load_ushort %1, %9, %10\n global_load_ushort %2, %9, %10 offset:32\n"
                 "global_load_ushort %3, %9, %11\n global_load_ushort %4, %9, %11 offset:32\n"
                 "global_load_ushort %5, %9, %12\n global_load_ushort %6, %9, %12 offset:32"
                 : "=&v"(ep), "=&v"(pl[0]), "=&v"(pl[1]), "=&v"(pl[2]), "=&v"(pl[3]), "=&v"(pl[4]), "=&v"(pl[5])
                 : "v"(0u), "s"(ws), "v"(stt * 2), "s"(lre), "s"(lim), "s"(logdt) : "memory");
    asm volatile("global_load_dwordx2 %0, %4, %5\n global_load_dwordx2 %1, %4, %5 offset:32\n"
                 "global_load_dwordx2 %2, %4, %5 offset:64\n global_load_dwordx2 %3, %4, %5 offset:96"
                 : "=&v"(dk[0]), "=&v"(dk[1]), "=&v"(dk[2]), "=&v"(dk[3]) : "v"((nb * 128 + 64 * w0 + 4 * g) * 2), "s"(dsk) : "memory");
  }
  u32x8 sM;
  u32x16 vP, vQ;
  u32x4 sB1 = mk_srd(w0 ? wu : wg, pn * 224, SZ_W);
  sM[1] = uni(65536 + w0 * 14352 + w1 * 7168);
  {
    unsigned pi = (l * 37) >> 9, cc = l - 14 * pi;
#pragma unroll
    for (int e = 0; e < 7; ++e) {
      vP[7 + e] = (32 * w1 + swap23(pi)) * 7168 + 16 * cc;
      const bool wr = cc >= 6;
      cc = wr ? cc - 6 : cc + 8; pi += wr ? 5 : 4;
    }
  }
  const unsigned tdt = (l * 43) >> 9, t12 = l - 12 * tdt, tr3 = (t12 * 11) >> 5, tli = t12 - 3 * tr3;
  const unsigned tw = (tdt * 64 + (2 * pm + w1) * 4 + tr3) * 7168 + (tli == 2 ? 220 : tli * 112);
  {
    asm volatile(ASM_BLOCK0 : "+{s[12:15]}"(sB1)
                 : "{v229}"(l < 60 ? tw : OOB), "{s89}"(sM[1]), "{v231}"(vP[7]), "{v232}"(vP[8]), "{v233}"(vP[9]), "{v234}"(vP[10]), "{v235}"(vP[11]), "{v236}"(vP[12]), "{v237}"(vP[13])
                 : "s98", "scc", "memory");
  }
  asm volatile("s_waitcnt vmcnt(19)" : "+v"(xr[0]), "+v"(xr[1]), "+v"(xr[2]), "+v"(xr[3]) :: "memory");
  ln_rows(xr);
  {
    const u32x4 so = mk_srd(xn, xrow, SZ_X);
#define XNCP " sc1"
    asm volatile("buffer_store_dwordx4 %0, %4, %5, 0 offen" XNCP "\n buffer_store_dwordx4 %1, %4, %5, 0 offen offset:1024" XNCP "\n"
                 "buffer_store_dwordx4 %2, %4, %5, 0 offen offset:2048" XNCP "\n buffer_store_dwordx4 %3, %4, %5, 0 offen offset:3072" XNCP "\n s_nop 1"
                 :: "v"(xr[0]), "v"(xr[1]), "v"(xr[2]), "v"(xr[3]), "v"(l * 16), "s"(so) : "memory");
  }
  asm volatile("s_waitcnt vmcnt(12)" : "+v"(ep), "+v"(pl[0]), "+v"(pl[1]), "+v"(pl[2]), "+v"(pl[3]), "+v"(pl[4]), "+v"(pl[5]),
               "+v"(dk[0]), "+v"(dk[1]), "+v"(dk[2]), "+v"(dk[3]) :: "memory");
  const unsigned tag = __builtin_amdgcn_readfirstlane(ep) + 1;
  unsigned st[32];
  u32x32 st1, st2;
  const unsigned swz = ((l & 7) ^ (l >> 4)) * 16;
  sM[0] = uni(w1 * 16384 + w0 * 1024);
  vP[6] = (128 * w1 + 8 * w0 + (l >> 3)) * 2048 + (swz ^ (64 * w0));
  const unsigned rowrd = c * 128 + ((g ^ (c >> 1)) * 16);
  vP[0] = w1 * 16384 + rowrd; vP[1] = vP[0] ^ 64;
  const unsigned pirow = 16 * (g >> 1) + 4 * (g & 1) + q4;
  vP[2] = 65536 + pirow * 224 + w0 * 128 + 8 * p4;
  vP[3] = 65536 + pirow * 224 + 96 + 16 * w0 + 8 * (p4 & 1) + (p4 >> 1) * 14352;
  vP[5] = l < 12 ? tw : OOB;
  vP[14] = 0; vP[15] = 0;
  vQ[0] = w1 * 8192 + rowrd; vQ[1] = vQ[0] ^ 64; vQ[2] = vQ[0] + 65536; vQ[3] = vQ[1] + 65536;
  vQ[4] = 16384 + w0 * 8192 + rowrd; vQ[5] = vQ[4] ^ 64; vQ[6] = vQ[4] + 65536; vQ[7] = vQ[5] + 65536;
  vQ[8] = (64 * w1 + 32 * w0 + 16 * (l >> 5) + ((l >> 3) & 3)) * 2048 + (swz ^ (64 * w0));
  vQ[9] = (8 * w1 + (l >> 3)) * 2048 + (swz ^ (64 * w1));
  {
    const unsigned wv = mb * 4 + w;
    const bool t0 = l < 6, t1 = l < 9, t2 = l < 13;
    const size_t bw = (size_t)wd, bb = (size_t)(w0 ? bim : bre), bc = (l & 1) ? (size_t)cre : (size_t)cim;
    const unsigned o0 = (h * 1792 + wv * 28 + (l >> 1) * 8) * 2048 + nb * 256 + (l & 1) * 128;
    const unsigned o1 = sb * 131072 + ((mb * 2 + w1) * 32 + (l - 6) * 10) * 128;
    const unsigned o2 = (nb * 128 + wv * 2 + (l >= 11 ? 1 : 0)) * 2048 + h * 1024;
    const size_t ta = (t0 ? bw : t1 ? bb : t2 ? bc : bw) + (t0 ? o0 : t1 ? o1 : t2 ? o2 : 0u);
    const unsigned ts = t0 ? 2048u : t1 ? 128u : t2 ? 88u : 0u;
    vP[4] = ts; vQ[10] = (unsigned)ta; vQ[11] = (unsigned)(ta >> 32);
  }
  vQ[12] = 0; vQ[13] = 0; vQ[14] = tag; vQ[15] = ~tag;
  const unsigned pq = pn & 15;
  const unsigned phi = q4 | ((g & 1) << 2), trrow = (8 * g + q4) * 256 + 8 * p4;
  st[0] = w1 * 8192 + l * 16;
#pragma unroll
  for (int t = 0; t < 4; ++t) { st[1 + t] = 16384 + trrow + (((4 * w0 + t) ^ phi) * 32);
    st[7 + t] = (g >> 1) * 4096 + (2 * w1 + (p4 >> 1)) * 1024 + (8 * (g & 1) + q4) * 64 + ((t ^ ((p4 >> 1) | ((g & 1) << 1))) * 16) + 8 * (p4 & 1); }
  st[5] = 2 * (w ^ h) * HROW + l * 16;
  const unsigned oct = c ^ ((g | (w0 << 2)) << 1);
  st[6] = g * 2048 + oct * 16;
  st[11] = w * 1024 + (l >> 2) * 64 + (((l & 3) ^ (w0 | ((l >> 5) << 1)) ^ (2 * h)) * 16);
  st[12] = (64 * w1 + 8 * w0 + (l >> 3)) * 2048 + (swz ^ (64 * w0));
  st[13] = (2 * pq + w0) * 1024 + l * 16;
  st[14] = (g & 1) * HROW + (w0 ? (48 + (pq >> 1)) * 1024 + (2 * (pq & 1) + (g >> 1)) * 256 : (32 + pq) * 1024 + (g >> 1) * 256) + c * 16;
  st[15] = ((g & 1) * 2 + (g >> 1)) * HROW + (32 + pq) * 1024 + (2 + w0) * 256 + c * 16;
  asm volatile("" : "+v"(vP), "+v"(vQ), "+v"(st[0]), "+v"(st[1]), "+v"(st[2]), "+v"(st[3]), "+v"(st[4]), "+v"(st[5]), "+v"(st[6]), "+v"(st[7]), "+v"(st[8]),
               "+v"(st[9]), "+v"(st[10]), "+v"(st[11]), "+v"(st[12]), "+v"(st[13]), "+v"(st[14]), "+v"(st[15]));
  st[16] = w0 * 8192 + w1 * 2048 + c * 64 + g * 16;
  st[17] = MI_AGG + (sb * 2 + w0) * 8192 + seg * 512 + c * 16;
  st[18] = seg == 0 ? MI_AGG + (sb * 2 + w0) * 8192 + mb * 256 + c * 16 : OOB;
  st[19] = l == 0 ? 0u : OOB;
  st[20] = (l & 31) * 8;
  { const unsigned k = ((idx * 4 + w) * 4 + (l & 3)) * 128; st[21] = k < CODE_BYTES ? k : 0; }
  st[22] = 152320 + (32 * w0 + c) * 80 + seg * 8;
  st[23] = 152320 + (32 * w0 + c) * 80;
  st[24] = seg;
  st[25] = (64 * w1 + c) * 4096 + (64 * w0 + 4 * g) * 4;
  st[26] = MI_XF + tid * 8;
  st[27] = l * 16;
  st[28] = (2 * seg < mb ? 1u : 0u) | (2 * seg + 1 < mb ? 2u : 0u);
  st[29] = w == 0 ? 786432 + bid * 512 + l * 4 : OOB;
  st[30] = (32 * h + 4 * w + g) * 2048 + (c ^ (4 * w + g)) * 16;
  st[31] = (32 * w1 + c) * 256 + (((8 * w0 + (g >> 1)) ^ c) * 16) + 8 * (g & 1);
#pragma unroll
  for (int k = 0; k < 32; ++k) st1[k] = st[k];
  asm volatile("" : "+a"(st1));
  float tb[24];
  auto ssm_tab = [&](const int n) {
    const float lr = bf2f(pl[n]), li = bf2f(pl[2 + n]);
    const float dt = rbf2(exp_f(bf2f(pl[4 + n])));
    const float er = exp_f(dt * lr);
    const cf u = cis(dt * li), lam = {er * u.r, er * u.i};
    const float nr = lam.r - 1.f, den = fmaf(lr, lr, li * li);
    float rd = __builtin_amdgcn_rcpf(den);
    rd = 2.f * fmaf(fmaf(-den, rd, 1.0f), rd, rd);
    cf q = lam;
#pragma unroll
    for (int k = 0; k < 4; ++k) q = csq(q);
    const cf l16 = q;
    cf p16 = csel(seg & 1, q, cf{1.f, 0.f});
    q = csq(q); p16 = csel(seg & 2, cmul(p16, q), p16);
    q = csq(q); p16 = csel(seg & 4, cmul(p16, q), p16);
    q = csq(q);
    const cf l128 = q;
    const int e1 = (int)mb - 2 - 2 * (int)seg;
    cf pw = csel(e1 & 1, q, cf{1.f, 0.f});
    q = csq(q); pw = csel(e1 & 2, cmul(pw, q), pw);
    q = csq(q); pw = csel(e1 & 4, cmul(pw, q), pw);
    q = csq(q); pw = csel(e1 & 8, cmul(pw, q), pw);
    const cf p1 = csel(e1 >= 0, pw, cf{0.f, 0.f});
    const cf p0 = csel(e1 >= 0, cmul(pw, l128), cf{e1 == -1 ? 1.f : 0.f, 0.f});
    const float t[12] = {lam.r, lam.i, l16.r, l16.i, (nr * lr + lam.i * li) * rd, (lam.i * lr - nr * li) * rd,
                         p1.r, p1.i, p0.r, p0.i, p16.r, p16.i};
#pragma unroll
    for (int k = 0; k < 12; ++k) tb[12 * n + k] = t[k];
  };
  ssm_tab(0);
  asm volatile("" : "+v"(tb[0]), "+v"(tb[1]), "+v"(tb[2]), "+v"(tb[3]), "+v"(tb[4]), "+v"(tb[5]), "+v"(tb[6]), "+v"(tb[7]), "+v"(tb[8]), "+v"(tb[9]),
               "+v"(tb[10]), "+v"(tb[11]));
  const u32x4 sMI = mk_srd(ws, 0, (unsigned)WS_XN);
  {
    const u32x2 tg = {tag, ~tag};
    asm volatile("s_waitcnt vmcnt(0)\n s_barrier\n buffer_store_dwordx2 %0, %1, %2, 0 offen sc1"
                 :: "v"(tg), "v"(tid == 0 ? MI_XF + (pm * 32 + pn) * 8 : OOB), "s"(sMI) : "memory");
  }
  u32x4 sA1 = mk_srd(xn, pm * 524288, SZ_X);
  asm volatile(ASM_BLOCKX : "+{s[8:11]}"(sA1), "+{s[12:15]}"(sB1)
               : "{s89}"(sM[1]), "{v231}"(vP[7]), "{v232}"(vP[8]), "{v233}"(vP[9]), "{v234}"(vP[10]), "{v235}"(vP[11]), "{v236}"(vP[12]), "{v237}"(vP[13]),
                 "{s[52:55]}"(sMI), "{s4}"(uni(MI_XF + pm * 256)), "{s88}"(sM[0]), "{v230}"(vP[6]), "{v2}"((l & 31) * 8), "{v[4:5]}"(u32x2{tag, ~tag})
               : "v6", "v7", "s72", "s73", "s74", "s75", "s76", "s77", "s78", "s87", "s98", "vcc", "scc", "memory");
  asm volatile("" : "+v"(pl[1]), "+v"(pl[3]), "+v"(pl[5]));
  ssm_tab(1);
#pragma unroll
  for (int k = 0; k < 24; ++k) st2[k] = __float_as_uint(tb[k]);
#pragma unroll
  for (int k = 0; k < 4; ++k) { st2[24 + 2 * k] = dk[k][0]; st2[25 + 2 * k] = dk[k][1]; }
  sM[2] = uni((8 * w1 + w0) * 1024);
  sM[3] = uni(16384 + (2 * w0 + w1) * 1024);
  sM[4] = uni(w * 4096);
  sM[5] = uni(16384 + (2 * w0 + w1) * 1024);
  sM[6] = uni((16 * w1 + 4 * w0) * 2048);
  sM[7] = uni((8 * w0 + 4 * w1) * 2048);
  u32x8 s0;
  const unsigned tile = mb * 8 + nb;
  s0[0] = uni(MI_HF + ((2 * pm + w1) * 2 + (pn >> 4)) * 256 + (pq * 2 + w0) * 8);
  s0[1] = uni(MI_HF + (mb * 2 + h) * 256);
  s0[2] = uni(MI_SF + (mb * 2 + (sb >> 3)) * 256 + ((sb & 7) * 4 + w) * 8);
  s0[3] = uni((2 * h) * 65536);
  s0[4] = uni(MI_XF + pm * 256);
  s0[5] = uni(MI_EX + ((tile * 2 + h) * 4 + w) * 8192);
  s0[6] = uni(nb * 256);
  s0[7] = uni(MI_FLAG + ((tile * 2 + h) * 4 + w) * 64);
  u32x2 sT = {tag, uni(MI_XF + (mb >> 1) * 256)};
  asm volatile(ASM_BLOCK1
               : "+{s[0:7]}"(s0), "+{s[8:11]}"(sA1), "+{s[12:15]}"(sB1), "+{s[88:95]}"(sM), "+{v[224:239]}"(vP), "+{v[240:255]}"(vQ)
               : "{a[192:223]}"(st1), "{a[224:255]}"(st2), "{s[52:55]}"(sMI), "{s[70:71]}"(sT), "{s[62:63]}"(__builtin_amdgcn_kernarg_segment_ptr()),
                 "{s18}"(uni(mb * 262144)), "{s22}"(uni(sb * 131072 + P2_K0)), "{s26}"(uni((16 * pm + 8 * w1) * HROW + (pn >> 4) * 57344)),
                 "{s30}"(uni(8 * mb * HROW + h * 57344)), "{s34}"(uni(h * 1792 * 2048 + nb * 256)), "{s38}"(uni((mb * 16 + sb) * 32768)),
                 "{s42}"(uni((mb * 16 + 8 * h) * 32768)), "{s46}"(uni(nb * 262144 + h * 1024)), "{s50}"(uni(nb * 262144 + h * 1024)),
                 "{s58}"(uni(mb * 262144 + nb * 256)), "{s66}"(uni(mb * 524288 + nb * 512))
               : CLOB_V, CLOB_A, "s16", "s17", "s19", "s20", "s21", "s23", "s24", "s25", "s27", "s28", "s29", "s31", "s33", "s35", "s36", "s37", "s39",
                 "s40", "s41", "s43", "s44", "s45", "s47", "s48", "s49", "s51", "s56", "s57", "s59", "s60", "s61", "s64", "s65", "s67", "s68", "s69",
                 "s72", "s73", "s74", "s75", "s76", "s77", "s78", "s79", "s80", "s81", "s82", "s83", "s84", "s85", "s86", "s87", "s96", "s97", "s98", "s99",
                 "vcc", "scc", "memory");
}

extern "C" void kernel_launch(void* const* d_in, const int* in_sizes, int n_in, void* d_out, int out_size, void* d_ws,
                              size_t ws_size, hipStream_t stream) {
  (void)hipFuncSetAttribute((const void*)fused_kernel, hipFuncAttributeMaxDynamicSharedMemorySize, LDS_BYTES);
  Args ar;
  ar.x = (const u16*)d_in[0]; ar.lre = (const u16*)d_in[1]; ar.lim = (const u16*)d_in[2]; ar.logdt = (const u16*)d_in[7];
  ar.wg = (const u16*)d_in[10]; ar.wu = (const u16*)d_in[11]; ar.dsk = (const u16*)d_in[8]; ar.ws = (char*)d_ws;
  ar.bre = (const u16*)d_in[3]; ar.bim = (const u16*)d_in[4]; ar.cre = (const u16*)d_in[5]; ar.cim = (const u16*)d_in[6];
  ar.wd = (const u16*)d_in[12]; ar.out = (float*)d_out;
  fused_kernel<<<256, 256, LDS_BYTES, stream>>>(ar);
}
```
